```python
import torch

def _softmax(x, axis=-1):
    return torch.softmax(x, dim=axis)

def _gelu(x):
    return torch.nn.functional.gelu(x, approximate='tanh')

def _silu(x):
    return torch.nn.functional.silu(x)

def _sigmoid(x):
    return torch.sigmoid(x)

def _layernorm(x, axis=-1, eps=1e-05):
    m = x.mean(dim=axis, keepdim=True)
    v = x.var(dim=axis, keepdim=True, unbiased=False)
    return (x - m) / torch.sqrt(v + eps)

def _scatter_add(tgt, idx, src, axis=0):
    return tgt.clone().index_add_(axis, idx, src)

def _segment_sum(src, seg, n):
    out = torch.zeros((n,) + src.shape[1:], dtype=src.dtype)
    return out.index_add_(0, seg, src)

def _pad(x, pw):
    flat = tuple((v for pair in reversed(pw) for v in pair))
    return torch.nn.functional.pad(x, flat)
kernel_name = 'parallel_sum__memory_aug_recurrent_mem_tr__dense_activation_gelu__prompt'

def setup_inputs():

    _w = lambda a: (lambda t: t.to(torch.bfloat16) if t.is_floating_point() else t)(torch.as_tensor(a))

    def _A_setup():
        B, S, Dm = (1, 2048, 448)
        H, Dh = (16, 256)
        M, seg = (8, 32)
        seg = min(seg, S)
        S = S // seg * seg
        return {'x': _w((lambda *s: torch.randn(*s, dtype=torch.bfloat16) * 0.05)(B, S, Dm)), 'mem0': _w((lambda *s: torch.randn(*s, dtype=torch.bfloat16) * 0.2)(B, M, Dm)), 'Wq': _w((lambda *s: torch.randn(*s, dtype=torch.bfloat16) * 1.6)(Dm, H, Dh) * Dm ** (-0.5)), 'Wk': _w((lambda *s: torch.randn(*s, dtype=torch.bfloat16) * 1.6)(Dm, H, Dh) * Dm ** (-0.5)), 'Wv': _w((lambda *s: torch.randn(*s, dtype=torch.bfloat16) * 0.2)(Dm, H, Dh) * Dm ** (-0.5)), 'Wo': _w((lambda *s: torch.randn(*s, dtype=torch.bfloat16) * 0.2)(H, Dh, Dm) * (H * Dh) ** (-0.5)), 'seg_len': seg}
    _A_in = _A_setup()

    def _B_setup():
        B, S, D, F = (1, 2048, 448, 1792)
        return {'x': _w((lambda *s: torch.randn(*s, dtype=torch.bfloat16) * 0.05)(B, S, D)), 'w1': _w((lambda *s: torch.randn(*s, dtype=torch.bfloat16) * 0.4)(D, F) * D ** (-0.5)), 'b1': _w((lambda *s: torch.randn(*s, dtype=torch.bfloat16) * 12.8)(F) * 0.01), 'w2': _w((lambda *s: torch.randn(*s, dtype=torch.bfloat16) * 0.1)(F, D) * F ** (-0.5)), 'b2': _w((lambda *s: torch.randn(*s, dtype=torch.bfloat16) * 1.6)(D) * 0.01)}
    _B_in = _B_setup()
    return {'A_x': _A_in['x'], 'A_mem0': _A_in['mem0'], 'A_Wq': _A_in['Wq'], 'A_Wk': _A_in['Wk'], 'A_Wv': _A_in['Wv'], 'A_Wo': _A_in['Wo'], 'A_seg_len': _A_in['seg_len'], 'B_x': _B_in['x'], 'B_w1': _B_in['w1'], 'B_b1': _B_in['b1'], 'B_w2': _B_in['w2'], 'B_b2': _B_in['b2']}

def reference(A_x, A_mem0, A_Wq, A_Wk, A_Wv, A_Wo, A_seg_len, B_x, B_w1, B_b1, B_w2, B_b2):

    def _A_ref(x, mem0, Wq, Wk, Wv, Wo, seg_len):
        B, S, Dm = x.shape
        H, Dh = (Wq.shape[1], Wq.shape[2])
        M = mem0.shape[1]
        scale = Dh ** (-0.5)
        n_seg = S // seg_len
        L = M + seg_len
        idx = torch.arange(L)
        mask = (idx[:, None] < M) | (idx[:, None] >= idx[None, :])
        mem = mem0
        outs = []
        for s in range(n_seg):
            xs = x[:, s * seg_len:(s + 1) * seg_len, :].clone()
            h = torch.concatenate([mem, xs], axis=1)
            q = torch.einsum('btd,dhe->bhte', h, Wq)
            k = torch.einsum('btd,dhe->bhte', h, Wk)
            v = torch.einsum('btd,dhe->bhte', h, Wv)
            scores = torch.einsum('bhqe,bhke->bhqk', q, k) * scale
            scores = torch.where(mask[None, None], scores, -1000000000.0)
            p = _softmax(scores, axis=-1)
            o = torch.einsum('bhqk,bhke->bhqe', p, v)
            o = torch.einsum('bhte,hed->btd', o, Wo)
            mem = o[:, :M, :].clone()
            outs.append(o[:, M:, :])
        return torch.concatenate(outs, axis=1)

    def _B_ref(x, w1, b1, w2, b2):
        h = torch.einsum('bsd,df->bsf', x, w1) + b1
        h = _gelu(h)
        return torch.einsum('bsf,fd->bsd', h, w2) + b2
    x = A_x
    xn = _layernorm(x, axis=-1)
    return x + _A_ref(xn, A_mem0, A_Wq, A_Wk, A_Wv, A_Wo, A_seg_len) + _B_ref(xn, B_w1, B_b1, B_w2, B_b2)
```

```cpp
#include <hip/hip_runtime.h>
#include <hip/hip_bf16.h>


typedef __attribute__((ext_vector_type(8))) short bf16x8;
typedef __attribute__((ext_vector_type(4))) short bf16x4;
typedef __attribute__((ext_vector_type(4))) float f32x4;
typedef __attribute__((ext_vector_type(4))) unsigned u32x4;
typedef __attribute__((ext_vector_type(2))) unsigned u32x2;
typedef __attribute__((ext_vector_type(2))) __bf16 bf2_t;
#define DI __device__ __forceinline__

constexpr int DM = 448, KK = 14, NT16 = 28, NH = 16, NSEG = 64, NRT = 128, FFD = 1792, KF = 56;
constexpr size_t KB = 1024;
constexpr size_t OFF_XNF = 0;
constexpr size_t OFF_WF = OFF_XNF + (size_t)NRT * KK * KB;
constexpr size_t OFF_ANF = OFF_WF + (size_t)1008 * KK * KB;
constexpr size_t OFF_W2F = OFF_ANF + (size_t)448 * KK * KB;
constexpr size_t OFF_GTF = OFF_W2F + (size_t)28 * KF * KB;
constexpr size_t OFF_UF = OFF_GTF + (size_t)NH * NRT * KK * KB;
constexpr size_t OFF_HF = OFF_UF + (size_t)NSEG * NT16 * NH * KB;
constexpr size_t OFF_MEMF = OFF_HF + (size_t)NRT * KF * KB;
constexpr size_t OFF_SMX = OFF_MEMF + (size_t)NSEG * 7168;
constexpr size_t OFF_SMM = OFF_SMX + (size_t)NSEG * 8192;
constexpr size_t OFF_T2F = OFF_SMM + (size_t)NSEG * 8192;
constexpr size_t OFF_YF = OFF_T2F + (size_t)NH * NSEG * 7168;
constexpr size_t OFF_PXF = OFF_YF + (size_t)NSEG * NT16 * 8 * 512;
constexpr size_t OFF_PMF = OFF_PXF + (size_t)NSEG * 2 * NH * KB;
constexpr size_t OFF_TMO = OFF_PMF + (size_t)NSEG * 2 * 8 * KB;
constexpr size_t WS_END = OFF_TMO + 4096;

constexpr unsigned SENT = 0xFFFFFFFFu;
constexpr unsigned kSpinCap = 1u << 16;

DI unsigned pack2(float a, float b) { bf2_t v = {(__bf16)a, (__bf16)b}; return __builtin_bit_cast(unsigned, v); }
DI float bf_lo(unsigned u) { return __uint_as_float(u << 16); }
DI float bf_hi(unsigned u) { return __uint_as_float(u & 0xFFFF0000u); }
DI float bf2f(unsigned short v) { return __uint_as_float((unsigned)v << 16); }
DI f32x4 mfma32(bf16x8 a, bf16x8 b, f32x4 c) { return __builtin_amdgcn_mfma_f32_16x16x32_bf16(a, b, c, 0, 0, 0); }
DI f32x4 mfma16(bf16x4 a, bf16x4 b, f32x4 c) { return __builtin_amdgcn_mfma_f32_16x16x16bf16_1k(a, b, c, 0, 0, 0); }
DI bf16x8 ldfrag(const char* p) { return *(const bf16x8*)p; }
DI bf16x8 pack8(f32x4 a, f32x4 b) {
    u32x4 r = {pack2(a[0], a[1]), pack2(a[2], a[3]), pack2(b[0], b[1]), pack2(b[2], b[3])};
    return __builtin_bit_cast(bf16x8, r);
}
DI bf16x4 pack4(f32x4 a) { u32x2 r = {pack2(a[0], a[1]), pack2(a[2], a[3])}; return __builtin_bit_cast(bf16x4, r); }

DI bool spin_fail(unsigned& spins, unsigned* tmo) {
    if (++spins > kSpinCap) { __hip_atomic_store(tmo, 1u, __ATOMIC_RELAXED, __HIP_MEMORY_SCOPE_AGENT); return true; }
    if ((spins & 63u) == 0u) return __builtin_amdgcn_readfirstlane(__hip_atomic_load(tmo, __ATOMIC_RELAXED, __HIP_MEMORY_SCOPE_AGENT)) != 0u;
    return false;
}

__global__ __launch_bounds__(256) void k_ln(const unsigned short* __restrict__ x, const unsigned short* __restrict__ mem0, char* __restrict__ ws) {
    const int wave = threadIdx.x >> 6, lane = threadIdx.x & 63;
    const int gw = blockIdx.x * 4 + wave;
    {
        const int row = gw;
        const unsigned short* xr = x + (size_t)row * DM;
        float v[2][4];
        float sum = 0.f;
#pragma unroll
        for (int it = 0; it < 2; ++it) {
            int grp = lane + it * 64;
            if (grp < 112) {
                u32x2 u = *(const u32x2*)(xr + grp * 4);
                v[it][0] = bf_lo(u.x); v[it][1] = bf_hi(u.x); v[it][2] = bf_lo(u.y); v[it][3] = bf_hi(u.y);
            } else { v[it][0] = v[it][1] = v[it][2] = v[it][3] = 0.f; }
            sum += v[it][0] + v[it][1] + v[it][2] + v[it][3];
        }
#pragma unroll
        for (int o = 32; o >= 1; o >>= 1) sum += __shfl_xor(sum, o);
        const float mean = sum * (1.f / DM);
        float sq = 0.f;
#pragma unroll
        for (int it = 0; it < 2; ++it) {
            int grp = lane + it * 64;
            if (grp < 112) {
#pragma unroll
                for (int j = 0; j < 4; ++j) { float d = v[it][j] - mean; sq += d * d; }
            }
        }
#pragma unroll
        for (int o = 32; o >= 1; o >>= 1) sq += __shfl_xor(sq, o);
        const float rstd = 1.f / sqrtf(sq * (1.f / DM) + 1e-5f);
        const int rt = row >> 4, i = row & 15;
#pragma unroll
        for (int it = 0; it < 2; ++it) {
            int grp = lane + it * 64;
            if (grp < 112) {
                int d = grp * 4, kk = d >> 5, dd = d & 31, half = dd >> 4, g = (dd & 15) >> 2;
                u32x2 o = {pack2((v[it][0] - mean) * rstd, (v[it][1] - mean) * rstd), pack2((v[it][2] - mean) * rstd, (v[it][3] - mean) * rstd)};
                *(u32x2*)(ws + OFF_XNF + ((size_t)(rt * KK + kk) * 64 + (i + 16 * g)) * 16 + half * 8) = o;
            }
        }
    }
    {
        const size_t nb = (size_t)(NSEG - 1) * 7168 + 2 * (size_t)NSEG * 8192;
        const size_t n16 = nb / 16;
        u32x4 sv = {SENT, SENT, SENT, SENT};
        for (size_t idx = (size_t)blockIdx.x * 256 + threadIdx.x; idx < n16; idx += (size_t)gridDim.x * 256)
            *(u32x4*)(ws + OFF_MEMF + 7168 + idx * 16) = sv;
    }
    if (blockIdx.x == 0) {
        for (int e = threadIdx.x; e < 8 * 112; e += 256) {
            int r = e / 112, grp = e % 112;
            int d = grp * 4, kk = d >> 5, dd = d & 31, half = dd >> 4, g = (dd & 15) >> 2;
            u32x2 u = *(const u32x2*)(mem0 + r * DM + d);
            *(u32x2*)(ws + OFF_MEMF + kk * 512 + g * 128 + r * 16 + half * 8) = u;
        }
        if (threadIdx.x == 0) *(unsigned*)(ws + OFF_TMO) = 0;
    }
}

__global__ __launch_bounds__(256) void k_ab(const unsigned short* __restrict__ Wq, const unsigned short* __restrict__ Wk,
                                            const unsigned short* __restrict__ Wv, const unsigned short* __restrict__ Wo, char* __restrict__ ws) {
    const int wave = threadIdx.x >> 6, lane = threadIdx.x & 63;
    const int u = blockIdx.x * 4 + wave;
    const int type = u / 448, h = (u % 448) / 28, tau = u % 28;
    const int i = lane & 15, g = lane >> 4;
    bf16x8 fix[8];
    const unsigned short* loopW;
    if (type == 0) {
        const unsigned short* p = Wq + (size_t)(16 * tau + i) * 4096 + h * 256 + 8 * g;
#pragma unroll
        for (int ke = 0; ke < 8; ++ke) fix[ke] = *(const bf16x8*)(p + ke * 32);
        loopW = Wk;
    } else if (type == 1) {
        const unsigned short* p = Wk + (size_t)(16 * tau + i) * 4096 + h * 256 + 8 * g;
#pragma unroll
        for (int ke = 0; ke < 8; ++ke) fix[ke] = *(const bf16x8*)(p + ke * 32);
        loopW = Wq;
    } else {
#pragma unroll
        for (int ke = 0; ke < 8; ++ke)
#pragma unroll
            for (int j = 0; j < 8; ++j)
                fix[ke][j] = (short)Wo[((size_t)h * 256 + ke * 32 + 8 * g + j) * DM + 16 * tau + i];
        loopW = Wv;
    }
    char* dst = (type == 0) ? ws + OFF_WF + (size_t)(h * 28 + tau) * KK * KB
              : (type == 1) ? ws + OFF_ANF + (size_t)(h * 28 + tau) * KK * KB
                            : ws + OFF_WF + (size_t)(448 + h * 28 + tau) * KK * KB;
    for (int kk = 0; kk < KK; ++kk) {
        f32x4 acc[2] = {{0.f, 0.f, 0.f, 0.f}, {0.f, 0.f, 0.f, 0.f}};
#pragma unroll
        for (int tt = 0; tt < 2; ++tt) {
            const unsigned short* p = loopW + (size_t)(32 * kk + 16 * tt + i) * 4096 + h * 256 + 8 * g;
#pragma unroll
            for (int ke = 0; ke < 8; ++ke) acc[tt] = mfma32(*(const bf16x8*)(p + ke * 32), fix[ke], acc[tt]);
        }
        *(bf16x8*)(dst + (size_t)kk * KB + lane * 16) = pack8(acc[0], acc[1]);
    }
}

__global__ __launch_bounds__(256) void k_w12(const unsigned short* __restrict__ w1, const unsigned short* __restrict__ w2, char* __restrict__ ws) {
    const int wave = threadIdx.x >> 6, lane = threadIdx.x & 63;
    const int f = blockIdx.x * 4 + wave;
    const int i = lane & 15, g = lane >> 4;
    bf16x8 v;
    if (f < 112 * KK) {
        int tf = f / KK, kk = f % KK;
#pragma unroll
        for (int j = 0; j < 8; ++j) v[j] = (short)w1[(size_t)(32 * kk + 16 * (j >> 2) + 4 * g + (j & 3)) * FFD + 16 * tf + i];
        *(bf16x8*)(ws + OFF_WF + ((size_t)(896 + tf) * KK + kk) * KB + lane * 16) = v;
    } else {
        int f2 = f - 112 * KK;
        int t = f2 / KF, kf = f2 % KF;
#pragma unroll
        for (int j = 0; j < 8; ++j) v[j] = (short)w2[(size_t)(32 * kf + 16 * (j >> 2) + 4 * g + (j & 3)) * DM + 16 * t + i];
        *(bf16x8*)(ws + OFF_W2F + ((size_t)t * KF + kf) * KB + lane * 16) = v;
    }
}

DI float gelu_tanh(float z) {
    float u = 0.7978845608028654f * (z + 0.044715f * z * z * z);
    float e = __expf(2.f * u);
    float t = 1.f - 2.f / (1.f + e);
    return 0.5f * z * (1.f + t);
}

__global__ __launch_bounds__(256, 1) void k_work(const unsigned short* __restrict__ b1, char* __restrict__ ws) {
    const int wave = threadIdx.x >> 6, lane = threadIdx.x & 63;
    const int unit = blockIdx.x;
    const int tile0 = unit * 4;
    const int i = lane & 15, g = lane >> 4;
    bf16x8 w[4][KK];
    {
        const char* wp = ws + OFF_WF + (size_t)tile0 * KK * KB + lane * 16;
#pragma unroll
        for (int t = 0; t < 4; ++t)
#pragma unroll
            for (int kk = 0; kk < KK; ++kk) w[t][kk] = ldfrag(wp + (size_t)(t * KK + kk) * KB);
    }
    const int type = tile0 < 448 ? 0 : (tile0 < 896 ? 1 : 2);
    for (int s = wave; s < NSEG; s += 4) {
        f32x4 acc[2][4];
#pragma unroll
        for (int q = 0; q < 2; ++q)
#pragma unroll
            for (int t = 0; t < 4; ++t) acc[q][t] = (f32x4){0.f, 0.f, 0.f, 0.f};
        const char* xp = ws + OFF_XNF + (size_t)(2 * s) * KK * KB + lane * 16;
        if (type == 1) {
#pragma unroll
            for (int kk = 0; kk < KK; ++kk)
#pragma unroll
                for (int q = 0; q < 2; ++q) {
                    bf16x8 xf = ldfrag(xp + (size_t)(q * KK + kk) * KB);
#pragma unroll
                    for (int t = 0; t < 4; ++t) acc[q][t] = mfma32(xf, w[t][kk], acc[q][t]);
                }
            const int h = (tile0 - 448) / 28, t0 = (tile0 - 448) % 28;
#pragma unroll
            for (int t = 0; t < 4; ++t)
                *(bf16x8*)(ws + OFF_UF + (((size_t)s * NT16 + t0 + t) * NH + h) * KB + lane * 16) = pack8(acc[0][t], acc[1][t]);
        } else {
#pragma unroll
            for (int kk = 0; kk < KK; ++kk)
#pragma unroll
                for (int q = 0; q < 2; ++q) {
                    bf16x8 xf = ldfrag(xp + (size_t)(q * KK + kk) * KB);
#pragma unroll
                    for (int t = 0; t < 4; ++t) acc[q][t] = mfma32(w[t][kk], xf, acc[q][t]);
                }
            if (type == 0) {
                const int h = tile0 / 28, t0 = tile0 % 28;
#pragma unroll
                for (int q = 0; q < 2; ++q)
#pragma unroll
                    for (int p = 0; p < 2; ++p)
                        *(bf16x8*)(ws + OFF_GTF + (((size_t)h * NRT + 2 * s + q) * KK + (t0 >> 1) + p) * KB + lane * 16) = pack8(acc[q][2 * p], acc[q][2 * p + 1]);
            } else {
                const int tf0 = tile0 - 896;
#pragma unroll
                for (int t = 0; t < 4; ++t) {
                    u32x2 bb = *(const u32x2*)(b1 + 16 * (tf0 + t) + 4 * g);
                    float bv[4] = {bf_lo(bb.x), bf_hi(bb.x), bf_lo(bb.y), bf_hi(bb.y)};
#pragma unroll
                    for (int q = 0; q < 2; ++q)
#pragma unroll
                        for (int r = 0; r < 4; ++r) acc[q][t][r] = gelu_tanh(acc[q][t][r] + bv[r]);
                }
#pragma unroll
                for (int q = 0; q < 2; ++q)
#pragma unroll
                    for (int p = 0; p < 2; ++p)
                        *(bf16x8*)(ws + OFF_HF + ((size_t)(2 * s + q) * KF + (tf0 >> 1) + p) * KB + lane * 16) = pack8(acc[q][2 * p], acc[q][2 * p + 1]);
            }
        }
    }
}

constexpr int CH_LDS_B = 0;
constexpr int CH_LDS_MEM = 112 * 1024;
constexpr int CH_LDS_RA = CH_LDS_MEM + 7168;
constexpr int CH_LDS_RB = CH_LDS_RA + 8192;
constexpr int CH_LDS_SIZE = CH_LDS_RB + 4096;

__global__ __launch_bounds__(256, 1) void k_chain(char* __restrict__ ws) {
    extern __shared__ __attribute__((aligned(16))) char lds[];
    if (blockIdx.x & 7) return;
    const int c = blockIdx.x >> 3;
    const int tid = threadIdx.x, wave = __builtin_amdgcn_readfirstlane(tid >> 6), lane = tid & 63;
    const int i = lane & 15, g = lane >> 4;
    const int h = c >> 1, half = c & 1;
    const bool hasB = c < NT16;
    const int tB = hasB ? c : 0;
    unsigned* tmo = (unsigned*)(ws + OFF_TMO);
    const __amdgpu_buffer_rsrc_t rs = __builtin_amdgcn_make_buffer_rsrc(ws + OFF_MEMF, 0, (int)(OFF_T2F - OFF_MEMF), 0x00020000);
    constexpr unsigned R_SMX = (unsigned)(OFF_SMX - OFF_MEMF), R_SMM = (unsigned)(OFF_SMM - OFF_MEMF);

    bf16x8 wa[3][KK], wsh[7], wb[2][KK];
    {
        const char* ap = ws + OFF_ANF + (size_t)(h * 28 + 14 * half) * KK * KB + lane * 16;
#pragma unroll
        for (int t = 0; t < 3; ++t)
#pragma unroll
            for (int kk = 0; kk < KK; ++kk) wa[t][kk] = ldfrag(ap + (size_t)((3 * wave + t) * KK + kk) * KB);
#pragma unroll
        for (int k = 0; k < 7; ++k) wsh[k] = ldfrag(ap + (size_t)((12 + (wave >> 1)) * KK + 7 * (wave & 1) + k) * KB);
        const char* bp = ws + OFF_WF + (size_t)(448 + tB) * KK * KB + lane * 16;
#pragma unroll
        for (int hh = 0; hh < 2; ++hh)
#pragma unroll
            for (int kk = 0; kk < KK; ++kk) wb[hh][kk] = ldfrag(bp + (size_t)((4 * wave + hh) * 28 * KK + kk) * KB);
#pragma unroll
        for (int hh = 0; hh < 2; ++hh)
#pragma unroll
            for (int kk = 0; kk < KK; ++kk)
                *(bf16x8*)(lds + CH_LDS_B + (size_t)((wave * 2 + hh) * KK + kk) * KB + lane * 16) = ldfrag(bp + (size_t)((4 * wave + 2 + hh) * 28 * KK + kk) * KB);
    }
    const int gk0 = wave < 2 ? 4 * wave : 8 + 3 * (wave - 2);
    const int gkn = wave < 2 ? 4 : 3;
    bf16x8 gf[4], uf[4];
    {
        const char* gp = ws + OFF_GTF + (((size_t)h * NRT + half) * KK + gk0) * KB + lane * 16;
#pragma unroll
        for (int k = 0; k < 4; ++k) gf[k] = ldfrag(gp + (size_t)(k < gkn ? k : 0) * KB);
        const char* up = ws + OFF_UF + ((size_t)tB * NH + 4 * wave) * KB + lane * 16;
#pragma unroll
        for (int k = 0; k < 4; ++k) uf[k] = ldfrag(up + (size_t)k * KB);
    }
    __syncthreads();

    for (int s = 0; s < NSEG - 1; ++s) {
        {
            u32x4 x0, x1;
            unsigned spins = 0;
            const unsigned o0 = (unsigned)(s * 7168 + tid * 16), o1 = o0 + 4096;
            for (;;) {
                x0 = __builtin_amdgcn_raw_buffer_load_b128(rs, o0, 0, 16);
                x1 = x0;
                if (tid < 192) x1 = __builtin_amdgcn_raw_buffer_load_b128(rs, o1, 0, 16);
                bool ok = (x0.x != SENT) & (x0.y != SENT) & (x0.z != SENT) & (x0.w != SENT) & (x1.x != SENT) & (x1.y != SENT) & (x1.z != SENT) & (x1.w != SENT);
                if (__all(ok)) break;
                if (spin_fail(spins, tmo)) break;
            }
            *(u32x4*)(lds + CH_LDS_MEM + tid * 16) = x0;
            if (tid < 192) *(u32x4*)(lds + CH_LDS_MEM + 4096 + tid * 16) = x1;
        }
        __syncthreads();
        bf16x8 mf[KK];
        const char* mp = lds + CH_LDS_MEM + g * 128 + (i & 7) * 16;
#pragma unroll
        for (int kk = 0; kk < KK; ++kk) mf[kk] = *(const bf16x8*)(mp + kk * 512);
        {
            f32x4 ta[4];
#pragma unroll
            for (int t = 0; t < 4; ++t) ta[t] = (f32x4){0.f, 0.f, 0.f, 0.f};
            f32x4 sx = {0.f, 0.f, 0.f, 0.f};
#pragma unroll
            for (int kk = 0; kk < KK; ++kk)
#pragma unroll
                for (int t = 0; t < 3; ++t) ta[t] = mfma32(wa[t][kk], mf[kk], ta[t]);
            if (wave & 1) {
#pragma unroll
                for (int k = 0; k < 7; ++k) ta[3] = mfma32(wsh[k], mf[7 + k], ta[3]);
            } else {
#pragma unroll
                for (int k = 0; k < 7; ++k) ta[3] = mfma32(wsh[k], mf[k], ta[3]);
            }
#pragma unroll
            for (int k = 0; k < 4; ++k)
                if (k < gkn) sx = mfma32(gf[k], *(const bf16x8*)(mp + (gk0 + k) * 512), sx);
            {
                const char* gp = ws + OFF_GTF + (((size_t)h * NRT + 2 * (s + 1) + half) * KK + gk0) * KB + lane * 16;
#pragma unroll
                for (int k = 0; k < 4; ++k) gf[k] = ldfrag(gp + (size_t)(k < gkn ? k : 0) * KB);
            }
            f32x4 sm = {0.f, 0.f, 0.f, 0.f};
#pragma unroll
            for (int t = 0; t < 4; ++t) {
                const int tl = (t < 3) ? 3 * wave + t : 12 + (wave >> 1);
                const int tg = 14 * half + tl;
                bf16x4 mk = *(const bf16x4*)(mp + (tg >> 1) * 512 + (tg & 1) * 8);
                sm = mfma16(mk, pack4(ta[t]), sm);
            }
            *(f32x4*)(lds + CH_LDS_RA + (wave * 2 + 0) * 1024 + lane * 16) = sx;
            *(f32x4*)(lds + CH_LDS_RA + (wave * 2 + 1) * 1024 + lane * 16) = sm;
        }
        __syncthreads();
        if (wave < 2) {
            f32x4 v = *(const f32x4*)(lds + CH_LDS_RA + (0 + wave) * 1024 + lane * 16);
#pragma unroll
            for (int w = 1; w < 4; ++w) { f32x4 o = *(const f32x4*)(lds + CH_LDS_RA + (w * 2 + wave) * 1024 + lane * 16); v += o; }
            if (wave == 0) {
                if (i < 8) {
                    u32x2 pk = {pack2(v[0], v[1]), pack2(v[2], v[3])};
                    __builtin_amdgcn_raw_buffer_store_b64(pk, rs, R_SMX + (unsigned)(s * 8192 + ((h * 8 + i) * 4 + g) * 16 + half * 8), 0, 16);
                }
            } else {
                if (i < 8 && g < 2)
                    __builtin_amdgcn_raw_buffer_store_b128(__builtin_bit_cast(u32x4, v), rs, R_SMM + (unsigned)(s * 8192 + ((h * 2 + half) * 8 + i) * 32 + g * 16), 0, 16);
            }
        }
        if (hasB) {
            f32x4 ya[4];
#pragma unroll
            for (int hh = 0; hh < 4; ++hh) ya[hh] = (f32x4){0.f, 0.f, 0.f, 0.f};
#pragma unroll
            for (int kk = 0; kk < KK; ++kk) {
                ya[0] = mfma32(mf[kk], wb[0][kk], ya[0]);
                ya[1] = mfma32(mf[kk], wb[1][kk], ya[1]);
                ya[2] = mfma32(mf[kk], *(const bf16x8*)(lds + CH_LDS_B + (size_t)((wave * 2 + 0) * KK + kk) * KB + lane * 16), ya[2]);
                ya[3] = mfma32(mf[kk], *(const bf16x8*)(lds + CH_LDS_B + (size_t)((wave * 2 + 1) * KK + kk) * KB + lane * 16), ya[3]);
            }
            bf16x8 yp0 = pack8(ya[0], ya[1]), yp1 = pack8(ya[2], ya[3]);
            u32x4 qx[4], qm[4][2];
            {
                unsigned spins = 0;
                for (;;) {
                    bool ok = true;
#pragma unroll
                    for (int hh = 0; hh < 4; ++hh) {
                        const int hd = 4 * wave + hh;
                        qx[hh] = __builtin_amdgcn_raw_buffer_load_b128(rs, R_SMX + (unsigned)(s * 8192 + ((hd * 8 + (i & 7)) * 4 + g) * 16), 0, 16);
#pragma unroll
                        for (int p = 0; p < 2; ++p)
                            qm[hh][p] = __builtin_amdgcn_raw_buffer_load_b128(rs, R_SMM + (unsigned)(s * 8192 + ((hd * 2 + p) * 8 + (i & 7)) * 32 + (g & 1) * 16), 0, 16);
                    }
#pragma unroll
                    for (int hh = 0; hh < 4; ++hh) {
                        ok &= (qx[hh].x != SENT) & (qx[hh].y != SENT) & (qx[hh].z != SENT) & (qx[hh].w != SENT);
#pragma unroll
                        for (int p = 0; p < 2; ++p) ok &= (qm[hh][p].x != SENT) & (qm[hh][p].y != SENT) & (qm[hh][p].z != SENT) & (qm[hh][p].w != SENT);
                    }
                    if (__all(ok)) break;
                    if (spin_fail(spins, tmo)) break;
                }
            }
            const float cs = 0.0625f * 1.4426950408889634f;
            f32x4 acc = {0.f, 0.f, 0.f, 0.f};
            f32x4 pm[4];
#pragma unroll
            for (int hh = 0; hh < 4; ++hh) {
                float xs[8] = {bf_lo(qx[hh].x), bf_hi(qx[hh].x), bf_lo(qx[hh].y), bf_hi(qx[hh].y), bf_lo(qx[hh].z), bf_hi(qx[hh].z), bf_lo(qx[hh].w), bf_hi(qx[hh].w)};
                f32x4 ms = __builtin_bit_cast(f32x4, qm[hh][0]) + __builtin_bit_cast(f32x4, qm[hh][1]);
                float m = fmaxf(fmaxf(fmaxf(xs[0], xs[1]), fmaxf(xs[2], xs[3])), fmaxf(fmaxf(xs[4], xs[5]), fmaxf(xs[6], xs[7])));
                m = fmaxf(m, fmaxf(fmaxf(ms[0], ms[1]), fmaxf(ms[2], ms[3])));
                m = fmaxf(m, __shfl_xor(m, 16));
                m = fmaxf(m, __shfl_xor(m, 32));
                float sum = 0.f;
#pragma unroll
                for (int j = 0; j < 8; ++j) { xs[j] = exp2f((xs[j] - m) * cs); sum += xs[j]; }
#pragma unroll
                for (int j = 0; j < 4; ++j) { ms[j] = (g < 2) ? exp2f((ms[j] - m) * cs) : 0.f; sum += ms[j]; }
                sum += __shfl_xor(sum, 16);
                sum += __shfl_xor(sum, 32);
                const float inv = 1.f / sum;
                u32x4 px = {pack2(xs[0] * inv, xs[1] * inv), pack2(xs[2] * inv, xs[3] * inv), pack2(xs[4] * inv, xs[5] * inv), pack2(xs[6] * inv, xs[7] * inv)};
                pm[hh] = ms * inv;
                acc = mfma32(uf[hh], __builtin_bit_cast(bf16x8, px), acc);
            }
            acc = mfma32(yp0, pack8(pm[0], pm[1]), acc);
            acc = mfma32(yp1, pack8(pm[2], pm[3]), acc);
            {
                const char* up = ws + OFF_UF + (((size_t)(s + 1) * NT16 + tB) * NH + 4 * wave) * KB + lane * 16;
#pragma unroll
                for (int k = 0; k < 4; ++k) uf[k] = ldfrag(up + (size_t)k * KB);
            }
            *(f32x4*)(lds + CH_LDS_RB + wave * 1024 + lane * 16) = acc;
        }
        __syncthreads();
        if (hasB && wave == 0) {
            f32x4 v = *(const f32x4*)(lds + CH_LDS_RB + lane * 16);
#pragma unroll
            for (int w = 1; w < 4; ++w) v += *(const f32x4*)(lds + CH_LDS_RB + w * 1024 + lane * 16);
            if (i < 8) {
                u32x2 pk = {pack2(v[0], v[1]), pack2(v[2], v[3])};
                __builtin_amdgcn_raw_buffer_store_b64(pk, rs, (unsigned)((s + 1) * 7168 + (tB >> 1) * 512 + g * 128 + i * 16 + (tB & 1) * 8), 0, 16);
            }
        }
    }
}

__global__ __launch_bounds__(256, 1) void k_memrows(char* __restrict__ ws) {
    const int wave = threadIdx.x >> 6, lane = threadIdx.x & 63;
    const int unit = blockIdx.x;
    const int tile0 = unit * 4;
    const int i = lane & 15, g = lane >> 4;
    bf16x8 w[4][KK];
    {
        const char* wp = ws + OFF_WF + (size_t)tile0 * KK * KB + lane * 16;
#pragma unroll
        for (int t = 0; t < 4; ++t)
#pragma unroll
            for (int kk = 0; kk < KK; ++kk) w[t][kk] = ldfrag(wp + (size_t)(t * KK + kk) * KB);
    }
    const int type = tile0 < 448 ? 0 : 1;
    for (int sp = wave; sp < NSEG / 2; sp += 4) {
        f32x4 acc[4];
#pragma unroll
        for (int t = 0; t < 4; ++t) acc[t] = (f32x4){0.f, 0.f, 0.f, 0.f};
        const char* mp = ws + OFF_MEMF + (size_t)(2 * sp + (i >> 3)) * 7168 + g * 128 + (i & 7) * 16;
        if (type == 0) {
#pragma unroll
            for (int kk = 0; kk < KK; ++kk) {
                bf16x8 xf = ldfrag(mp + kk * 512);
#pragma unroll
                for (int t = 0; t < 4; ++t) acc[t] = mfma32(w[t][kk], xf, acc[t]);
            }
            const int h = tile0 / 28, t0 = tile0 % 28;
#pragma unroll
            for (int p = 0; p < 2; ++p)
                *(bf16x8*)(ws + OFF_T2F + ((size_t)h * NSEG + 2 * sp + (i >> 3)) * 7168 + ((t0 >> 1) + p) * 512 + g * 128 + (i & 7) * 16) = pack8(acc[2 * p], acc[2 * p + 1]);
        } else {
#pragma unroll
            for (int kk = 0; kk < KK; ++kk) {
                bf16x8 xf = ldfrag(mp + kk * 512);
#pragma unroll
                for (int t = 0; t < 4; ++t) acc[t] = mfma32(xf, w[t][kk], acc[t]);
            }
            const int h = (tile0 - 448) / 28, t0 = (tile0 - 448) % 28;
#pragma unroll
            for (int t = 0; t < 4; ++t)
                *(bf16x4*)(ws + OFF_YF + (((size_t)(2 * sp + (g >> 1)) * NT16 + t0 + t) * 8 + (h >> 1)) * 512 + (g & 1) * 256 + i * 16 + (h & 1) * 8) = pack4(acc[t]);
        }
    }
}

__global__ __launch_bounds__(256) void k_f1(char* __restrict__ ws) {
    const int wave = threadIdx.x >> 6, lane = threadIdx.x & 63;
    const int u = blockIdx.x * 4 + wave;
    const int s = u >> 4, h = u & 15;
    const int i = lane & 15, g = lane >> 4;
    f32x4 sc[2][3];
#pragma unroll
    for (int q = 0; q < 2; ++q)
#pragma unroll
        for (int k = 0; k < 3; ++k) sc[q][k] = (f32x4){0.f, 0.f, 0.f, 0.f};
    const char* xp = ws + OFF_XNF + (size_t)(2 * s) * KK * KB + lane * 16;
    const char* gp = ws + OFF_GTF + ((size_t)h * NRT + 2 * s) * KK * KB + lane * 16;
    const char* tp = ws + OFF_T2F + ((size_t)h * NSEG + s) * 7168 + g * 128 + (i & 7) * 16;
#pragma unroll
    for (int kk = 0; kk < KK; ++kk) {
        bf16x8 g0 = ldfrag(gp + (size_t)kk * KB), g1 = ldfrag(gp + (size_t)(KK + kk) * KB), t2 = ldfrag(tp + kk * 512);
#pragma unroll
        for (int q = 0; q < 2; ++q) {
            bf16x8 xf = ldfrag(xp + (size_t)(q * KK + kk) * KB);
            sc[q][0] = mfma32(g0, xf, sc[q][0]);
            sc[q][1] = mfma32(g1, xf, sc[q][1]);
            sc[q][2] = mfma32(t2, xf, sc[q][2]);
        }
    }
    const float cs = 0.0625f * 1.4426950408889634f;
    const float NEG = -3.0e38f;
#pragma unroll
    for (int q = 0; q < 2; ++q) {
        const int qi = 16 * q + i;
        float xs[8], ms[4];
#pragma unroll
        for (int j = 0; j < 8; ++j) {
            const int key = 16 * (j >> 2) + 4 * g + (j & 3);
            xs[j] = (key <= qi) ? sc[q][j >> 2][j & 3] : NEG;
        }
#pragma unroll
        for (int j = 0; j < 4; ++j) ms[j] = (g < 2) ? sc[q][2][j] : NEG;
        float m = fmaxf(fmaxf(fmaxf(xs[0], xs[1]), fmaxf(xs[2], xs[3])), fmaxf(fmaxf(xs[4], xs[5]), fmaxf(xs[6], xs[7])));
        m = fmaxf(m, fmaxf(fmaxf(ms[0], ms[1]), fmaxf(ms[2], ms[3])));
        m = fmaxf(m, __shfl_xor(m, 16));
        m = fmaxf(m, __shfl_xor(m, 32));
        float sum = 0.f;
#pragma unroll
        for (int j = 0; j < 8; ++j) { const int key = 16 * (j >> 2) + 4 * g + (j & 3); xs[j] = (key <= qi) ? exp2f((xs[j] - m) * cs) : 0.f; sum += xs[j]; }
#pragma unroll
        for (int j = 0; j < 4; ++j) { ms[j] = (g < 2) ? exp2f((ms[j] - m) * cs) : 0.f; sum += ms[j]; }
        sum += __shfl_xor(sum, 16);
        sum += __shfl_xor(sum, 32);
        const float inv = 1.f / sum;
        u32x4 px = {pack2(xs[0] * inv, xs[1] * inv), pack2(xs[2] * inv, xs[3] * inv), pack2(xs[4] * inv, xs[5] * inv), pack2(xs[6] * inv, xs[7] * inv)};
        *(u32x4*)(ws + OFF_PXF + (((size_t)s * 2 + q) * NH + h) * KB + lane * 16) = px;
        u32x2 pmv = {pack2(ms[0] * inv, ms[1] * inv), pack2(ms[2] * inv, ms[3] * inv)};
        *(u32x2*)(ws + OFF_PMF + (((size_t)s * 2 + q) * 8 + (h >> 1)) * KB + lane * 16 + (h & 1) * 8) = pmv;
    }
}

__global__ __launch_bounds__(256) void k_f2(const unsigned short* __restrict__ x, const unsigned short* __restrict__ b2, const char* __restrict__ ws, unsigned short* __restrict__ out) {
    const int wave = threadIdx.x >> 6, lane = threadIdx.x & 63;
    const int s = blockIdx.x / 7, t = (blockIdx.x % 7) * 4 + wave;
    const int i = lane & 15, g = lane >> 4;
    f32x4 acc[2] = {{0.f, 0.f, 0.f, 0.f}, {0.f, 0.f, 0.f, 0.f}};
    {
        const char* up = ws + OFF_UF + ((size_t)s * NT16 + t) * NH * KB + lane * 16;
        const char* pp = ws + OFF_PXF + (size_t)s * 2 * NH * KB + lane * 16;
#pragma unroll 4
        for (int h = 0; h < NH; ++h) {
            bf16x8 a = ldfrag(up + (size_t)h * KB);
            acc[0] = mfma32(a, ldfrag(pp + (size_t)h * KB), acc[0]);
            acc[1] = mfma32(a, ldfrag(pp + (size_t)(NH + h) * KB), acc[1]);
        }
    }
    {
        const char* yp = ws + OFF_YF + ((size_t)s * NT16 + t) * 8 * 512 + (g & 1) * 256 + i * 16;
        const char* pp = ws + OFF_PMF + (size_t)s * 2 * 8 * KB + lane * 16;
#pragma unroll 4
        for (int hp = 0; hp < 8; ++hp) {
            bf16x8 a = ldfrag(yp + hp * 512);
            acc[0] = mfma32(a, ldfrag(pp + (size_t)hp * KB), acc[0]);
            acc[1] = mfma32(a, ldfrag(pp + (size_t)(8 + hp) * KB), acc[1]);
        }
    }
    {
        const char* wp = ws + OFF_W2F + (size_t)t * KF * KB + lane * 16;
        const char* hp = ws + OFF_HF + (size_t)(2 * s) * KF * KB + lane * 16;
#pragma unroll 4
        for (int kf = 0; kf < KF; ++kf) {
            bf16x8 a = ldfrag(wp + (size_t)kf * KB);
            acc[0] = mfma32(a, ldfrag(hp + (size_t)kf * KB), acc[0]);
            acc[1] = mfma32(a, ldfrag(hp + (size_t)(KF + kf) * KB), acc[1]);
        }
    }
    u32x2 bb = *(const u32x2*)(b2 + 16 * t + 4 * g);
    const float bv[4] = {bf_lo(bb.x), bf_hi(bb.x), bf_lo(bb.y), bf_hi(bb.y)};
#pragma unroll
    for (int q = 0; q < 2; ++q) {
        const size_t o = (size_t)(32 * s + 16 * q + i) * DM + 16 * t + 4 * g;
        u32x2 xv = *(const u32x2*)(x + o);
        u32x2 r = {pack2(bf_lo(xv.x) + acc[q][0] + bv[0], bf_hi(xv.x) + acc[q][1] + bv[1]), pack2(bf_lo(xv.y) + acc[q][2] + bv[2], bf_hi(xv.y) + acc[q][3] + bv[3])};
        *(u32x2*)(out + o) = r;
    }
}

extern "C" void kernel_launch(void* const* d_in, const int* in_sizes, int n_in,
                              void* d_out, int out_size, void* d_ws, size_t ws_size,
                              hipStream_t stream) {
    const unsigned short* x = (const unsigned short*)d_in[0];
    const unsigned short* mem0 = (const unsigned short*)d_in[1];
    const unsigned short* Wq = (const unsigned short*)d_in[2];
    const unsigned short* Wk = (const unsigned short*)d_in[3];
    const unsigned short* Wv = (const unsigned short*)d_in[4];
    const unsigned short* Wo = (const unsigned short*)d_in[5];
    const unsigned short* w1 = (const unsigned short*)d_in[8];
    const unsigned short* b1 = (const unsigned short*)d_in[9];
    const unsigned short* w2 = (const unsigned short*)d_in[10];
    const unsigned short* b2 = (const unsigned short*)d_in[11];
    char* ws = (char*)d_ws;
    unsigned short* out = (unsigned short*)d_out;
    hipFuncSetAttribute((const void*)k_chain, hipFuncAttributeMaxDynamicSharedMemorySize, CH_LDS_SIZE);
    k_ln<<<512, 256, 0, stream>>>(x, mem0, ws);
    k_ab<<<336, 256, 0, stream>>>(Wq, Wk, Wv, Wo, ws);
    k_w12<<<784, 256, 0, stream>>>(w1, w2, ws);
    k_work<<<252, 256, 0, stream>>>(b1, ws);
    k_chain<<<256, 256, CH_LDS_SIZE, stream>>>(ws);
    k_memrows<<<224, 256, 0, stream>>>(ws);
    k_f1<<<256, 256, 0, stream>>>(ws);
    k_f2<<<448, 256, 0, stream>>>(x, b2, ws, out);
}
```

```cpp
#include <hip/hip_runtime.h>
#include <hip/hip_bf16.h>

typedef __attribute__((ext_vector_type(8))) short bf16x8;
typedef __attribute__((ext_vector_type(4))) short bf16x4;
typedef __attribute__((ext_vector_type(4))) float f32x4;
typedef __attribute__((ext_vector_type(4))) unsigned u32x4;
typedef __attribute__((ext_vector_type(2))) unsigned u32x2;
typedef __attribute__((ext_vector_type(2))) __bf16 bf2_t;
#define DI __device__ __forceinline__

constexpr int DM = 448, KK = 14, NT16 = 28, NH = 16, NSEG = 64, NRT = 128, FFD = 1792, KF = 56;
constexpr size_t KB = 1024;
constexpr size_t OFF_XNF = 0;
constexpr size_t OFF_WF = OFF_XNF + (size_t)NRT * KK * KB;
constexpr size_t OFF_ANF = OFF_WF + (size_t)1008 * KK * KB;
constexpr size_t OFF_W2F = OFF_ANF + (size_t)448 * KK * KB;
constexpr size_t OFF_GTF = OFF_W2F + (size_t)28 * KF * KB;
constexpr size_t OFF_UF = OFF_GTF + (size_t)NH * NRT * KK * KB;
constexpr size_t OFF_HF = OFF_UF + (size_t)NSEG * NT16 * NH * KB;
constexpr size_t OFF_MEMF = OFF_HF + (size_t)NRT * KF * KB;
constexpr size_t OFF_SMX = OFF_MEMF + (size_t)NSEG * 7168;
constexpr size_t OFF_SMM = OFF_SMX + (size_t)NSEG * 8192;
constexpr size_t OFF_SST = OFF_SMM + (size_t)NSEG * 8192;
constexpr size_t OFF_T2F = OFF_SST + (size_t)NSEG * 2048;
constexpr size_t OFF_YF = OFF_T2F + (size_t)NH * NSEG * 7168;
constexpr size_t OFF_PXF = OFF_YF + (size_t)NSEG * NT16 * 8 * 512;
constexpr size_t OFF_PMF = OFF_PXF + (size_t)NSEG * 2 * NH * KB;
constexpr size_t OFF_TMO = OFF_PMF + (size_t)NSEG * 2 * 8 * KB;
constexpr size_t WS_END = OFF_TMO + 4096;
constexpr size_t OFF_DBG = WS_END;
#define STAMP(k)

constexpr unsigned SENT = 0xFFFFFFFFu;
constexpr unsigned kSpinCap = 1u << 16;

DI unsigned pack2(float a, float b) { bf2_t v = {(__bf16)a, (__bf16)b}; return __builtin_bit_cast(unsigned, v); }
DI float bf_lo(unsigned u) { return __uint_as_float(u << 16); }
DI float bf_hi(unsigned u) { return __uint_as_float(u & 0xFFFF0000u); }
DI float bf2f(unsigned short v) { return __uint_as_float((unsigned)v << 16); }
DI f32x4 mfma32(bf16x8 a, bf16x8 b, f32x4 c) { return __builtin_amdgcn_mfma_f32_16x16x32_bf16(a, b, c, 0, 0, 0); }
DI f32x4 mfma16(bf16x4 a, bf16x4 b, f32x4 c) { return __builtin_amdgcn_mfma_f32_16x16x16bf16_1k(a, b, c, 0, 0, 0); }
DI bf16x8 ldfrag(const char* p) { return *(const bf16x8*)p; }
DI bf16x8 pack8(f32x4 a, f32x4 b) {
    u32x4 r = {pack2(a[0], a[1]), pack2(a[2], a[3]), pack2(b[0], b[1]), pack2(b[2], b[3])};
    return __builtin_bit_cast(bf16x8, r);
}
DI bf16x4 pack4(f32x4 a) { u32x2 r = {pack2(a[0], a[1]), pack2(a[2], a[3])}; return __builtin_bit_cast(bf16x4, r); }

DI bool spin_fail(unsigned& spins, unsigned* tmo) {
    if (++spins > kSpinCap) { __hip_atomic_store(tmo, 1u, __ATOMIC_RELAXED, __HIP_MEMORY_SCOPE_AGENT); return true; }
    if ((spins & 63u) == 0u) return __builtin_amdgcn_readfirstlane(__hip_atomic_load(tmo, __ATOMIC_RELAXED, __HIP_MEMORY_SCOPE_AGENT)) != 0u;
    return false;
}

__global__ __launch_bounds__(256) void k_ln(const unsigned short* __restrict__ x, const unsigned short* __restrict__ mem0, char* __restrict__ ws) {
    const int wave = threadIdx.x >> 6, lane = threadIdx.x & 63;
    const int gw = blockIdx.x * 4 + wave;
    {
        const int row = gw;
        const unsigned short* xr = x + (size_t)row * DM;
        float v[2][4];
        float sum = 0.f;
#pragma unroll
        for (int it = 0; it < 2; ++it) {
            int grp = lane + it * 64;
            if (grp < 112) {
                u32x2 u = *(const u32x2*)(xr + grp * 4);
                v[it][0] = bf_lo(u.x); v[it][1] = bf_hi(u.x); v[it][2] = bf_lo(u.y); v[it][3] = bf_hi(u.y);
            } else { v[it][0] = v[it][1] = v[it][2] = v[it][3] = 0.f; }
            sum += v[it][0] + v[it][1] + v[it][2] + v[it][3];
        }
#pragma unroll
        for (int o = 32; o >= 1; o >>= 1) sum += __shfl_xor(sum, o);
        const float mean = sum * (1.f / DM);
        float sq = 0.f;
#pragma unroll
        for (int it = 0; it < 2; ++it) {
            int grp = lane + it * 64;
            if (grp < 112) {
#pragma unroll
                for (int j = 0; j < 4; ++j) { float d = v[it][j] - mean; sq += d * d; }
            }
        }
#pragma unroll
        for (int o = 32; o >= 1; o >>= 1) sq += __shfl_xor(sq, o);
        const float rstd = 1.f / sqrtf(sq * (1.f / DM) + 1e-5f);
        const int rt = row >> 4, i = row & 15;
#pragma unroll
        for (int it = 0; it < 2; ++it) {
            int grp = lane + it * 64;
            if (grp < 112) {
                int d = grp * 4, kk = d >> 5, dd = d & 31, half = dd >> 4, g = (dd & 15) >> 2;
                u32x2 o = {pack2((v[it][0] - mean) * rstd, (v[it][1] - mean) * rstd), pack2((v[it][2] - mean) * rstd, (v[it][3] - mean) * rstd)};
                *(u32x2*)(ws + OFF_XNF + ((size_t)(rt * KK + kk) * 64 + (i + 16 * g)) * 16 + half * 8) = o;
            }
        }
    }
    {
        const size_t nb = (size_t)(NSEG - 1) * 7168 + 2 * (size_t)NSEG * 8192 + (size_t)NSEG * 2048;
        const size_t n16 = nb / 16;
        u32x4 sv = {SENT, SENT, SENT, SENT};
        for (size_t idx = (size_t)blockIdx.x * 256 + threadIdx.x; idx < n16; idx += (size_t)gridDim.x * 256)
            *(u32x4*)(ws + OFF_MEMF + 7168 + idx * 16) = sv;
    }
    if (blockIdx.x == 0) {
        for (int e = threadIdx.x; e < 8 * 112; e += 256) {
            int r = e / 112, grp = e % 112;
            int d = grp * 4, kk = d >> 5, dd = d & 31, half = dd >> 4, g = (dd & 15) >> 2;
            u32x2 u = *(const u32x2*)(mem0 + r * DM + d);
            *(u32x2*)(ws + OFF_MEMF + kk * 512 + g * 128 + r * 16 + half * 8) = u;
        }
        if (threadIdx.x == 0) *(unsigned*)(ws + OFF_TMO) = 0;
    }
}

__global__ __launch_bounds__(256) void k_ab(const unsigned short* __restrict__ Wq, const unsigned short* __restrict__ Wk,
                                            const unsigned short* __restrict__ Wv, const unsigned short* __restrict__ Wo, char* __restrict__ ws) {
    const int wave = threadIdx.x >> 6, lane = threadIdx.x & 63;
    const int u = blockIdx.x * 4 + wave;
    const int type = u / 448, h = (u % 448) / 28, tau = u % 28;
    const int i = lane & 15, g = lane >> 4;
    bf16x8 fix[8];
    const unsigned short* loopW;
    if (type == 0) {
        const unsigned short* p = Wq + (size_t)(16 * tau + i) * 4096 + h * 256 + 8 * g;
#pragma unroll
        for (int ke = 0; ke < 8; ++ke) fix[ke] = *(const bf16x8*)(p + ke * 32);
        loopW = Wk;
    } else if (type == 1) {
        const unsigned short* p = Wk + (size_t)(16 * tau + i) * 4096 + h * 256 + 8 * g;
#pragma unroll
        for (int ke = 0; ke < 8; ++ke) fix[ke] = *(const bf16x8*)(p + ke * 32);
        loopW = Wq;
    } else {
#pragma unroll
        for (int ke = 0; ke < 8; ++ke)
#pragma unroll
            for (int j = 0; j < 8; ++j)
                fix[ke][j] = (short)Wo[((size_t)h * 256 + ke * 32 + 8 * g + j) * DM + 16 * tau + i];
        loopW = Wv;
    }
    char* dst = (type == 0) ? ws + OFF_WF + (size_t)(h * 28 + tau) * KK * KB
              : (type == 1) ? ws + OFF_ANF + (size_t)(h * 28 + tau) * KK * KB
                            : ws + OFF_WF + (size_t)(448 + h * 28 + tau) * KK * KB;
    for (int kk = 0; kk < KK; ++kk) {
        f32x4 acc[2] = {{0.f, 0.f, 0.f, 0.f}, {0.f, 0.f, 0.f, 0.f}};
#pragma unroll
        for (int tt = 0; tt < 2; ++tt) {
            const unsigned short* p = loopW + (size_t)(32 * kk + 16 * tt + i) * 4096 + h * 256 + 8 * g;
#pragma unroll
            for (int ke = 0; ke < 8; ++ke) acc[tt] = mfma32(*(const bf16x8*)(p + ke * 32), fix[ke], acc[tt]);
        }
        *(bf16x8*)(dst + (size_t)kk * KB + lane * 16) = pack8(acc[0], acc[1]);
    }
}

__global__ __launch_bounds__(256) void k_w12(const unsigned short* __restrict__ w1, const unsigned short* __restrict__ w2, char* __restrict__ ws) {
    const int wave = threadIdx.x >> 6, lane = threadIdx.x & 63;
    const int f = blockIdx.x * 4 + wave;
    const int i = lane & 15, g = lane >> 4;
    bf16x8 v;
    if (f < 112 * KK) {
        int tf = f / KK, kk = f % KK;
#pragma unroll
        for (int j = 0; j < 8; ++j) v[j] = (short)w1[(size_t)(32 * kk + 16 * (j >> 2) + 4 * g + (j & 3)) * FFD + 16 * tf + i];
        *(bf16x8*)(ws + OFF_WF + ((size_t)(896 + tf) * KK + kk) * KB + lane * 16) = v;
    } else {
        int f2 = f - 112 * KK;
        int t = f2 / KF, kf = f2 % KF;
#pragma unroll
        for (int j = 0; j < 8; ++j) v[j] = (short)w2[(size_t)(32 * kf + 16 * (j >> 2) + 4 * g + (j & 3)) * DM + 16 * t + i];
        *(bf16x8*)(ws + OFF_W2F + ((size_t)t * KF + kf) * KB + lane * 16) = v;
    }
}

DI float gelu_tanh(float z) {
    float u = 0.7978845608028654f * (z + 0.044715f * z * z * z);
    float e = __expf(2.f * u);
    float t = 1.f - 2.f / (1.f + e);
    return 0.5f * z * (1.f + t);
}

__global__ __launch_bounds__(256, 1) void k_work(const unsigned short* __restrict__ b1, char* __restrict__ ws) {
    const int wave = threadIdx.x >> 6, lane = threadIdx.x & 63;
    const int unit = blockIdx.x;
    const int tile0 = unit * 4;
    const int i = lane & 15, g = lane >> 4;
    bf16x8 w[4][KK];
    {
        const char* wp = ws + OFF_WF + (size_t)tile0 * KK * KB + lane * 16;
#pragma unroll
        for (int t = 0; t < 4; ++t)
#pragma unroll
            for (int kk = 0; kk < KK; ++kk) w[t][kk] = ldfrag(wp + (size_t)(t * KK + kk) * KB);
    }
    const int type = tile0 < 448 ? 0 : (tile0 < 896 ? 1 : 2);
    for (int s = wave; s < NSEG; s += 4) {
        f32x4 acc[2][4];
#pragma unroll
        for (int q = 0; q < 2; ++q)
#pragma unroll
            for (int t = 0; t < 4; ++t) acc[q][t] = (f32x4){0.f, 0.f, 0.f, 0.f};
        const char* xp = ws + OFF_XNF + (size_t)(2 * s) * KK * KB + lane * 16;
        if (type == 1) {
#pragma unroll
            for (int kk = 0; kk < KK; ++kk)
#pragma unroll
                for (int q = 0; q < 2; ++q) {
                    bf16x8 xf = ldfrag(xp + (size_t)(q * KK + kk) * KB);
#pragma unroll
                    for (int t = 0; t < 4; ++t) acc[q][t] = mfma32(xf, w[t][kk], acc[q][t]);
                }
            const int h = (tile0 - 448) / 28, t0 = (tile0 - 448) % 28;
#pragma unroll
            for (int t = 0; t < 4; ++t)
                *(bf16x8*)(ws + OFF_UF + (((size_t)s * NT16 + t0 + t) * NH + h) * KB + lane * 16) = pack8(acc[0][t], acc[1][t]);
        } else {
#pragma unroll
            for (int kk = 0; kk < KK; ++kk)
#pragma unroll
                for (int q = 0; q < 2; ++q) {
                    bf16x8 xf = ldfrag(xp + (size_t)(q * KK + kk) * KB);
#pragma unroll
                    for (int t = 0; t < 4; ++t) acc[q][t] = mfma32(w[t][kk], xf, acc[q][t]);
                }
            if (type == 0) {
                const int h = tile0 / 28, t0 = tile0 % 28;
#pragma unroll
                for (int q = 0; q < 2; ++q)
#pragma unroll
                    for (int p = 0; p < 2; ++p)
                        *(bf16x8*)(ws + OFF_GTF + (((size_t)h * NRT + 2 * s + q) * KK + (t0 >> 1) + p) * KB + lane * 16) = pack8(acc[q][2 * p], acc[q][2 * p + 1]);
            } else {
                const int tf0 = tile0 - 896;
#pragma unroll
                for (int t = 0; t < 4; ++t) {
                    u32x2 bb = *(const u32x2*)(b1 + 16 * (tf0 + t) + 4 * g);
                    float bv[4] = {bf_lo(bb.x), bf_hi(bb.x), bf_lo(bb.y), bf_hi(bb.y)};
#pragma unroll
                    for (int q = 0; q < 2; ++q)
#pragma unroll
                        for (int r = 0; r < 4; ++r) acc[q][t][r] = gelu_tanh(acc[q][t][r] + bv[r]);
                }
#pragma unroll
                for (int q = 0; q < 2; ++q)
#pragma unroll
                    for (int p = 0; p < 2; ++p)
                        *(bf16x8*)(ws + OFF_HF + ((size_t)(2 * s + q) * KF + (tf0 >> 1) + p) * KB + lane * 16) = pack8(acc[q][2 * p], acc[q][2 * p + 1]);
            }
        }
    }
}

constexpr int CH_LDS_B = 0;
constexpr int CH_LDS_MEM = 112 * 1024;
constexpr int CH_LDS_RA = CH_LDS_MEM + 7168;
constexpr int CH_LDS_RB = CH_LDS_RA + 8192;
constexpr int CH_LDS_PW = CH_LDS_RB + 4096;
constexpr int CH_LDS_SIZE = CH_LDS_PW + 4096;

DI float xmax16(float v) { auto r = __builtin_amdgcn_permlane16_swap(__float_as_uint(v), __float_as_uint(v), false, false); return fmaxf(__uint_as_float(r[0]), __uint_as_float(r[1])); }
DI float xmax32(float v) { auto r = __builtin_amdgcn_permlane32_swap(__float_as_uint(v), __float_as_uint(v), false, false); return fmaxf(__uint_as_float(r[0]), __uint_as_float(r[1])); }
DI float xadd16(float v) { auto r = __builtin_amdgcn_permlane16_swap(__float_as_uint(v), __float_as_uint(v), false, false); return __uint_as_float(r[0]) + __uint_as_float(r[1]); }
DI float xadd32(float v) { auto r = __builtin_amdgcn_permlane32_swap(__float_as_uint(v), __float_as_uint(v), false, false); return __uint_as_float(r[0]) + __uint_as_float(r[1]); }
DI float qswap(float v) { return __uint_as_float(__builtin_amdgcn_update_dpp(0, __float_as_uint(v), 0xB1, 0xF, 0xF, true)); }
DI unsigned umax4(u32x4 v) { return max(max(v.x, v.y), max(v.z, v.w)); }
DI float fexp2(float x) { return __builtin_amdgcn_exp2f(x); }

#define D1 3
#define D2 3

__global__ __launch_bounds__(256, 1) void k_chain(char* __restrict__ ws) {
    extern __shared__ __attribute__((aligned(16))) char lds[];
    if (blockIdx.x & 7) return;
    const int c = blockIdx.x >> 3;
    const int tid = threadIdx.x, wave = __builtin_amdgcn_readfirstlane(tid >> 6), lane = tid & 63;
    const int i = lane & 15, g = lane >> 4;
    const int h = c >> 1, half = c & 1;
    const bool hasB = c < NT16;
    const int tB = hasB ? c : 0;
    unsigned* tmo = (unsigned*)(ws + OFF_TMO);
    const __amdgpu_buffer_rsrc_t rs = __builtin_amdgcn_make_buffer_rsrc(ws + OFF_MEMF, 0, (int)(OFF_T2F - OFF_MEMF), 0x00020000);
    constexpr unsigned R_SMX = (unsigned)(OFF_SMX - OFF_MEMF), R_SMM = (unsigned)(OFF_SMM - OFF_MEMF), R_SST = (unsigned)(OFF_SST - OFF_MEMF);
    const float cs = 0.0625f * 1.4426950408889634f;

    bf16x8 wa[3][KK], wsh[7], wb[2][KK];
    {
        const char* ap = ws + OFF_ANF + (size_t)(h * 28 + 14 * half) * KK * KB + lane * 16;
#pragma unroll
        for (int t = 0; t < 3; ++t)
#pragma unroll
            for (int kk = 0; kk < KK; ++kk) wa[t][kk] = ldfrag(ap + (size_t)((3 * wave + t) * KK + kk) * KB);
#pragma unroll
        for (int k = 0; k < 7; ++k) wsh[k] = ldfrag(ap + (size_t)((12 + (wave >> 1)) * KK + 7 * (wave & 1) + k) * KB);
        const char* bp = ws + OFF_WF + (size_t)(448 + tB) * KK * KB + lane * 16;
#pragma unroll
        for (int hh = 0; hh < 2; ++hh)
#pragma unroll
            for (int kk = 0; kk < KK; ++kk) wb[hh][kk] = ldfrag(bp + (size_t)((4 * wave + hh) * 28 * KK + kk) * KB);
#pragma unroll
        for (int hh = 0; hh < 2; ++hh)
#pragma unroll
            for (int kk = 0; kk < KK; ++kk)
                *(bf16x8*)(lds + CH_LDS_B + (size_t)((wave * 2 + hh) * KK + kk) * KB + lane * 16) = ldfrag(bp + (size_t)((4 * wave + 2 + hh) * 28 * KK + kk) * KB);
    }
    const int gk0 = wave < 2 ? 4 * wave : 8 + 3 * (wave - 2);
    const int gkn = wave < 2 ? 4 : 3;
    bf16x8 gf[4], uf[4];
    {
        const char* gp = ws + OFF_GTF + (((size_t)h * NRT + half) * KK + gk0) * KB + lane * 16;
#pragma unroll
        for (int k = 0; k < 4; ++k) gf[k] = ldfrag(gp + (size_t)(k < gkn ? k : 0) * KB);
        const char* up = ws + OFF_UF + ((size_t)tB * NH + 4 * wave) * KB + lane * 16;
#pragma unroll
        for (int k = 0; k < 4; ++k) uf[k] = ldfrag(up + (size_t)k * KB);
    }
    __syncthreads();
    const char* mp = lds + CH_LDS_MEM + g * 128 + (i & 7) * 16;
    const int hh2 = lane >> 4, r2 = (lane >> 1) & 7, q2 = lane & 1;

    for (int s = 0; s < NSEG - 1; ++s) {
        STAMP(0);
        {
            u32x4 x0, x1;
            unsigned spins = 0;
            const unsigned o0 = (unsigned)(s * 7168 + tid * 16), o1 = o0 + (tid < 192 ? 4096 : 0);
            for (int d = 0; d < D1; ++d) __builtin_amdgcn_s_sleep(1);
            for (;;) {
                x0 = __builtin_amdgcn_raw_buffer_load_b128(rs, o0, 0, 16);
                x1 = __builtin_amdgcn_raw_buffer_load_b128(rs, o1, 0, 16);
                bool ok = max(umax4(x0), umax4(x1)) != SENT;
                if (__all(ok)) break;
                if (spin_fail(spins, tmo)) break;
            }
            *(u32x4*)(lds + CH_LDS_MEM + tid * 16) = x0;
            if (tid < 192) *(u32x4*)(lds + CH_LDS_MEM + 4096 + tid * 16) = x1;
        }
        STAMP(1);
        __syncthreads();
        STAMP(2);
        bf16x8 mf[KK];
#pragma unroll
        for (int kk = 0; kk < KK; ++kk) mf[kk] = *(const bf16x8*)(mp + kk * 512);
        {
            bf16x8 mg[4];
            bf16x4 mk[4];
#pragma unroll
            for (int k = 0; k < 4; ++k) mg[k] = *(const bf16x8*)(mp + (gk0 + (k < gkn ? k : 0)) * 512);
#pragma unroll
            for (int t = 0; t < 4; ++t) {
                const int tl = (t < 3) ? 3 * wave + t : 12 + (wave >> 1);
                const int tg = 14 * half + tl;
                mk[t] = *(const bf16x4*)(mp + (tg >> 1) * 512 + (tg & 1) * 8);
            }
            f32x4 ta[4];
#pragma unroll
            for (int t = 0; t < 4; ++t) ta[t] = (f32x4){0.f, 0.f, 0.f, 0.f};
            f32x4 sx = {0.f, 0.f, 0.f, 0.f};
#pragma unroll
            for (int kk = 0; kk < KK; ++kk)
#pragma unroll
                for (int t = 0; t < 3; ++t) ta[t] = mfma32(wa[t][kk], mf[kk], ta[t]);
            if (wave & 1) {
#pragma unroll
                for (int k = 0; k < 7; ++k) ta[3] = mfma32(wsh[k], mf[7 + k], ta[3]);
            } else {
#pragma unroll
                for (int k = 0; k < 7; ++k) ta[3] = mfma32(wsh[k], mf[k], ta[3]);
            }
            f32x4 sm = {0.f, 0.f, 0.f, 0.f};
#pragma unroll
            for (int t = 0; t < 3; ++t) sm = mfma16(mk[t], pack4(ta[t]), sm);
#pragma unroll
            for (int k = 0; k < 3; ++k) sx = mfma32(gf[k], mg[k], sx);
            if (wave < 2) sx = mfma32(gf[3], mg[3], sx);
            sm = mfma16(mk[3], pack4(ta[3]), sm);
            *(f32x4*)(lds + CH_LDS_RA + (wave * 2 + 0) * 1024 + lane * 16) = sx;
            *(f32x4*)(lds + CH_LDS_RA + (wave * 2 + 1) * 1024 + lane * 16) = sm;
        }
        STAMP(3);
        __syncthreads();
        if (wave < 2) {
            f32x4 v0 = *(const f32x4*)(lds + CH_LDS_RA + (0 + wave) * 1024 + lane * 16);
            f32x4 v1 = *(const f32x4*)(lds + CH_LDS_RA + (2 + wave) * 1024 + lane * 16);
            f32x4 v2 = *(const f32x4*)(lds + CH_LDS_RA + (4 + wave) * 1024 + lane * 16);
            f32x4 v3 = *(const f32x4*)(lds + CH_LDS_RA + (6 + wave) * 1024 + lane * 16);
            f32x4 v = (v0 + v1) + (v2 + v3);
            if (wave == 0) {
                float m = fmaxf(fmaxf(v[0], v[1]), fmaxf(v[2], v[3]));
                m = xmax16(m); m = xmax32(m);
                const float mc = m * cs;
                float e0 = fexp2(v[0] * cs - mc), e1 = fexp2(v[1] * cs - mc), e2 = fexp2(v[2] * cs - mc), e3 = fexp2(v[3] * cs - mc);
                u32x2 pk = {pack2(e0, e1), pack2(e2, e3)};
                if (i < 8) __builtin_amdgcn_raw_buffer_store_b64(pk, rs, R_SMX + (unsigned)(s * 8192 + ((h * 8 + i) * 4 + g) * 16 + half * 8), 0, 16);
                float sum = (bf_lo(pk.x) + bf_hi(pk.x)) + (bf_lo(pk.y) + bf_hi(pk.y));
                sum = xadd16(sum); sum = xadd32(sum);
                u32x2 st = {__float_as_uint(m), __float_as_uint(sum)};
                if (lane < 8) __builtin_amdgcn_raw_buffer_store_b64(st, rs, R_SST + (unsigned)(s * 2048 + (h * 8 + i) * 16 + half * 8), 0, 16);
            } else {
                if (i < 8 && g < 2)
                    __builtin_amdgcn_raw_buffer_store_b128(__builtin_bit_cast(u32x4, v), rs, R_SMM + (unsigned)(s * 8192 + ((h * 2 + half) * 8 + i) * 32 + g * 16), 0, 16);
            }
        }
        {
            const char* gp = ws + OFF_GTF + (((size_t)h * NRT + 2 * (s + 1) + half) * KK + gk0) * KB + lane * 16;
#pragma unroll
            for (int k = 0; k < 4; ++k) gf[k] = ldfrag(gp + (size_t)(k < gkn ? k : 0) * KB);
        }
        if (hasB) {
            f32x4 ya[4];
#pragma unroll
            for (int hh = 0; hh < 4; ++hh) ya[hh] = (f32x4){0.f, 0.f, 0.f, 0.f};
            const char* lb = lds + CH_LDS_B + (size_t)(wave * 2) * KK * KB + lane * 16;
#pragma unroll
            for (int k0 = 0; k0 < KK; k0 += 7) {
                bf16x8 l0[7], l1[7];
#pragma unroll
                for (int k = 0; k < 7; ++k) { l0[k] = *(const bf16x8*)(lb + (size_t)(k0 + k) * KB); l1[k] = *(const bf16x8*)(lb + (size_t)(KK + k0 + k) * KB); }
#pragma unroll
                for (int k = 0; k < 7; ++k) {
                    ya[0] = mfma32(mf[k0 + k], wb[0][k0 + k], ya[0]);
                    ya[1] = mfma32(mf[k0 + k], wb[1][k0 + k], ya[1]);
                }
#pragma unroll
                for (int k = 0; k < 7; ++k) {
                    ya[2] = mfma32(mf[k0 + k], l0[k], ya[2]);
                    ya[3] = mfma32(mf[k0 + k], l1[k], ya[3]);
                }
            }
            bf16x8 yp0 = pack8(ya[0], ya[1]), yp1 = pack8(ya[2], ya[3]);
            STAMP(4);
            u32x4 qx[4], qm0, qm1, qs;
            {
                unsigned spins = 0;
                const unsigned ox = R_SMX + (unsigned)(s * 8192 + ((4 * wave * 8 + (i & 7)) * 4 + g) * 16);
                const unsigned om = R_SMM + (unsigned)(s * 8192 + (((4 * wave + hh2) * 2) * 8 + r2) * 32 + q2 * 16);
                const unsigned os = R_SST + (unsigned)(s * 2048 + ((4 * wave + hh2) * 8 + r2) * 16);
                for (int d = 0; d < D2; ++d) __builtin_amdgcn_s_sleep(1);
                for (;;) {
#pragma unroll
                    for (int hh = 0; hh < 4; ++hh) qx[hh] = __builtin_amdgcn_raw_buffer_load_b128(rs, ox + hh * 512, 0, 16);
                    qm0 = __builtin_amdgcn_raw_buffer_load_b128(rs, om, 0, 16);
                    qm1 = __builtin_amdgcn_raw_buffer_load_b128(rs, om + 256, 0, 16);
                    qs = __builtin_amdgcn_raw_buffer_load_b128(rs, os, 0, 16);
                    unsigned mx = max(max(max(umax4(qx[0]), umax4(qx[1])), max(umax4(qx[2]), umax4(qx[3]))), max(max(umax4(qm0), umax4(qm1)), umax4(qs)));
                    if (__all(mx != SENT)) break;
                    if (spin_fail(spins, tmo)) break;
                }
            }
            STAMP(5);
            f32x4 dd[8];
#pragma unroll
            for (int hh = 0; hh < 4; ++hh) {
                const u32x4 u = __builtin_bit_cast(u32x4, uf[hh]);
                u32x2 ulo = {u.x, u.y}, uhi = {u.z, u.w}, elo = {qx[hh].x, qx[hh].y}, ehi = {qx[hh].z, qx[hh].w};
                dd[2 * hh] = mfma16(__builtin_bit_cast(bf16x4, ulo), __builtin_bit_cast(bf16x4, elo), (f32x4){0.f, 0.f, 0.f, 0.f});
                dd[2 * hh + 1] = mfma16(__builtin_bit_cast(bf16x4, uhi), __builtin_bit_cast(bf16x4, ehi), (f32x4){0.f, 0.f, 0.f, 0.f});
            }
            f32x4 ms = __builtin_bit_cast(f32x4, qm0) + __builtin_bit_cast(f32x4, qm1);
            const float m0 = __uint_as_float(qs.x), s0 = __uint_as_float(qs.y), m1 = __uint_as_float(qs.z), s1 = __uint_as_float(qs.w);
            float mx = fmaxf(fmaxf(ms[0], ms[1]), fmaxf(ms[2], ms[3]));
            mx = fmaxf(mx, qswap(mx));
            const float Mc = fmaxf(mx, fmaxf(m0, m1)) * cs;
            float e0 = fexp2(ms[0] * cs - Mc), e1 = fexp2(ms[1] * cs - Mc), e2 = fexp2(ms[2] * cs - Mc), e3 = fexp2(ms[3] * cs - Mc);
            float f0 = fexp2(m0 * cs - Mc), f1 = fexp2(m1 * cs - Mc);
            float sm = (e0 + e1) + (e2 + e3);
            sm += qswap(sm);
            const float inv = __builtin_amdgcn_rcpf(sm + f0 * s0 + f1 * s1);
            char* pw = lds + CH_LDS_PW + wave * 1024;
            *(u32x2*)(pw + ((hh2 * 8 + r2) * 2 + q2) * 8) = (u32x2){pack2(e0 * inv, e1 * inv), pack2(e2 * inv, e3 * inv)};
            if (q2 == 0) *(u32x2*)(pw + 512 + (r2 * 4 + hh2) * 8) = (u32x2){__float_as_uint(f0 * inv), __float_as_uint(f1 * inv)};
            __builtin_amdgcn_fence(__ATOMIC_RELEASE, "wavefront");
            __builtin_amdgcn_wave_barrier();
            __builtin_amdgcn_fence(__ATOMIC_ACQUIRE, "wavefront");
            u32x2 pa[4];
#pragma unroll
            for (int hh = 0; hh < 4; ++hh) pa[hh] = *(const u32x2*)(pw + ((hh * 8 + (i & 7)) * 2 + (g & 1)) * 8);
            f32x4 fa = *(const f32x4*)(pw + 512 + (i & 7) * 32), fb = *(const f32x4*)(pw + 512 + (i & 7) * 32 + 16);
            if (g >= 2) {
#pragma unroll
                for (int hh = 0; hh < 4; ++hh) pa[hh] = (u32x2){0u, 0u};
            }
            f32x4 acc = dd[0] * fa[0] + dd[1] * fa[1] + dd[2] * fa[2] + dd[3] * fa[3] + dd[4] * fb[0] + dd[5] * fb[1] + dd[6] * fb[2] + dd[7] * fb[3];
            u32x4 p01 = {pa[0].x, pa[0].y, pa[1].x, pa[1].y}, p23 = {pa[2].x, pa[2].y, pa[3].x, pa[3].y};
            acc = mfma32(yp0, __builtin_bit_cast(bf16x8, p01), acc);
            acc = mfma32(yp1, __builtin_bit_cast(bf16x8, p23), acc);
            *(f32x4*)(lds + CH_LDS_RB + wave * 1024 + lane * 16) = acc;
        }
        STAMP(6);
        __syncthreads();
        if (hasB) {
            if (wave == 0) {
                f32x4 v0 = *(const f32x4*)(lds + CH_LDS_RB + lane * 16);
                f32x4 v1 = *(const f32x4*)(lds + CH_LDS_RB + 1024 + lane * 16);
                f32x4 v2 = *(const f32x4*)(lds + CH_LDS_RB + 2048 + lane * 16);
                f32x4 v3 = *(const f32x4*)(lds + CH_LDS_RB + 3072 + lane * 16);
                f32x4 v = (v0 + v1) + (v2 + v3);
                if (i < 8) {
                    u32x2 pk = {pack2(v[0], v[1]), pack2(v[2], v[3])};
                    __builtin_amdgcn_raw_buffer_store_b64(pk, rs, (unsigned)((s + 1) * 7168 + (tB >> 1) * 512 + g * 128 + i * 16 + (tB & 1) * 8), 0, 16);
                }
            }
            const char* up = ws + OFF_UF + (((size_t)(s + 1) * NT16 + tB) * NH + 4 * wave) * KB + lane * 16;
#pragma unroll
            for (int k = 0; k < 4; ++k) uf[k] = ldfrag(up + (size_t)k * KB);
        }
    }
}

__global__ __launch_bounds__(256, 1) void k_memrows(char* __restrict__ ws) {
    const int wave = threadIdx.x >> 6, lane = threadIdx.x & 63;
    const int unit = blockIdx.x;
    const int tile0 = unit * 4;
    const int i = lane & 15, g = lane >> 4;
    bf16x8 w[4][KK];
    {
        const char* wp = ws + OFF_WF + (size_t)tile0 * KK * KB + lane * 16;
#pragma unroll
        for (int t = 0; t < 4; ++t)
#pragma unroll
            for (int kk = 0; kk < KK; ++kk) w[t][kk] = ldfrag(wp + (size_t)(t * KK + kk) * KB);
    }
    const int type = tile0 < 448 ? 0 : 1;
    for (int sp = wave; sp < NSEG / 2; sp += 4) {
        f32x4 acc[4];
#pragma unroll
        for (int t = 0; t < 4; ++t) acc[t] = (f32x4){0.f, 0.f, 0.f, 0.f};
        const char* mp = ws + OFF_MEMF + (size_t)(2 * sp + (i >> 3)) * 7168 + g * 128 + (i & 7) * 16;
        if (type == 0) {
#pragma unroll
            for (int kk = 0; kk < KK; ++kk) {
                bf16x8 xf = ldfrag(mp + kk * 512);
#pragma unroll
                for (int t = 0; t < 4; ++t) acc[t] = mfma32(w[t][kk], xf, acc[t]);
            }
            const int h = tile0 / 28, t0 = tile0 % 28;
#pragma unroll
            for (int p = 0; p < 2; ++p)
                *(bf16x8*)(ws + OFF_T2F + ((size_t)h * NSEG + 2 * sp + (i >> 3)) * 7168 + ((t0 >> 1) + p) * 512 + g * 128 + (i & 7) * 16) = pack8(acc[2 * p], acc[2 * p + 1]);
        } else {
#pragma unroll
            for (int kk = 0; kk < KK; ++kk) {
                bf16x8 xf = ldfrag(mp + kk * 512);
#pragma unroll
                for (int t = 0; t < 4; ++t) acc[t] = mfma32(xf, w[t][kk], acc[t]);
            }
            const int h = (tile0 - 448) / 28, t0 = (tile0 - 448) % 28;
#pragma unroll
            for (int t = 0; t < 4; ++t)
                *(bf16x4*)(ws + OFF_YF + (((size_t)(2 * sp + (g >> 1)) * NT16 + t0 + t) * 8 + (h >> 1)) * 512 + (g & 1) * 256 + i * 16 + (h & 1) * 8) = pack4(acc[t]);
        }
    }
}

__global__ __launch_bounds__(256) void k_f1(char* __restrict__ ws) {
    const int wave = threadIdx.x >> 6, lane = threadIdx.x & 63;
    const int u = blockIdx.x * 4 + wave;
    const int s = u >> 4, h = u & 15;
    const int i = lane & 15, g = lane >> 4;
    f32x4 sc[2][3];
#pragma unroll
    for (int q = 0; q < 2; ++q)
#pragma unroll
        for (int k = 0; k < 3; ++k) sc[q][k] = (f32x4){0.f, 0.f, 0.f, 0.f};
    const char* xp = ws + OFF_XNF + (size_t)(2 * s) * KK * KB + lane * 16;
    const char* gp = ws + OFF_GTF + ((size_t)h * NRT + 2 * s) * KK * KB + lane * 16;
    const char* tp = ws + OFF_T2F + ((size_t)h * NSEG + s) * 7168 + g * 128 + (i & 7) * 16;
#pragma unroll
    for (int kk = 0; kk < KK; ++kk) {
        bf16x8 g0 = ldfrag(gp + (size_t)kk * KB), g1 = ldfrag(gp + (size_t)(KK + kk) * KB), t2 = ldfrag(tp + kk * 512);
#pragma unroll
        for (int q = 0; q < 2; ++q) {
            bf16x8 xf = ldfrag(xp + (size_t)(q * KK + kk) * KB);
            sc[q][0] = mfma32(g0, xf, sc[q][0]);
            sc[q][1] = mfma32(g1, xf, sc[q][1]);
            sc[q][2] = mfma32(t2, xf, sc[q][2]);
        }
    }
    const float cs = 0.0625f * 1.4426950408889634f;
    const float NEG = -3.0e38f;
#pragma unroll
    for (int q = 0; q < 2; ++q) {
        const int qi = 16 * q + i;
        float xs[8], ms[4];
#pragma unroll
        for (int j = 0; j < 8; ++j) {
            const int key = 16 * (j >> 2) + 4 * g + (j & 3);
            xs[j] = (key <= qi) ? sc[q][j >> 2][j & 3] : NEG;
        }
#pragma unroll
        for (int j = 0; j < 4; ++j) ms[j] = (g < 2) ? sc[q][2][j] : NEG;
        float m = fmaxf(fmaxf(fmaxf(xs[0], xs[1]), fmaxf(xs[2], xs[3])), fmaxf(fmaxf(xs[4], xs[5]), fmaxf(xs[6], xs[7])));
        m = fmaxf(m, fmaxf(fmaxf(ms[0], ms[1]), fmaxf(ms[2], ms[3])));
        m = fmaxf(m, __shfl_xor(m, 16));
        m = fmaxf(m, __shfl_xor(m, 32));
        float sum = 0.f;
#pragma unroll
        for (int j = 0; j < 8; ++j) { const int key = 16 * (j >> 2) + 4 * g + (j & 3); xs[j] = (key <= qi) ? exp2f((xs[j] - m) * cs) : 0.f; sum += xs[j]; }
#pragma unroll
        for (int j = 0; j < 4; ++j) { ms[j] = (g < 2) ? exp2f((ms[j] - m) * cs) : 0.f; sum += ms[j]; }
        sum += __shfl_xor(sum, 16);
        sum += __shfl_xor(sum, 32);
        const float inv = 1.f / sum;
        u32x4 px = {pack2(xs[0] * inv, xs[1] * inv), pack2(xs[2] * inv, xs[3] * inv), pack2(xs[4] * inv, xs[5] * inv), pack2(xs[6] * inv, xs[7] * inv)};
        *(u32x4*)(ws + OFF_PXF + (((size_t)s * 2 + q) * NH + h) * KB + lane * 16) = px;
        u32x2 pmv = {pack2(ms[0] * inv, ms[1] * inv), pack2(ms[2] * inv, ms[3] * inv)};
        *(u32x2*)(ws + OFF_PMF + (((size_t)s * 2 + q) * 8 + (h >> 1)) * KB + lane * 16 + (h & 1) * 8) = pmv;
    }
}

__global__ __launch_bounds__(256) void k_f2(const unsigned short* __restrict__ x, const unsigned short* __restrict__ b2, const char* __restrict__ ws, unsigned short* __restrict__ out) {
    const int wave = threadIdx.x >> 6, lane = threadIdx.x & 63;
    const int s = blockIdx.x / 7, t = (blockIdx.x % 7) * 4 + wave;
    const int i = lane & 15, g = lane >> 4;
    f32x4 acc[2] = {{0.f, 0.f, 0.f, 0.f}, {0.f, 0.f, 0.f, 0.f}};
    {
        const char* up = ws + OFF_UF + ((size_t)s * NT16 + t) * NH * KB + lane * 16;
        const char* pp = ws + OFF_PXF + (size_t)s * 2 * NH * KB + lane * 16;
#pragma unroll 4
        for (int h = 0; h < NH; ++h) {
            bf16x8 a = ldfrag(up + (size_t)h * KB);
            acc[0] = mfma32(a, ldfrag(pp + (size_t)h * KB), acc[0]);
            acc[1] = mfma32(a, ldfrag(pp + (size_t)(NH + h) * KB), acc[1]);
        }
    }
    {
        const char* yp = ws + OFF_YF + ((size_t)s * NT16 + t) * 8 * 512 + (g & 1) * 256 + i * 16;
        const char* pp = ws + OFF_PMF + (size_t)s * 2 * 8 * KB + lane * 16;
#pragma unroll 4
        for (int hp = 0; hp < 8; ++hp) {
            bf16x8 a = ldfrag(yp + hp * 512);
            acc[0] = mfma32(a, ldfrag(pp + (size_t)hp * KB), acc[0]);
            acc[1] = mfma32(a, ldfrag(pp + (size_t)(8 + hp) * KB), acc[1]);
        }
    }
    {
        const char* wp = ws + OFF_W2F + (size_t)t * KF * KB + lane * 16;
        const char* hp = ws + OFF_HF + (size_t)(2 * s) * KF * KB + lane * 16;
#pragma unroll 4
        for (int kf = 0; kf < KF; ++kf) {
            bf16x8 a = ldfrag(wp + (size_t)kf * KB);
            acc[0] = mfma32(a, ldfrag(hp + (size_t)kf * KB), acc[0]);
            acc[1] = mfma32(a, ldfrag(hp + (size_t)(KF + kf) * KB), acc[1]);
        }
    }
    u32x2 bb = *(const u32x2*)(b2 + 16 * t + 4 * g);
    const float bv[4] = {bf_lo(bb.x), bf_hi(bb.x), bf_lo(bb.y), bf_hi(bb.y)};
#pragma unroll
    for (int q = 0; q < 2; ++q) {
        const size_t o = (size_t)(32 * s + 16 * q + i) * DM + 16 * t + 4 * g;
        u32x2 xv = *(const u32x2*)(x + o);
        u32x2 r = {pack2(bf_lo(xv.x) + acc[q][0] + bv[0], bf_hi(xv.x) + acc[q][1] + bv[1]), pack2(bf_lo(xv.y) + acc[q][2] + bv[2], bf_hi(xv.y) + acc[q][3] + bv[3])};
        *(u32x2*)(out + o) = r;
    }
}

extern "C" void kernel_launch(void* const* d_in, const int* in_sizes, int n_in,
                              void* d_out, int out_size, void* d_ws, size_t ws_size,
                              hipStream_t stream) {
    const unsigned short* x = (const unsigned short*)d_in[0];
    const unsigned short* mem0 = (const unsigned short*)d_in[1];
    const unsigned short* Wq = (const unsigned short*)d_in[2];
    const unsigned short* Wk = (const unsigned short*)d_in[3];
    const unsigned short* Wv = (const unsigned short*)d_in[4];
    const unsigned short* Wo = (const unsigned short*)d_in[5];
    const unsigned short* w1 = (const unsigned short*)d_in[8];
    const unsigned short* b1 = (const unsigned short*)d_in[9];
    const unsigned short* w2 = (const unsigned short*)d_in[10];
    const unsigned short* b2 = (const unsigned short*)d_in[11];
    char* ws = (char*)d_ws;
    unsigned short* out = (unsigned short*)d_out;
    hipFuncSetAttribute((const void*)k_chain, hipFuncAttributeMaxDynamicSharedMemorySize, CH_LDS_SIZE);
    k_ln<<<512, 256, 0, stream>>>(x, mem0, ws);
    k_ab<<<336, 256, 0, stream>>>(Wq, Wk, Wv, Wo, ws);
    k_w12<<<784, 256, 0, stream>>>(w1, w2, ws);
    k_work<<<252, 256, 0, stream>>>(b1, ws);
    k_chain<<<256, 256, CH_LDS_SIZE, stream>>>(ws);
    k_memrows<<<224, 256, 0, stream>>>(ws);
    k_f1<<<256, 256, 0, stream>>>(ws);
    k_f2<<<448, 256, 0, stream>>>(x, b2, ws, out);
}
```

```cpp
#include <hip/hip_runtime.h>
#include <hip/hip_bf16.h>

typedef __attribute__((ext_vector_type(8))) short bf16x8;
typedef __attribute__((ext_vector_type(4))) short bf16x4;
typedef __attribute__((ext_vector_type(4))) float f32x4;
typedef __attribute__((ext_vector_type(4))) unsigned u32x4;
typedef __attribute__((ext_vector_type(2))) unsigned u32x2;
typedef __attribute__((ext_vector_type(2))) __bf16 bf2_t;
#define DI __device__ __forceinline__

constexpr int DM = 448, KK = 14, NT16 = 28, NH = 16, NSEG = 64, NRT = 128, FFD = 1792, KF = 56;
constexpr size_t KB = 1024;
constexpr size_t OFF_XNF = 0;
constexpr size_t OFF_WF = OFF_XNF + (size_t)NRT * KK * KB;
constexpr size_t OFF_ANF = OFF_WF + (size_t)1008 * KK * KB;
constexpr size_t OFF_W2F = OFF_ANF + (size_t)448 * KK * KB;
constexpr size_t OFF_GTF = OFF_W2F + (size_t)28 * KF * KB;
constexpr size_t OFF_UF = OFF_GTF + (size_t)NH * NRT * KK * KB;
constexpr size_t OFF_HF = OFF_UF + (size_t)NSEG * NT16 * NH * KB;
constexpr size_t OFF_MEMF = OFF_HF + (size_t)NRT * KF * KB;
constexpr size_t OFF_SMX = OFF_MEMF + (size_t)NSEG * 7168;
constexpr size_t OFF_SMM = OFF_SMX + (size_t)NSEG * 8192;
constexpr size_t OFF_SST = OFF_SMM + (size_t)NSEG * 8192;
constexpr size_t OFF_T2F = OFF_SST + (size_t)NSEG * 2048;
constexpr size_t OFF_YF = OFF_T2F + (size_t)NH * NSEG * 7168;
constexpr size_t OFF_PXF = OFF_YF + (size_t)NSEG * NT16 * 8 * 512;
constexpr size_t OFF_PMF = OFF_PXF + (size_t)NSEG * 2 * NH * KB;
constexpr size_t OFF_TMO = OFF_PMF + (size_t)NSEG * 2 * 8 * KB;
constexpr size_t WS_END = OFF_TMO + 4096;
constexpr size_t OFF_DBG = WS_END;
#define STAMP(k)

constexpr unsigned SENT = 0xFFFFFFFFu;
constexpr unsigned kSpinCap = 1u << 16;

DI unsigned pack2(float a, float b) { bf2_t v = {(__bf16)a, (__bf16)b}; return __builtin_bit_cast(unsigned, v); }
DI float bf_lo(unsigned u) { return __uint_as_float(u << 16); }
DI float bf_hi(unsigned u) { return __uint_as_float(u & 0xFFFF0000u); }
DI float bf2f(unsigned short v) { return __uint_as_float((unsigned)v << 16); }
DI f32x4 mfma32(bf16x8 a, bf16x8 b, f32x4 c) { return __builtin_amdgcn_mfma_f32_16x16x32_bf16(a, b, c, 0, 0, 0); }
DI f32x4 mfma16(bf16x4 a, bf16x4 b, f32x4 c) { return __builtin_amdgcn_mfma_f32_16x16x16bf16_1k(a, b, c, 0, 0, 0); }
DI bf16x8 ldfrag(const char* p) { return *(const bf16x8*)p; }
DI bf16x8 pack8(f32x4 a, f32x4 b) {
    u32x4 r = {pack2(a[0], a[1]), pack2(a[2], a[3]), pack2(b[0], b[1]), pack2(b[2], b[3])};
    return __builtin_bit_cast(bf16x8, r);
}
DI bf16x4 pack4(f32x4 a) { u32x2 r = {pack2(a[0], a[1]), pack2(a[2], a[3])}; return __builtin_bit_cast(bf16x4, r); }

DI bool spin_fail(unsigned& spins, unsigned* tmo) {
    if (++spins > kSpinCap) { __hip_atomic_store(tmo, 1u, __ATOMIC_RELAXED, __HIP_MEMORY_SCOPE_AGENT); return true; }
    if ((spins & 63u) == 0u) return __builtin_amdgcn_readfirstlane(__hip_atomic_load(tmo, __ATOMIC_RELAXED, __HIP_MEMORY_SCOPE_AGENT)) != 0u;
    return false;
}

__global__ __launch_bounds__(256) void k_ln(const unsigned short* __restrict__ x, const unsigned short* __restrict__ mem0, char* __restrict__ ws) {
    const int wave = threadIdx.x >> 6, lane = threadIdx.x & 63;
    const int gw = blockIdx.x * 4 + wave;
    {
        const int row = gw;
        const unsigned short* xr = x + (size_t)row * DM;
        float v[2][4];
        float sum = 0.f;
#pragma unroll
        for (int it = 0; it < 2; ++it) {
            int grp = lane + it * 64;
            if (grp < 112) {
                u32x2 u = *(const u32x2*)(xr + grp * 4);
                v[it][0] = bf_lo(u.x); v[it][1] = bf_hi(u.x); v[it][2] = bf_lo(u.y); v[it][3] = bf_hi(u.y);
            } else { v[it][0] = v[it][1] = v[it][2] = v[it][3] = 0.f; }
            sum += v[it][0] + v[it][1] + v[it][2] + v[it][3];
        }
#pragma unroll
        for (int o = 32; o >= 1; o >>= 1) sum += __shfl_xor(sum, o);
        const float mean = sum * (1.f / DM);
        float sq = 0.f;
#pragma unroll
        for (int it = 0; it < 2; ++it) {
            int grp = lane + it * 64;
            if (grp < 112) {
#pragma unroll
                for (int j = 0; j < 4; ++j) { float d = v[it][j] - mean; sq += d * d; }
            }
        }
#pragma unroll
        for (int o = 32; o >= 1; o >>= 1) sq += __shfl_xor(sq, o);
        const float rstd = 1.f / sqrtf(sq * (1.f / DM) + 1e-5f);
        const int rt = row >> 4, i = row & 15;
#pragma unroll
        for (int it = 0; it < 2; ++it) {
            int grp = lane + it * 64;
            if (grp < 112) {
                int d = grp * 4, kk = d >> 5, dd = d & 31, half = dd >> 4, g = (dd & 15) >> 2;
                u32x2 o = {pack2((v[it][0] - mean) * rstd, (v[it][1] - mean) * rstd), pack2((v[it][2] - mean) * rstd, (v[it][3] - mean) * rstd)};
                *(u32x2*)(ws + OFF_XNF + ((size_t)(rt * KK + kk) * 64 + (i + 16 * g)) * 16 + half * 8) = o;
            }
        }
    }
    {
        const size_t nb = (size_t)(NSEG - 1) * 7168 + 2 * (size_t)NSEG * 8192 + (size_t)NSEG * 2048;
        const size_t n16 = nb / 16;
        u32x4 sv = {SENT, SENT, SENT, SENT};
        for (size_t idx = (size_t)blockIdx.x * 256 + threadIdx.x; idx < n16; idx += (size_t)gridDim.x * 256)
            *(u32x4*)(ws + OFF_MEMF + 7168 + idx * 16) = sv;
    }
    if (blockIdx.x == 0) {
        for (int e = threadIdx.x; e < 8 * 112; e += 256) {
            int r = e / 112, grp = e % 112;
            int d = grp * 4, kk = d >> 5, dd = d & 31, half = dd >> 4, g = (dd & 15) >> 2;
            u32x2 u = *(const u32x2*)(mem0 + r * DM + d);
            *(u32x2*)(ws + OFF_MEMF + kk * 512 + g * 128 + r * 16 + half * 8) = u;
        }
        if (threadIdx.x == 0) *(unsigned*)(ws + OFF_TMO) = 0;
    }
}

__global__ __launch_bounds__(256) void k_ab(const unsigned short* __restrict__ Wq, const unsigned short* __restrict__ Wk,
                                            const unsigned short* __restrict__ Wv, const unsigned short* __restrict__ Wo, char* __restrict__ ws) {
    const int wave = threadIdx.x >> 6, lane = threadIdx.x & 63;
    const int u = blockIdx.x * 4 + wave;
    const int type = u / 448, h = (u % 448) / 28, tau = u % 28;
    const int i = lane & 15, g = lane >> 4;
    bf16x8 fix[8];
    const unsigned short* loopW;
    if (type == 0) {
        const unsigned short* p = Wq + (size_t)(16 * tau + i) * 4096 + h * 256 + 8 * g;
#pragma unroll
        for (int ke = 0; ke < 8; ++ke) fix[ke] = *(const bf16x8*)(p + ke * 32);
        loopW = Wk;
    } else if (type == 1) {
        const unsigned short* p = Wk + (size_t)(16 * tau + i) * 4096 + h * 256 + 8 * g;
#pragma unroll
        for (int ke = 0; ke < 8; ++ke) fix[ke] = *(const bf16x8*)(p + ke * 32);
        loopW = Wq;
    } else {
#pragma unroll
        for (int ke = 0; ke < 8; ++ke)
#pragma unroll
            for (int j = 0; j < 8; ++j)
                fix[ke][j] = (short)Wo[((size_t)h * 256 + ke * 32 + 8 * g + j) * DM + 16 * tau + i];
        loopW = Wv;
    }
    char* dst = (type == 0) ? ws + OFF_WF + (size_t)(h * 28 + tau) * KK * KB
              : (type == 1) ? ws + OFF_ANF + (size_t)(h * 28 + tau) * KK * KB
                            : ws + OFF_WF + (size_t)(448 + h * 28 + tau) * KK * KB;
    for (int kk = 0; kk < KK; ++kk) {
        f32x4 acc[2] = {{0.f, 0.f, 0.f, 0.f}, {0.f, 0.f, 0.f, 0.f}};
#pragma unroll
        for (int tt = 0; tt < 2; ++tt) {
            const unsigned short* p = loopW + (size_t)(32 * kk + 16 * tt + i) * 4096 + h * 256 + 8 * g;
#pragma unroll
            for (int ke = 0; ke < 8; ++ke) acc[tt] = mfma32(*(const bf16x8*)(p + ke * 32), fix[ke], acc[tt]);
        }
        *(bf16x8*)(dst + (size_t)kk * KB + lane * 16) = pack8(acc[0], acc[1]);
    }
}

__global__ __launch_bounds__(256) void k_w12(const unsigned short* __restrict__ w1, const unsigned short* __restrict__ w2, char* __restrict__ ws) {
    const int wave = threadIdx.x >> 6, lane = threadIdx.x & 63;
    const int f = blockIdx.x * 4 + wave;
    const int i = lane & 15, g = lane >> 4;
    bf16x8 v;
    if (f < 112 * KK) {
        int tf = f / KK, kk = f % KK;
#pragma unroll
        for (int j = 0; j < 8; ++j) v[j] = (short)w1[(size_t)(32 * kk + 16 * (j >> 2) + 4 * g + (j & 3)) * FFD + 16 * tf + i];
        *(bf16x8*)(ws + OFF_WF + ((size_t)(896 + tf) * KK + kk) * KB + lane * 16) = v;
    } else {
        int f2 = f - 112 * KK;
        int t = f2 / KF, kf = f2 % KF;
#pragma unroll
        for (int j = 0; j < 8; ++j) v[j] = (short)w2[(size_t)(32 * kf + 16 * (j >> 2) + 4 * g + (j & 3)) * DM + 16 * t + i];
        *(bf16x8*)(ws + OFF_W2F + ((size_t)t * KF + kf) * KB + lane * 16) = v;
    }
}

DI float gelu_tanh(float z) {
    float u = 0.7978845608028654f * (z + 0.044715f * z * z * z);
    float e = __expf(2.f * u);
    float t = 1.f - 2.f / (1.f + e);
    return 0.5f * z * (1.f + t);
}

__global__ __launch_bounds__(256, 1) void k_work(const unsigned short* __restrict__ b1, char* __restrict__ ws) {
    const int wave = threadIdx.x >> 6, lane = threadIdx.x & 63;
    const int unit = blockIdx.x;
    const int tile0 = unit * 4;
    const int i = lane & 15, g = lane >> 4;
    bf16x8 w[4][KK];
    {
        const char* wp = ws + OFF_WF + (size_t)tile0 * KK * KB + lane * 16;
#pragma unroll
        for (int t = 0; t < 4; ++t)
#pragma unroll
            for (int kk = 0; kk < KK; ++kk) w[t][kk] = ldfrag(wp + (size_t)(t * KK + kk) * KB);
    }
    const int type = tile0 < 448 ? 0 : (tile0 < 896 ? 1 : 2);
    for (int s = wave; s < NSEG; s += 4) {
        f32x4 acc[2][4];
#pragma unroll
        for (int q = 0; q < 2; ++q)
#pragma unroll
            for (int t = 0; t < 4; ++t) acc[q][t] = (f32x4){0.f, 0.f, 0.f, 0.f};
        const char* xp = ws + OFF_XNF + (size_t)(2 * s) * KK * KB + lane * 16;
        if (type == 1) {
#pragma unroll
            for (int kk = 0; kk < KK; ++kk)
#pragma unroll
                for (int q = 0; q < 2; ++q) {
                    bf16x8 xf = ldfrag(xp + (size_t)(q * KK + kk) * KB);
#pragma unroll
                    for (int t = 0; t < 4; ++t) acc[q][t] = mfma32(xf, w[t][kk], acc[q][t]);
                }
            const int h = (tile0 - 448) / 28, t0 = (tile0 - 448) % 28;
#pragma unroll
            for (int t = 0; t < 4; ++t)
                *(bf16x8*)(ws + OFF_UF + (((size_t)s * NT16 + t0 + t) * NH + h) * KB + lane * 16) = pack8(acc[0][t], acc[1][t]);
        } else {
#pragma unroll
            for (int kk = 0; kk < KK; ++kk)
#pragma unroll
                for (int q = 0; q < 2; ++q) {
                    bf16x8 xf = ldfrag(xp + (size_t)(q * KK + kk) * KB);
#pragma unroll
                    for (int t = 0; t < 4; ++t) acc[q][t] = mfma32(w[t][kk], xf, acc[q][t]);
                }
            if (type == 0) {
                const int h = tile0 / 28, t0 = tile0 % 28;
#pragma unroll
                for (int q = 0; q < 2; ++q)
#pragma unroll
                    for (int p = 0; p < 2; ++p)
                        *(bf16x8*)(ws + OFF_GTF + (((size_t)h * NRT + 2 * s + q) * KK + (t0 >> 1) + p) * KB + lane * 16) = pack8(acc[q][2 * p], acc[q][2 * p + 1]);
            } else {
                const int tf0 = tile0 - 896;
#pragma unroll
                for (int t = 0; t < 4; ++t) {
                    u32x2 bb = *(const u32x2*)(b1 + 16 * (tf0 + t) + 4 * g);
                    float bv[4] = {bf_lo(bb.x), bf_hi(bb.x), bf_lo(bb.y), bf_hi(bb.y)};
#pragma unroll
                    for (int q = 0; q < 2; ++q)
#pragma unroll
                        for (int r = 0; r < 4; ++r) acc[q][t][r] = gelu_tanh(acc[q][t][r] + bv[r]);
                }
#pragma unroll
                for (int q = 0; q < 2; ++q)
#pragma unroll
                    for (int p = 0; p < 2; ++p)
                        *(bf16x8*)(ws + OFF_HF + ((size_t)(2 * s + q) * KF + (tf0 >> 1) + p) * KB + lane * 16) = pack8(acc[q][2 * p], acc[q][2 * p + 1]);
            }
        }
    }
}

constexpr int CH_LDS_B = 0;
constexpr int CH_LDS_MEM = 112 * 1024;
constexpr int CH_LDS_RA = CH_LDS_MEM + 7168;
constexpr int CH_LDS_RB = CH_LDS_RA + 8192;
constexpr int CH_LDS_PW = CH_LDS_RB + 4096;
constexpr int CH_LDS_SIZE = CH_LDS_PW + 4096;

DI float xmax16(float v) { auto r = __builtin_amdgcn_permlane16_swap(__float_as_uint(v), __float_as_uint(v), false, false); return fmaxf(__uint_as_float(r[0]), __uint_as_float(r[1])); }
DI float xmax32(float v) { auto r = __builtin_amdgcn_permlane32_swap(__float_as_uint(v), __float_as_uint(v), false, false); return fmaxf(__uint_as_float(r[0]), __uint_as_float(r[1])); }
DI float xadd16(float v) { auto r = __builtin_amdgcn_permlane16_swap(__float_as_uint(v), __float_as_uint(v), false, false); return __uint_as_float(r[0]) + __uint_as_float(r[1]); }
DI float xadd32(float v) { auto r = __builtin_amdgcn_permlane32_swap(__float_as_uint(v), __float_as_uint(v), false, false); return __uint_as_float(r[0]) + __uint_as_float(r[1]); }
DI float qswap(float v) { return __uint_as_float(__builtin_amdgcn_update_dpp(0, __float_as_uint(v), 0xB1, 0xF, 0xF, true)); }
DI unsigned umax4(u32x4 v) { return max(max(v.x, v.y), max(v.z, v.w)); }
DI float fexp2(float x) { return __builtin_amdgcn_exp2f(x); }

DI void mfma_Aa0(f32x4& acc, const bf16x8& w, const bf16x8& m) { asm("v_mfma_f32_16x16x32_bf16 %0, %1, %2, 0" : "=&v"(acc) : "a"(w), "v"(m)); }
DI void mfma_Aa(f32x4& acc, const bf16x8& w, const bf16x8& m) { asm("v_mfma_f32_16x16x32_bf16 %0, %1, %2, %0" : "+v"(acc) : "a"(w), "v"(m)); }
DI void mfma_Ba0(f32x4& acc, const bf16x8& m, const bf16x8& w) { asm("v_mfma_f32_16x16x32_bf16 %0, %1, %2, 0" : "=&v"(acc) : "v"(m), "a"(w)); }
DI void mfma_Ba(f32x4& acc, const bf16x8& m, const bf16x8& w) { asm("v_mfma_f32_16x16x32_bf16 %0, %1, %2, %0" : "+v"(acc) : "v"(m), "a"(w)); }
DI void mfma_fence4(f32x4& a, f32x4& b, f32x4& c, f32x4& d) { asm volatile("s_nop 7\n\ts_nop 1" : "+v"(a), "+v"(b), "+v"(c), "+v"(d)); }
DI void mfma_fence1(f32x4& a) { asm volatile("s_nop 7\n\ts_nop 1" : "+v"(a)); }

#define D1 3
#define D2 3

__global__ __launch_bounds__(256, 1) void k_chain(char* __restrict__ ws) {
    extern __shared__ __attribute__((aligned(16))) char lds[];
    if (blockIdx.x & 7) return;
    const int c = blockIdx.x >> 3;
    const int tid = threadIdx.x, wave = __builtin_amdgcn_readfirstlane(tid >> 6), lane = tid & 63;
    const int i = lane & 15, g = lane >> 4;
    const int h = c >> 1, half = c & 1;
    const bool hasB = c < NT16;
    const int tB = hasB ? c : 0;
    unsigned* tmo = (unsigned*)(ws + OFF_TMO);
    const __amdgpu_buffer_rsrc_t rs = __builtin_amdgcn_make_buffer_rsrc(ws + OFF_MEMF, 0, (int)(OFF_T2F - OFF_MEMF), 0x00020000);
    constexpr unsigned R_SMX = (unsigned)(OFF_SMX - OFF_MEMF), R_SMM = (unsigned)(OFF_SMM - OFF_MEMF), R_SST = (unsigned)(OFF_SST - OFF_MEMF);
    const float cs = 0.0625f * 1.4426950408889634f;

    bf16x8 wa[3][KK], wsh[7], wb[2][KK];
    {
        const char* ap = ws + OFF_ANF + (size_t)(h * 28 + 14 * half) * KK * KB + lane * 16;
#pragma unroll
        for (int t = 0; t < 3; ++t)
#pragma unroll
            for (int kk = 0; kk < KK; ++kk) wa[t][kk] = ldfrag(ap + (size_t)((3 * wave + t) * KK + kk) * KB);
#pragma unroll
        for (int k = 0; k < 7; ++k) wsh[k] = ldfrag(ap + (size_t)((12 + (wave >> 1)) * KK + 7 * (wave & 1) + k) * KB);
        const char* bp = ws + OFF_WF + (size_t)(448 + tB) * KK * KB + lane * 16;
#pragma unroll
        for (int hh = 0; hh < 2; ++hh)
#pragma unroll
            for (int kk = 0; kk < KK; ++kk) wb[hh][kk] = ldfrag(bp + (size_t)((4 * wave + hh) * 28 * KK + kk) * KB);
#pragma unroll
        for (int hh = 0; hh < 2; ++hh)
#pragma unroll
            for (int kk = 0; kk < KK; ++kk)
                *(bf16x8*)(lds + CH_LDS_B + (size_t)((wave * 2 + hh) * KK + kk) * KB + lane * 16) = ldfrag(bp + (size_t)((4 * wave + 2 + hh) * 28 * KK + kk) * KB);
    }
    const int gk0 = wave < 2 ? 4 * wave : 8 + 3 * (wave - 2);
    const int gkn = wave < 2 ? 4 : 3;
    bf16x8 gf[4], uf[4];
    {
        const char* gp = ws + OFF_GTF + (((size_t)h * NRT + half) * KK + gk0) * KB + lane * 16;
#pragma unroll
        for (int k = 0; k < 4; ++k) gf[k] = ldfrag(gp + (size_t)(k < gkn ? k : 0) * KB);
    }
    __syncthreads();
    const char* mp = lds + CH_LDS_MEM + g * 128 + (i & 7) * 16;
    const int hh2 = lane >> 4, r2 = (lane >> 1) & 7, q2 = lane & 1;

    for (int s = 0; s < NSEG - 1; ++s) {
        STAMP(0);
        {
            u32x4 x0, x1;
            unsigned spins = 0;
            const unsigned o0 = (unsigned)(s * 7168 + tid * 16), o1 = o0 + (tid < 192 ? 4096 : 0);
            for (int d = 0; d < D1; ++d) __builtin_amdgcn_s_sleep(1);
            for (;;) {
                x0 = __builtin_amdgcn_raw_buffer_load_b128(rs, o0, 0, 16);
                x1 = __builtin_amdgcn_raw_buffer_load_b128(rs, o1, 0, 16);
                bool ok = max(umax4(x0), umax4(x1)) != SENT;
                if (__all(ok)) break;
                if (spin_fail(spins, tmo)) break;
            }
            *(u32x4*)(lds + CH_LDS_MEM + tid * 16) = x0;
            if (tid < 192) *(u32x4*)(lds + CH_LDS_MEM + 4096 + tid * 16) = x1;
        }
        {
            const char* up = ws + OFF_UF + (((size_t)s * NT16 + tB) * NH + 4 * wave) * KB + lane * 16;
#pragma unroll
            for (int k = 0; k < 4; ++k) uf[k] = ldfrag(up + (size_t)k * KB);
        }
        STAMP(1);
        __syncthreads();
        STAMP(2);
        bf16x8 mf[KK];
#pragma unroll
        for (int kk = 0; kk < KK; ++kk) mf[kk] = *(const bf16x8*)(mp + kk * 512);
        {
            bf16x8 mg[4];
            bf16x4 mk[4];
#pragma unroll
            for (int k = 0; k < 4; ++k) mg[k] = *(const bf16x8*)(mp + (gk0 + (k < gkn ? k : 0)) * 512);
#pragma unroll
            for (int t = 0; t < 4; ++t) {
                const int tl = (t < 3) ? 3 * wave + t : 12 + (wave >> 1);
                const int tg = 14 * half + tl;
                mk[t] = *(const bf16x4*)(mp + (tg >> 1) * 512 + (tg & 1) * 8);
            }
            f32x4 ta[4];
            f32x4 sx = {0.f, 0.f, 0.f, 0.f};
#pragma unroll
            for (int k = 0; k < 3; ++k) sx = mfma32(gf[k], mg[k], sx);
            if (wave < 2) sx = mfma32(gf[3], mg[3], sx);
            if (wave & 1) {
                mfma_Aa0(ta[3], wsh[0], mf[7]);
#pragma unroll
                for (int k = 1; k < 7; ++k) mfma_Aa(ta[3], wsh[k], mf[7 + k]);
            } else {
                mfma_Aa0(ta[3], wsh[0], mf[0]);
#pragma unroll
                for (int k = 1; k < 7; ++k) mfma_Aa(ta[3], wsh[k], mf[k]);
            }
#pragma unroll
            for (int t = 0; t < 3; ++t) mfma_Aa0(ta[t], wa[t][0], mf[0]);
#pragma unroll
            for (int kk = 1; kk < KK; ++kk)
#pragma unroll
                for (int t = 0; t < 3; ++t) mfma_Aa(ta[t], wa[t][kk], mf[kk]);
            mfma_fence4(ta[0], ta[1], ta[2], ta[3]);
            f32x4 sm = {0.f, 0.f, 0.f, 0.f};
#pragma unroll
            for (int t = 0; t < 4; ++t) sm = mfma16(mk[t], pack4(ta[t]), sm);
            *(f32x4*)(lds + CH_LDS_RA + (wave * 2 + 0) * 1024 + lane * 16) = sx;
            *(f32x4*)(lds + CH_LDS_RA + (wave * 2 + 1) * 1024 + lane * 16) = sm;
        }
        STAMP(3);
        __syncthreads();
        if (wave < 2) {
            f32x4 v0 = *(const f32x4*)(lds + CH_LDS_RA + (0 + wave) * 1024 + lane * 16);
            f32x4 v1 = *(const f32x4*)(lds + CH_LDS_RA + (2 + wave) * 1024 + lane * 16);
            f32x4 v2 = *(const f32x4*)(lds + CH_LDS_RA + (4 + wave) * 1024 + lane * 16);
            f32x4 v3 = *(const f32x4*)(lds + CH_LDS_RA + (6 + wave) * 1024 + lane * 16);
            f32x4 v = (v0 + v1) + (v2 + v3);
            if (wave == 0) {
                float m = fmaxf(fmaxf(v[0], v[1]), fmaxf(v[2], v[3]));
                m = xmax16(m); m = xmax32(m);
                const float mc = m * cs;
                float e0 = fexp2(v[0] * cs - mc), e1 = fexp2(v[1] * cs - mc), e2 = fexp2(v[2] * cs - mc), e3 = fexp2(v[3] * cs - mc);
                u32x2 pk = {pack2(e0, e1), pack2(e2, e3)};
                if (i < 8) __builtin_amdgcn_raw_buffer_store_b64(pk, rs, R_SMX + (unsigned)(s * 8192 + ((h * 8 + i) * 4 + g) * 16 + half * 8), 0, 16);
                float sum = (bf_lo(pk.x) + bf_hi(pk.x)) + (bf_lo(pk.y) + bf_hi(pk.y));
                sum = xadd16(sum); sum = xadd32(sum);
                u32x2 st = {__float_as_uint(m), __float_as_uint(sum)};
                if (lane < 8) __builtin_amdgcn_raw_buffer_store_b64(st, rs, R_SST + (unsigned)(s * 2048 + (h * 8 + i) * 16 + half * 8), 0, 16);
            } else {
                if (i < 8 && g < 2)
                    __builtin_amdgcn_raw_buffer_store_b128(__builtin_bit_cast(u32x4, v), rs, R_SMM + (unsigned)(s * 8192 + ((h * 2 + half) * 8 + i) * 32 + g * 16), 0, 16);
            }
        }
        {
            const char* gp = ws + OFF_GTF + (((size_t)h * NRT + 2 * (s + 1) + half) * KK + gk0) * KB + lane * 16;
#pragma unroll
            for (int k = 0; k < 4; ++k) gf[k] = ldfrag(gp + (size_t)(k < gkn ? k : 0) * KB);
        }
        if (hasB) {
            f32x4 ya[4];
#pragma unroll
            for (int hh = 1; hh < 4; ++hh) ya[hh] = (f32x4){0.f, 0.f, 0.f, 0.f};
            const char* lb = lds + CH_LDS_B + (size_t)(wave * 2) * KK * KB + lane * 16;
            bf16x8 l0[KK], l1[KK];
#pragma unroll
            for (int k = 0; k < KK; ++k) { l0[k] = *(const bf16x8*)(lb + (size_t)k * KB); l1[k] = *(const bf16x8*)(lb + (size_t)(KK + k) * KB); }
            mfma_Ba0(ya[0], mf[0], wb[0][0]);
            ya[1] = mfma32(mf[0], wb[1][0], ya[1]);
#pragma unroll
            for (int k = 1; k < KK; ++k) {
                mfma_Ba(ya[0], mf[k], wb[0][k]);
                ya[1] = mfma32(mf[k], wb[1][k], ya[1]);
            }
#pragma unroll
            for (int k = 0; k < KK; ++k) {
                ya[2] = mfma32(mf[k], l0[k], ya[2]);
                ya[3] = mfma32(mf[k], l1[k], ya[3]);
            }
            mfma_fence1(ya[0]);
            bf16x8 yp0 = pack8(ya[0], ya[1]), yp1 = pack8(ya[2], ya[3]);
            STAMP(4);
            u32x4 qx[4], qm0, qm1, qs;
            {
                unsigned spins = 0;
                const unsigned ox = R_SMX + (unsigned)(s * 8192 + ((4 * wave * 8 + (i & 7)) * 4 + g) * 16);
                const unsigned om = R_SMM + (unsigned)(s * 8192 + (((4 * wave + hh2) * 2) * 8 + r2) * 32 + q2 * 16);
                const unsigned os = R_SST + (unsigned)(s * 2048 + ((4 * wave + hh2) * 8 + r2) * 16);
                for (int d = 0; d < D2; ++d) __builtin_amdgcn_s_sleep(1);
                for (;;) {
#pragma unroll
                    for (int hh = 0; hh < 4; ++hh) qx[hh] = __builtin_amdgcn_raw_buffer_load_b128(rs, ox + hh * 512, 0, 16);
                    qm0 = __builtin_amdgcn_raw_buffer_load_b128(rs, om, 0, 16);
                    qm1 = __builtin_amdgcn_raw_buffer_load_b128(rs, om + 256, 0, 16);
                    qs = __builtin_amdgcn_raw_buffer_load_b128(rs, os, 0, 16);
                    unsigned mx = max(max(max(umax4(qx[0]), umax4(qx[1])), max(umax4(qx[2]), umax4(qx[3]))), max(max(umax4(qm0), umax4(qm1)), umax4(qs)));
                    if (__all(mx != SENT)) break;
                    if (spin_fail(spins, tmo)) break;
                }
            }
            STAMP(5);
            f32x4 dd[8];
#pragma unroll
            for (int hh = 0; hh < 4; ++hh) {
                const u32x4 u = __builtin_bit_cast(u32x4, uf[hh]);
                u32x2 ulo = {u.x, u.y}, uhi = {u.z, u.w}, elo = {qx[hh].x, qx[hh].y}, ehi = {qx[hh].z, qx[hh].w};
                dd[2 * hh] = mfma16(__builtin_bit_cast(bf16x4, ulo), __builtin_bit_cast(bf16x4, elo), (f32x4){0.f, 0.f, 0.f, 0.f});
                dd[2 * hh + 1] = mfma16(__builtin_bit_cast(bf16x4, uhi), __builtin_bit_cast(bf16x4, ehi), (f32x4){0.f, 0.f, 0.f, 0.f});
            }
            f32x4 ms = __builtin_bit_cast(f32x4, qm0) + __builtin_bit_cast(f32x4, qm1);
            const float m0 = __uint_as_float(qs.x), s0 = __uint_as_float(qs.y), m1 = __uint_as_float(qs.z), s1 = __uint_as_float(qs.w);
            float mx = fmaxf(fmaxf(ms[0], ms[1]), fmaxf(ms[2], ms[3]));
            mx = fmaxf(mx, qswap(mx));
            const float Mc = fmaxf(mx, fmaxf(m0, m1)) * cs;
            float e0 = fexp2(ms[0] * cs - Mc), e1 = fexp2(ms[1] * cs - Mc), e2 = fexp2(ms[2] * cs - Mc), e3 = fexp2(ms[3] * cs - Mc);
            float f0 = fexp2(m0 * cs - Mc), f1 = fexp2(m1 * cs - Mc);
            float sm = (e0 + e1) + (e2 + e3);
            sm += qswap(sm);
            const float inv = __builtin_amdgcn_rcpf(sm + f0 * s0 + f1 * s1);
            char* pw = lds + CH_LDS_PW + wave * 1024;
            *(u32x2*)(pw + ((hh2 * 8 + r2) * 2 + q2) * 8) = (u32x2){pack2(e0 * inv, e1 * inv), pack2(e2 * inv, e3 * inv)};
            if (q2 == 0) *(u32x2*)(pw + 512 + (r2 * 4 + hh2) * 8) = (u32x2){__float_as_uint(f0 * inv), __float_as_uint(f1 * inv)};
            __builtin_amdgcn_fence(__ATOMIC_RELEASE, "wavefront");
            __builtin_amdgcn_wave_barrier();
            __builtin_amdgcn_fence(__ATOMIC_ACQUIRE, "wavefront");
            u32x2 pa[4];
#pragma unroll
            for (int hh = 0; hh < 4; ++hh) pa[hh] = *(const u32x2*)(pw + ((hh * 8 + (i & 7)) * 2 + (g & 1)) * 8);
            f32x4 fa = *(const f32x4*)(pw + 512 + (i & 7) * 32), fb = *(const f32x4*)(pw + 512 + (i & 7) * 32 + 16);
            if (g >= 2) {
#pragma unroll
                for (int hh = 0; hh < 4; ++hh) pa[hh] = (u32x2){0u, 0u};
            }
            f32x4 acc = dd[0] * fa[0] + dd[1] * fa[1] + dd[2] * fa[2] + dd[3] * fa[3] + dd[4] * fb[0] + dd[5] * fb[1] + dd[6] * fb[2] + dd[7] * fb[3];
            u32x4 p01 = {pa[0].x, pa[0].y, pa[1].x, pa[1].y}, p23 = {pa[2].x, pa[2].y, pa[3].x, pa[3].y};
            acc = mfma32(yp0, __builtin_bit_cast(bf16x8, p01), acc);
            acc = mfma32(yp1, __builtin_bit_cast(bf16x8, p23), acc);
            *(f32x4*)(lds + CH_LDS_RB + wave * 1024 + lane * 16) = acc;
        }
        STAMP(6);
        __syncthreads();
        if (hasB) {
            if (wave == 0) {
                f32x4 v0 = *(const f32x4*)(lds + CH_LDS_RB + lane * 16);
                f32x4 v1 = *(const f32x4*)(lds + CH_LDS_RB + 1024 + lane * 16);
                f32x4 v2 = *(const f32x4*)(lds + CH_LDS_RB + 2048 + lane * 16);
                f32x4 v3 = *(const f32x4*)(lds + CH_LDS_RB + 3072 + lane * 16);
                f32x4 v = (v0 + v1) + (v2 + v3);
                if (i < 8) {
                    u32x2 pk = {pack2(v[0], v[1]), pack2(v[2], v[3])};
                    __builtin_amdgcn_raw_buffer_store_b64(pk, rs, (unsigned)((s + 1) * 7168 + (tB >> 1) * 512 + g * 128 + i * 16 + (tB & 1) * 8), 0, 16);
                }
            }
        }
    }
}

__global__ __launch_bounds__(256, 1) void k_memrows(char* __restrict__ ws) {
    const int wave = threadIdx.x >> 6, lane = threadIdx.x & 63;
    const int unit = blockIdx.x;
    const int tile0 = unit * 4;
    const int i = lane & 15, g = lane >> 4;
    bf16x8 w[4][KK];
    {
        const char* wp = ws + OFF_WF + (size_t)tile0 * KK * KB + lane * 16;
#pragma unroll
        for (int t = 0; t < 4; ++t)
#pragma unroll
            for (int kk = 0; kk < KK; ++kk) w[t][kk] = ldfrag(wp + (size_t)(t * KK + kk) * KB);
    }
    const int type = tile0 < 448 ? 0 : 1;
    for (int sp = wave; sp < NSEG / 2; sp += 4) {
        f32x4 acc[4];
#pragma unroll
        for (int t = 0; t < 4; ++t) acc[t] = (f32x4){0.f, 0.f, 0.f, 0.f};
        const char* mp = ws + OFF_MEMF + (size_t)(2 * sp + (i >> 3)) * 7168 + g * 128 + (i & 7) * 16;
        if (type == 0) {
#pragma unroll
            for (int kk = 0; kk < KK; ++kk) {
                bf16x8 xf = ldfrag(mp + kk * 512);
#pragma unroll
                for (int t = 0; t < 4; ++t) acc[t] = mfma32(w[t][kk], xf, acc[t]);
            }
            const int h = tile0 / 28, t0 = tile0 % 28;
#pragma unroll
            for (int p = 0; p < 2; ++p)
                *(bf16x8*)(ws + OFF_T2F + ((size_t)h * NSEG + 2 * sp + (i >> 3)) * 7168 + ((t0 >> 1) + p) * 512 + g * 128 + (i & 7) * 16) = pack8(acc[2 * p], acc[2 * p + 1]);
        } else {
#pragma unroll
            for (int kk = 0; kk < KK; ++kk) {
                bf16x8 xf = ldfrag(mp + kk * 512);
#pragma unroll
                for (int t = 0; t < 4; ++t) acc[t] = mfma32(xf, w[t][kk], acc[t]);
            }
            const int h = (tile0 - 448) / 28, t0 = (tile0 - 448) % 28;
#pragma unroll
            for (int t = 0; t < 4; ++t)
                *(bf16x4*)(ws + OFF_YF + (((size_t)(2 * sp + (g >> 1)) * NT16 + t0 + t) * 8 + (h >> 1)) * 512 + (g & 1) * 256 + i * 16 + (h & 1) * 8) = pack4(acc[t]);
        }
    }
}

__global__ __launch_bounds__(256) void k_f1(char* __restrict__ ws) {
    const int wave = threadIdx.x >> 6, lane = threadIdx.x & 63;
    const int u = blockIdx.x * 4 + wave;
    const int s = u >> 4, h = u & 15;
    const int i = lane & 15, g = lane >> 4;
    f32x4 sc[2][3];
#pragma unroll
    for (int q = 0; q < 2; ++q)
#pragma unroll
        for (int k = 0; k < 3; ++k) sc[q][k] = (f32x4){0.f, 0.f, 0.f, 0.f};
    const char* xp = ws + OFF_XNF + (size_t)(2 * s) * KK * KB + lane * 16;
    const char* gp = ws + OFF_GTF + ((size_t)h * NRT + 2 * s) * KK * KB + lane * 16;
    const char* tp = ws + OFF_T2F + ((size_t)h * NSEG + s) * 7168 + g * 128 + (i & 7) * 16;
#pragma unroll
    for (int kk = 0; kk < KK; ++kk) {
        bf16x8 g0 = ldfrag(gp + (size_t)kk * KB), g1 = ldfrag(gp + (size_t)(KK + kk) * KB), t2 = ldfrag(tp + kk * 512);
#pragma unroll
        for (int q = 0; q < 2; ++q) {
            bf16x8 xf = ldfrag(xp + (size_t)(q * KK + kk) * KB);
            sc[q][0] = mfma32(g0, xf, sc[q][0]);
            sc[q][1] = mfma32(g1, xf, sc[q][1]);
            sc[q][2] = mfma32(t2, xf, sc[q][2]);
        }
    }
    const float cs = 0.0625f * 1.4426950408889634f;
    const float NEG = -3.0e38f;
#pragma unroll
    for (int q = 0; q < 2; ++q) {
        const int qi = 16 * q + i;
        float xs[8], ms[4];
#pragma unroll
        for (int j = 0; j < 8; ++j) {
            const int key = 16 * (j >> 2) + 4 * g + (j & 3);
            xs[j] = (key <= qi) ? sc[q][j >> 2][j & 3] : NEG;
        }
#pragma unroll
        for (int j = 0; j < 4; ++j) ms[j] = (g < 2) ? sc[q][2][j] : NEG;
        float m = fmaxf(fmaxf(fmaxf(xs[0], xs[1]), fmaxf(xs[2], xs[3])), fmaxf(fmaxf(xs[4], xs[5]), fmaxf(xs[6], xs[7])));
        m = fmaxf(m, fmaxf(fmaxf(ms[0], ms[1]), fmaxf(ms[2], ms[3])));
        m = fmaxf(m, __shfl_xor(m, 16));
        m = fmaxf(m, __shfl_xor(m, 32));
        float sum = 0.f;
#pragma unroll
        for (int j = 0; j < 8; ++j) { const int key = 16 * (j >> 2) + 4 * g + (j & 3); xs[j] = (key <= qi) ? exp2f((xs[j] - m) * cs) : 0.f; sum += xs[j]; }
#pragma unroll
        for (int j = 0; j < 4; ++j) { ms[j] = (g < 2) ? exp2f((ms[j] - m) * cs) : 0.f; sum += ms[j]; }
        sum += __shfl_xor(sum, 16);
        sum += __shfl_xor(sum, 32);
        const float inv = 1.f / sum;
        u32x4 px = {pack2(xs[0] * inv, xs[1] * inv), pack2(xs[2] * inv, xs[3] * inv), pack2(xs[4] * inv, xs[5] * inv), pack2(xs[6] * inv, xs[7] * inv)};
        *(u32x4*)(ws + OFF_PXF + (((size_t)s * 2 + q) * NH + h) * KB + lane * 16) = px;
        u32x2 pmv = {pack2(ms[0] * inv, ms[1] * inv), pack2(ms[2] * inv, ms[3] * inv)};
        *(u32x2*)(ws + OFF_PMF + (((size_t)s * 2 + q) * 8 + (h >> 1)) * KB + lane * 16 + (h & 1) * 8) = pmv;
    }
}

__global__ __launch_bounds__(256) void k_f2(const unsigned short* __restrict__ x, const unsigned short* __restrict__ b2, const char* __restrict__ ws, unsigned short* __restrict__ out) {
    const int wave = threadIdx.x >> 6, lane = threadIdx.x & 63;
    const int s = blockIdx.x / 7, t = (blockIdx.x % 7) * 4 + wave;
    const int i = lane & 15, g = lane >> 4;
    f32x4 acc[2] = {{0.f, 0.f, 0.f, 0.f}, {0.f, 0.f, 0.f, 0.f}};
    {
        const char* up = ws + OFF_UF + ((size_t)s * NT16 + t) * NH * KB + lane * 16;
        const char* pp = ws + OFF_PXF + (size_t)s * 2 * NH * KB + lane * 16;
#pragma unroll 4
        for (int h = 0; h < NH; ++h) {
            bf16x8 a = ldfrag(up + (size_t)h * KB);
            acc[0] = mfma32(a, ldfrag(pp + (size_t)h * KB), acc[0]);
            acc[1] = mfma32(a, ldfrag(pp + (size_t)(NH + h) * KB), acc[1]);
        }
    }
    {
        const char* yp = ws + OFF_YF + ((size_t)s * NT16 + t) * 8 * 512 + (g & 1) * 256 + i * 16;
        const char* pp = ws + OFF_PMF + (size_t)s * 2 * 8 * KB + lane * 16;
#pragma unroll 4
        for (int hp = 0; hp < 8; ++hp) {
            bf16x8 a = ldfrag(yp + hp * 512);
            acc[0] = mfma32(a, ldfrag(pp + (size_t)hp * KB), acc[0]);
            acc[1] = mfma32(a, ldfrag(pp + (size_t)(8 + hp) * KB), acc[1]);
        }
    }
    {
        const char* wp = ws + OFF_W2F + (size_t)t * KF * KB + lane * 16;
        const char* hp = ws + OFF_HF + (size_t)(2 * s) * KF * KB + lane * 16;
#pragma unroll 4
        for (int kf = 0; kf < KF; ++kf) {
            bf16x8 a = ldfrag(wp + (size_t)kf * KB);
            acc[0] = mfma32(a, ldfrag(hp + (size_t)kf * KB), acc[0]);
            acc[1] = mfma32(a, ldfrag(hp + (size_t)(KF + kf) * KB), acc[1]);
        }
    }
    u32x2 bb = *(const u32x2*)(b2 + 16 * t + 4 * g);
    const float bv[4] = {bf_lo(bb.x), bf_hi(bb.x), bf_lo(bb.y), bf_hi(bb.y)};
#pragma unroll
    for (int q = 0; q < 2; ++q) {
        const size_t o = (size_t)(32 * s + 16 * q + i) * DM + 16 * t + 4 * g;
        u32x2 xv = *(const u32x2*)(x + o);
        u32x2 r = {pack2(bf_lo(xv.x) + acc[q][0] + bv[0], bf_hi(xv.x) + acc[q][1] + bv[1]), pack2(bf_lo(xv.y) + acc[q][2] + bv[2], bf_hi(xv.y) + acc[q][3] + bv[3])};
        *(u32x2*)(out + o) = r;
    }
}

extern "C" void kernel_launch(void* const* d_in, const int* in_sizes, int n_in,
                              void* d_out, int out_size, void* d_ws, size_t ws_size,
                              hipStream_t stream) {
    const unsigned short* x = (const unsigned short*)d_in[0];
    const unsigned short* mem0 = (const unsigned short*)d_in[1];
    const unsigned short* Wq = (const unsigned short*)d_in[2];
    const unsigned short* Wk = (const unsigned short*)d_in[3];
    const unsigned short* Wv = (const unsigned short*)d_in[4];
    const unsigned short* Wo = (const unsigned short*)d_in[5];
    const unsigned short* w1 = (const unsigned short*)d_in[8];
    const unsigned short* b1 = (const unsigned short*)d_in[9];
    const unsigned short* w2 = (const unsigned short*)d_in[10];
    const unsigned short* b2 = (const unsigned short*)d_in[11];
    char* ws = (char*)d_ws;
    unsigned short* out = (unsigned short*)d_out;
    hipFuncSetAttribute((const void*)k_chain, hipFuncAttributeMaxDynamicSharedMemorySize, CH_LDS_SIZE);
    k_ln<<<512, 256, 0, stream>>>(x, mem0, ws);
    k_ab<<<336, 256, 0, stream>>>(Wq, Wk, Wv, Wo, ws);
    k_w12<<<784, 256, 0, stream>>>(w1, w2, ws);
    k_work<<<252, 256, 0, stream>>>(b1, ws);
    k_chain<<<256, 256, CH_LDS_SIZE, stream>>>(ws);
    k_memrows<<<224, 256, 0, stream>>>(ws);
    k_f1<<<256, 256, 0, stream>>>(ws);
    k_f2<<<448, 256, 0, stream>>>(x, b2, ws, out);
}
```

```cpp
#include <hip/hip_runtime.h>
#include <hip/hip_bf16.h>

typedef __attribute__((ext_vector_type(8))) short bf16x8;
typedef __attribute__((ext_vector_type(4))) short bf16x4;
typedef __attribute__((ext_vector_type(4))) float f32x4;
typedef __attribute__((ext_vector_type(4))) unsigned u32x4;
typedef __attribute__((ext_vector_type(2))) unsigned u32x2;
typedef __attribute__((ext_vector_type(2))) __bf16 bf2_t;
#define DI __device__ __forceinline__

constexpr int DM = 448, KK = 14, NT16 = 28, NH = 16, NSEG = 64, NRT = 128, FFD = 1792, KF = 56;
constexpr size_t KB = 1024;
constexpr size_t OFF_XNF = 0;
constexpr size_t OFF_WF = OFF_XNF + (size_t)NRT * KK * KB;
constexpr size_t OFF_ANF = OFF_WF + (size_t)1008 * KK * KB;
constexpr size_t OFF_W2F = OFF_ANF + (size_t)448 * KK * KB;
constexpr size_t OFF_GTF = OFF_W2F + (size_t)28 * KF * KB;
constexpr size_t OFF_UF = OFF_GTF + (size_t)NH * NRT * KK * KB;
constexpr size_t OFF_HF = OFF_UF + (size_t)NSEG * NT16 * NH * KB;
constexpr size_t OFF_MEMF = OFF_HF + (size_t)NRT * KF * KB;
constexpr size_t OFF_SMX = OFF_MEMF + (size_t)NSEG * 7168;
constexpr size_t OFF_SMM = OFF_SMX + (size_t)NSEG * 8192;
constexpr size_t OFF_SST = OFF_SMM + (size_t)NSEG * 8192;
constexpr size_t OFF_T2F = OFF_SST + (size_t)NSEG * 2048;
constexpr size_t OFF_YF = OFF_T2F + (size_t)NH * NSEG * 7168;
constexpr size_t OFF_PXF = OFF_YF + (size_t)NSEG * NT16 * 8 * 512;
constexpr size_t OFF_PMF = OFF_PXF + (size_t)NSEG * 2 * NH * KB;
constexpr size_t OFF_TMO = OFF_PMF + (size_t)NSEG * 2 * 8 * KB;
constexpr size_t WS_END = OFF_TMO + 4096;
constexpr size_t OFF_DBG = WS_END;
#define STAMP(k)

constexpr unsigned SENT = 0xFFFFFFFFu;
constexpr unsigned kSpinCap = 1u << 16;

DI unsigned pack2(float a, float b) { bf2_t v = {(__bf16)a, (__bf16)b}; return __builtin_bit_cast(unsigned, v); }
DI float bf_lo(unsigned u) { return __uint_as_float(u << 16); }
DI float bf_hi(unsigned u) { return __uint_as_float(u & 0xFFFF0000u); }
DI float bf2f(unsigned short v) { return __uint_as_float((unsigned)v << 16); }
DI f32x4 mfma32(bf16x8 a, bf16x8 b, f32x4 c) { return __builtin_amdgcn_mfma_f32_16x16x32_bf16(a, b, c, 0, 0, 0); }
DI f32x4 mfma16(bf16x4 a, bf16x4 b, f32x4 c) { return __builtin_amdgcn_mfma_f32_16x16x16bf16_1k(a, b, c, 0, 0, 0); }
DI bf16x8 ldfrag(const char* p) { return *(const bf16x8*)p; }
DI bf16x8 pack8(f32x4 a, f32x4 b) {
    u32x4 r = {pack2(a[0], a[1]), pack2(a[2], a[3]), pack2(b[0], b[1]), pack2(b[2], b[3])};
    return __builtin_bit_cast(bf16x8, r);
}
DI bf16x4 pack4(f32x4 a) { u32x2 r = {pack2(a[0], a[1]), pack2(a[2], a[3])}; return __builtin_bit_cast(bf16x4, r); }

DI bool spin_fail(unsigned& spins, unsigned* tmo) {
    if (++spins > kSpinCap) { __hip_atomic_store(tmo, 1u, __ATOMIC_RELAXED, __HIP_MEMORY_SCOPE_AGENT); return true; }
    if ((spins & 63u) == 0u) return __builtin_amdgcn_readfirstlane(__hip_atomic_load(tmo, __ATOMIC_RELAXED, __HIP_MEMORY_SCOPE_AGENT)) != 0u;
    return false;
}

__global__ __launch_bounds__(256) void k_ln(const unsigned short* __restrict__ x, const unsigned short* __restrict__ mem0, char* __restrict__ ws) {
    const int wave = threadIdx.x >> 6, lane = threadIdx.x & 63;
    const int gw = blockIdx.x * 4 + wave;
    {
        const int row = gw;
        const unsigned short* xr = x + (size_t)row * DM;
        float v[2][4];
        float sum = 0.f;
#pragma unroll
        for (int it = 0; it < 2; ++it) {
            int grp = lane + it * 64;
            if (grp < 112) {
                u32x2 u = *(const u32x2*)(xr + grp * 4);
                v[it][0] = bf_lo(u.x); v[it][1] = bf_hi(u.x); v[it][2] = bf_lo(u.y); v[it][3] = bf_hi(u.y);
            } else { v[it][0] = v[it][1] = v[it][2] = v[it][3] = 0.f; }
            sum += v[it][0] + v[it][1] + v[it][2] + v[it][3];
        }
#pragma unroll
        for (int o = 32; o >= 1; o >>= 1) sum += __shfl_xor(sum, o);
        const float mean = sum * (1.f / DM);
        float sq = 0.f;
#pragma unroll
        for (int it = 0; it < 2; ++it) {
            int grp = lane + it * 64;
            if (grp < 112) {
#pragma unroll
                for (int j = 0; j < 4; ++j) { float d = v[it][j] - mean; sq += d * d; }
            }
        }
#pragma unroll
        for (int o = 32; o >= 1; o >>= 1) sq += __shfl_xor(sq, o);
        const float rstd = 1.f / sqrtf(sq * (1.f / DM) + 1e-5f);
        const int rt = row >> 4, i = row & 15;
#pragma unroll
        for (int it = 0; it < 2; ++it) {
            int grp = lane + it * 64;
            if (grp < 112) {
                int d = grp * 4, kk = d >> 5, dd = d & 31, half = dd >> 4, g = (dd & 15) >> 2;
                u32x2 o = {pack2((v[it][0] - mean) * rstd, (v[it][1] - mean) * rstd), pack2((v[it][2] - mean) * rstd, (v[it][3] - mean) * rstd)};
                *(u32x2*)(ws + OFF_XNF + ((size_t)(rt * KK + kk) * 64 + (i + 16 * g)) * 16 + half * 8) = o;
            }
        }
    }
    {
        const size_t nb = (size_t)(NSEG - 1) * 7168 + 2 * (size_t)NSEG * 8192 + (size_t)NSEG * 2048;
        const size_t n16 = nb / 16;
        u32x4 sv = {SENT, SENT, SENT, SENT};
        for (size_t idx = (size_t)blockIdx.x * 256 + threadIdx.x; idx < n16; idx += (size_t)gridDim.x * 256)
            *(u32x4*)(ws + OFF_MEMF + 7168 + idx * 16) = sv;
    }
    if (blockIdx.x == 0) {
        for (int e = threadIdx.x; e < 8 * 112; e += 256) {
            int r = e / 112, grp = e % 112;
            int d = grp * 4, kk = d >> 5, dd = d & 31, half = dd >> 4, g = (dd & 15) >> 2;
            u32x2 u = *(const u32x2*)(mem0 + r * DM + d);
            *(u32x2*)(ws + OFF_MEMF + kk * 512 + g * 128 + r * 16 + half * 8) = u;
        }
        if (threadIdx.x == 0) *(unsigned*)(ws + OFF_TMO) = 0;
    }
}

__global__ __launch_bounds__(256) void k_ab(const unsigned short* __restrict__ Wq, const unsigned short* __restrict__ Wk,
                                            const unsigned short* __restrict__ Wv, const unsigned short* __restrict__ Wo, char* __restrict__ ws) {
    const int wave = threadIdx.x >> 6, lane = threadIdx.x & 63;
    const int u = blockIdx.x * 4 + wave;
    const int type = u / 448, h = (u % 448) / 28, tau = u % 28;
    const int i = lane & 15, g = lane >> 4;
    bf16x8 fix[8];
    const unsigned short* loopW;
    if (type == 0) {
        const unsigned short* p = Wq + (size_t)(16 * tau + i) * 4096 + h * 256 + 8 * g;
#pragma unroll
        for (int ke = 0; ke < 8; ++ke) fix[ke] = *(const bf16x8*)(p + ke * 32);
        loopW = Wk;
    } else if (type == 1) {
        const unsigned short* p = Wk + (size_t)(16 * tau + i) * 4096 + h * 256 + 8 * g;
#pragma unroll
        for (int ke = 0; ke < 8; ++ke) fix[ke] = *(const bf16x8*)(p + ke * 32);
        loopW = Wq;
    } else {
#pragma unroll
        for (int ke = 0; ke < 8; ++ke)
#pragma unroll
            for (int j = 0; j < 8; ++j)
                fix[ke][j] = (short)Wo[((size_t)h * 256 + ke * 32 + 8 * g + j) * DM + 16 * tau + i];
        loopW = Wv;
    }
    char* dst = (type == 0) ? ws + OFF_WF + (size_t)(h * 28 + tau) * KK * KB
              : (type == 1) ? ws + OFF_ANF + (size_t)(h * 28 + tau) * KK * KB
                            : ws + OFF_WF + (size_t)(448 + h * 28 + tau) * KK * KB;
    for (int kk = 0; kk < KK; ++kk) {
        f32x4 acc[2] = {{0.f, 0.f, 0.f, 0.f}, {0.f, 0.f, 0.f, 0.f}};
#pragma unroll
        for (int tt = 0; tt < 2; ++tt) {
            const unsigned short* p = loopW + (size_t)(32 * kk + 16 * tt + i) * 4096 + h * 256 + 8 * g;
#pragma unroll
            for (int ke = 0; ke < 8; ++ke) acc[tt] = mfma32(*(const bf16x8*)(p + ke * 32), fix[ke], acc[tt]);
        }
        *(bf16x8*)(dst + (size_t)kk * KB + lane * 16) = pack8(acc[0], acc[1]);
    }
}

__global__ __launch_bounds__(256) void k_w12(const unsigned short* __restrict__ w1, const unsigned short* __restrict__ w2, char* __restrict__ ws) {
    const int wave = threadIdx.x >> 6, lane = threadIdx.x & 63;
    const int f = blockIdx.x * 4 + wave;
    const int i = lane & 15, g = lane >> 4;
    bf16x8 v;
    if (f < 112 * KK) {
        int tf = f / KK, kk = f % KK;
#pragma unroll
        for (int j = 0; j < 8; ++j) v[j] = (short)w1[(size_t)(32 * kk + 16 * (j >> 2) + 4 * g + (j & 3)) * FFD + 16 * tf + i];
        *(bf16x8*)(ws + OFF_WF + ((size_t)(896 + tf) * KK + kk) * KB + lane * 16) = v;
    } else {
        int f2 = f - 112 * KK;
        int t = f2 / KF, kf = f2 % KF;
#pragma unroll
        for (int j = 0; j < 8; ++j) v[j] = (short)w2[(size_t)(32 * kf + 16 * (j >> 2) + 4 * g + (j & 3)) * DM + 16 * t + i];
        *(bf16x8*)(ws + OFF_W2F + ((size_t)t * KF + kf) * KB + lane * 16) = v;
    }
}

DI float gelu_tanh(float z) {
    float u = 0.7978845608028654f * (z + 0.044715f * z * z * z);
    float e = __expf(2.f * u);
    float t = 1.f - 2.f / (1.f + e);
    return 0.5f * z * (1.f + t);
}

__global__ __launch_bounds__(256, 1) void k_work(const unsigned short* __restrict__ b1, char* __restrict__ ws) {
    const int wave = threadIdx.x >> 6, lane = threadIdx.x & 63;
    const int unit = blockIdx.x;
    const int tile0 = unit * 4;
    const int i = lane & 15, g = lane >> 4;
    bf16x8 w[4][KK];
    {
        const char* wp = ws + OFF_WF + (size_t)tile0 * KK * KB + lane * 16;
#pragma unroll
        for (int t = 0; t < 4; ++t)
#pragma unroll
            for (int kk = 0; kk < KK; ++kk) w[t][kk] = ldfrag(wp + (size_t)(t * KK + kk) * KB);
    }
    const int type = tile0 < 448 ? 0 : (tile0 < 896 ? 1 : 2);
    for (int s = wave; s < NSEG; s += 4) {
        f32x4 acc[2][4];
#pragma unroll
        for (int q = 0; q < 2; ++q)
#pragma unroll
            for (int t = 0; t < 4; ++t) acc[q][t] = (f32x4){0.f, 0.f, 0.f, 0.f};
        const char* xp = ws + OFF_XNF + (size_t)(2 * s) * KK * KB + lane * 16;
        if (type == 1) {
#pragma unroll
            for (int kk = 0; kk < KK; ++kk)
#pragma unroll
                for (int q = 0; q < 2; ++q) {
                    bf16x8 xf = ldfrag(xp + (size_t)(q * KK + kk) * KB);
#pragma unroll
                    for (int t = 0; t < 4; ++t) acc[q][t] = mfma32(xf, w[t][kk], acc[q][t]);
                }
            const int h = (tile0 - 448) / 28, t0 = (tile0 - 448) % 28;
#pragma unroll
            for (int t = 0; t < 4; ++t)
                *(bf16x8*)(ws + OFF_UF + (((size_t)s * NT16 + t0 + t) * NH + h) * KB + lane * 16) = pack8(acc[0][t], acc[1][t]);
        } else {
#pragma unroll
            for (int kk = 0; kk < KK; ++kk)
#pragma unroll
                for (int q = 0; q < 2; ++q) {
                    bf16x8 xf = ldfrag(xp + (size_t)(q * KK + kk) * KB);
#pragma unroll
                    for (int t = 0; t < 4; ++t) acc[q][t] = mfma32(w[t][kk], xf, acc[q][t]);
                }
            if (type == 0) {
                const int h = tile0 / 28, t0 = tile0 % 28;
#pragma unroll
                for (int q = 0; q < 2; ++q)
#pragma unroll
                    for (int p = 0; p < 2; ++p)
                        *(bf16x8*)(ws + OFF_GTF + (((size_t)h * NRT + 2 * s + q) * KK + (t0 >> 1) + p) * KB + lane * 16) = pack8(acc[q][2 * p], acc[q][2 * p + 1]);
            } else {
                const int tf0 = tile0 - 896;
#pragma unroll
                for (int t = 0; t < 4; ++t) {
                    u32x2 bb = *(const u32x2*)(b1 + 16 * (tf0 + t) + 4 * g);
                    float bv[4] = {bf_lo(bb.x), bf_hi(bb.x), bf_lo(bb.y), bf_hi(bb.y)};
#pragma unroll
                    for (int q = 0; q < 2; ++q)
#pragma unroll
                        for (int r = 0; r < 4; ++r) acc[q][t][r] = gelu_tanh(acc[q][t][r] + bv[r]);
                }
#pragma unroll
                for (int q = 0; q < 2; ++q)
#pragma unroll
                    for (int p = 0; p < 2; ++p)
                        *(bf16x8*)(ws + OFF_HF + ((size_t)(2 * s + q) * KF + (tf0 >> 1) + p) * KB + lane * 16) = pack8(acc[q][2 * p], acc[q][2 * p + 1]);
            }
        }
    }
}

constexpr int CH_LDS_B = 0;
constexpr int CH_LDS_MEM = 112 * 1024;
constexpr int CH_LDS_RA = CH_LDS_MEM + 7168;
constexpr int CH_LDS_RB = CH_LDS_RA + 8192;
constexpr int CH_LDS_PW = CH_LDS_RB + 4096;
constexpr int CH_LDS_SIZE = CH_LDS_PW + 4096;

DI float xmax16(float v) { auto r = __builtin_amdgcn_permlane16_swap(__float_as_uint(v), __float_as_uint(v), false, false); return fmaxf(__uint_as_float(r[0]), __uint_as_float(r[1])); }
DI float xmax32(float v) { auto r = __builtin_amdgcn_permlane32_swap(__float_as_uint(v), __float_as_uint(v), false, false); return fmaxf(__uint_as_float(r[0]), __uint_as_float(r[1])); }
DI float xadd16(float v) { auto r = __builtin_amdgcn_permlane16_swap(__float_as_uint(v), __float_as_uint(v), false, false); return __uint_as_float(r[0]) + __uint_as_float(r[1]); }
DI float xadd32(float v) { auto r = __builtin_amdgcn_permlane32_swap(__float_as_uint(v), __float_as_uint(v), false, false); return __uint_as_float(r[0]) + __uint_as_float(r[1]); }
DI float qswap(float v) { return __uint_as_float(__builtin_amdgcn_update_dpp(0, __float_as_uint(v), 0xB1, 0xF, 0xF, true)); }
DI unsigned umax4(u32x4 v) { return max(max(v.x, v.y), max(v.z, v.w)); }
DI float fexp2(float x) { return __builtin_amdgcn_exp2f(x); }

DI void mfma_Aa0(f32x4& acc, const bf16x8& w, const bf16x8& m) { asm("s_nop 1\n\tv_mfma_f32_16x16x32_bf16 %0, %1, %2, 0" : "=&v"(acc) : "a"(w), "v"(m)); }
DI void mfma_Aa(f32x4& acc, const bf16x8& w, const bf16x8& m) { asm("s_nop 1\n\tv_mfma_f32_16x16x32_bf16 %0, %1, %2, %0" : "+v"(acc) : "a"(w), "v"(m)); }
DI void mfma_Ba0(f32x4& acc, const bf16x8& m, const bf16x8& w) { asm("s_nop 1\n\tv_mfma_f32_16x16x32_bf16 %0, %1, %2, 0" : "=&v"(acc) : "v"(m), "a"(w)); }
DI void mfma_Ba(f32x4& acc, const bf16x8& m, const bf16x8& w) { asm("s_nop 1\n\tv_mfma_f32_16x16x32_bf16 %0, %1, %2, %0" : "+v"(acc) : "v"(m), "a"(w)); }
DI void mfma_v0(f32x4& acc, const bf16x8& a, const bf16x8& b) { asm("s_nop 1\n\tv_mfma_f32_16x16x32_bf16 %0, %1, %2, 0" : "=&v"(acc) : "v"(a), "v"(b)); }
DI void mfma_v(f32x4& acc, const bf16x8& a, const bf16x8& b) { asm("s_nop 1\n\tv_mfma_f32_16x16x32_bf16 %0, %1, %2, %0" : "+v"(acc) : "v"(a), "v"(b)); }
DI void mfma16_v0(f32x4& acc, const bf16x4& a, const bf16x4& b) { asm("s_nop 1\n\tv_mfma_f32_16x16x16_bf16 %0, %1, %2, 0" : "=&v"(acc) : "v"(a), "v"(b)); }
DI void mfma16_v(f32x4& acc, const bf16x4& a, const bf16x4& b) { asm("s_nop 1\n\tv_mfma_f32_16x16x16_bf16 %0, %1, %2, %0" : "+v"(acc) : "v"(a), "v"(b)); }
DI void mfma_fence4(f32x4& a, f32x4& b, f32x4& c, f32x4& d) { asm volatile("s_nop 7\n\ts_nop 1" : "+v"(a), "+v"(b), "+v"(c), "+v"(d)); }
DI void mfma_fence1(f32x4& a) { asm volatile("s_nop 7\n\ts_nop 1" : "+v"(a)); }

#define D1 3
#define D2 3

__global__ __launch_bounds__(256, 1) void k_chain(char* __restrict__ ws) {
    extern __shared__ __attribute__((aligned(16))) char lds[];
    if (blockIdx.x & 7) return;
    const int c = blockIdx.x >> 3;
    const int tid = threadIdx.x, wave = __builtin_amdgcn_readfirstlane(tid >> 6), lane = tid & 63;
    const int i = lane & 15, g = lane >> 4;
    const int h = c >> 1, half = c & 1;
    const bool hasB = c < NT16;
    const int tB = hasB ? c : 0;
    unsigned* tmo = (unsigned*)(ws + OFF_TMO);
    const __amdgpu_buffer_rsrc_t rs = __builtin_amdgcn_make_buffer_rsrc(ws + OFF_MEMF, 0, (int)(OFF_T2F - OFF_MEMF), 0x00020000);
    constexpr unsigned R_SMX = (unsigned)(OFF_SMX - OFF_MEMF), R_SMM = (unsigned)(OFF_SMM - OFF_MEMF), R_SST = (unsigned)(OFF_SST - OFF_MEMF);
    const float cs = 0.0625f * 1.4426950408889634f;

    bf16x8 wa[3][KK], wsh[7], wb[2][KK];
    {
        const char* ap = ws + OFF_ANF + (size_t)(h * 28 + 14 * half) * KK * KB + lane * 16;
#pragma unroll
        for (int t = 0; t < 3; ++t)
#pragma unroll
            for (int kk = 0; kk < KK; ++kk) wa[t][kk] = ldfrag(ap + (size_t)((3 * wave + t) * KK + kk) * KB);
#pragma unroll
        for (int k = 0; k < 7; ++k) wsh[k] = ldfrag(ap + (size_t)((12 + (wave >> 1)) * KK + 7 * (wave & 1) + k) * KB);
        const char* bp = ws + OFF_WF + (size_t)(448 + tB) * KK * KB + lane * 16;
#pragma unroll
        for (int hh = 0; hh < 2; ++hh)
#pragma unroll
            for (int kk = 0; kk < KK; ++kk) wb[hh][kk] = ldfrag(bp + (size_t)((4 * wave + hh) * 28 * KK + kk) * KB);
#pragma unroll
        for (int hh = 0; hh < 2; ++hh)
#pragma unroll
            for (int kk = 0; kk < KK; ++kk)
                *(bf16x8*)(lds + CH_LDS_B + (size_t)((wave * 2 + hh) * KK + kk) * KB + lane * 16) = ldfrag(bp + (size_t)((4 * wave + 2 + hh) * 28 * KK + kk) * KB);
    }
    const int gk0 = wave < 2 ? 4 * wave : 8 + 3 * (wave - 2);
    const int gkn = wave < 2 ? 4 : 3;
    bf16x8 gf[4], uf[4];
    {
        const char* gp = ws + OFF_GTF + (((size_t)h * NRT + half) * KK + gk0) * KB + lane * 16;
#pragma unroll
        for (int k = 0; k < 4; ++k) gf[k] = ldfrag(gp + (size_t)(k < gkn ? k : 0) * KB);
    }
    __syncthreads();
    const char* mp = lds + CH_LDS_MEM + g * 128 + (i & 7) * 16;
    const int hh2 = lane >> 4, r2 = (lane >> 1) & 7, q2 = lane & 1;

    for (int s = 0; s < NSEG - 1; ++s) {
        STAMP(0);
        {
            u32x4 x0, x1;
            unsigned spins = 0;
            const unsigned o0 = (unsigned)(s * 7168 + tid * 16), o1 = o0 + (tid < 192 ? 4096 : 0);
            for (int d = 0; d < D1; ++d) __builtin_amdgcn_s_sleep(1);
            for (;;) {
                x0 = __builtin_amdgcn_raw_buffer_load_b128(rs, o0, 0, 16);
                x1 = __builtin_amdgcn_raw_buffer_load_b128(rs, o1, 0, 16);
                bool ok = max(umax4(x0), umax4(x1)) != SENT;
                if (__all(ok)) break;
                if (spin_fail(spins, tmo)) break;
            }
            *(u32x4*)(lds + CH_LDS_MEM + tid * 16) = x0;
            if (tid < 192) *(u32x4*)(lds + CH_LDS_MEM + 4096 + tid * 16) = x1;
        }
        {
            const char* up = ws + OFF_UF + (((size_t)s * NT16 + tB) * NH + 4 * wave) * KB + lane * 16;
#pragma unroll
            for (int k = 0; k < 4; ++k) uf[k] = ldfrag(up + (size_t)k * KB);
        }
        STAMP(1);
        __syncthreads();
        STAMP(2);
        bf16x8 mf[KK];
#pragma unroll
        for (int kk = 0; kk < KK; ++kk) mf[kk] = *(const bf16x8*)(mp + kk * 512);
        {
            bf16x8 mg[4];
            bf16x4 mk[4];
#pragma unroll
            for (int k = 0; k < 4; ++k) mg[k] = *(const bf16x8*)(mp + (gk0 + (k < gkn ? k : 0)) * 512);
#pragma unroll
            for (int t = 0; t < 4; ++t) {
                const int tl = (t < 3) ? 3 * wave + t : 12 + (wave >> 1);
                const int tg = 14 * half + tl;
                mk[t] = *(const bf16x4*)(mp + (tg >> 1) * 512 + (tg & 1) * 8);
            }
            f32x4 ta[4];
            f32x4 sx;
            mfma_v0(sx, gf[0], mg[0]);
            mfma_v(sx, gf[1], mg[1]);
            mfma_v(sx, gf[2], mg[2]);
            if (wave < 2) mfma_v(sx, gf[3], mg[3]);
            if (wave & 1) {
                mfma_Aa0(ta[3], wsh[0], mf[7]);
#pragma unroll
                for (int k = 1; k < 7; ++k) mfma_Aa(ta[3], wsh[k], mf[7 + k]);
            } else {
                mfma_Aa0(ta[3], wsh[0], mf[0]);
#pragma unroll
                for (int k = 1; k < 7; ++k) mfma_Aa(ta[3], wsh[k], mf[k]);
            }
#pragma unroll
            for (int t = 0; t < 3; ++t) mfma_Aa0(ta[t], wa[t][0], mf[0]);
#pragma unroll
            for (int kk = 1; kk < KK; ++kk)
#pragma unroll
                for (int t = 0; t < 3; ++t) mfma_Aa(ta[t], wa[t][kk], mf[kk]);
            mfma_fence4(ta[0], ta[1], ta[2], ta[3]);
            f32x4 sm;
            mfma16_v0(sm, mk[0], pack4(ta[0]));
#pragma unroll
            for (int t = 1; t < 4; ++t) mfma16_v(sm, mk[t], pack4(ta[t]));
            mfma_fence4(sm, sx, ta[0], ta[1]);
            *(f32x4*)(lds + CH_LDS_RA + (wave * 2 + 0) * 1024 + lane * 16) = sx;
            *(f32x4*)(lds + CH_LDS_RA + (wave * 2 + 1) * 1024 + lane * 16) = sm;
        }
        STAMP(3);
        __syncthreads();
        if (wave < 2) {
            f32x4 v0 = *(const f32x4*)(lds + CH_LDS_RA + (0 + wave) * 1024 + lane * 16);
            f32x4 v1 = *(const f32x4*)(lds + CH_LDS_RA + (2 + wave) * 1024 + lane * 16);
            f32x4 v2 = *(const f32x4*)(lds + CH_LDS_RA + (4 + wave) * 1024 + lane * 16);
            f32x4 v3 = *(const f32x4*)(lds + CH_LDS_RA + (6 + wave) * 1024 + lane * 16);
            f32x4 v = (v0 + v1) + (v2 + v3);
            if (wave == 0) {
                float m = fmaxf(fmaxf(v[0], v[1]), fmaxf(v[2], v[3]));
                m = xmax16(m); m = xmax32(m);
                const float mc = m * cs;
                float e0 = fexp2(v[0] * cs - mc), e1 = fexp2(v[1] * cs - mc), e2 = fexp2(v[2] * cs - mc), e3 = fexp2(v[3] * cs - mc);
                u32x2 pk = {pack2(e0, e1), pack2(e2, e3)};
                if (i < 8) __builtin_amdgcn_raw_buffer_store_b64(pk, rs, R_SMX + (unsigned)(s * 8192 + ((h * 8 + i) * 4 + g) * 16 + half * 8), 0, 16);
                float sum = (bf_lo(pk.x) + bf_hi(pk.x)) + (bf_lo(pk.y) + bf_hi(pk.y));
                sum = xadd16(sum); sum = xadd32(sum);
                u32x2 st = {__float_as_uint(m), __float_as_uint(sum)};
                if (lane < 8) __builtin_amdgcn_raw_buffer_store_b64(st, rs, R_SST + (unsigned)(s * 2048 + (h * 8 + i) * 16 + half * 8), 0, 16);
            } else {
                if (i < 8 && g < 2)
                    __builtin_amdgcn_raw_buffer_store_b128(__builtin_bit_cast(u32x4, v), rs, R_SMM + (unsigned)(s * 8192 + ((h * 2 + half) * 8 + i) * 32 + g * 16), 0, 16);
            }
        }
        {
            const char* gp = ws + OFF_GTF + (((size_t)h * NRT + 2 * (s + 1) + half) * KK + gk0) * KB + lane * 16;
#pragma unroll
            for (int k = 0; k < 4; ++k) gf[k] = ldfrag(gp + (size_t)(k < gkn ? k : 0) * KB);
        }
        if (hasB) {
            f32x4 ya[4];
            const char* lb = lds + CH_LDS_B + (size_t)(wave * 2) * KK * KB + lane * 16;
            bf16x8 l0[KK], l1[KK];
#pragma unroll
            for (int k = 0; k < KK; ++k) { l0[k] = *(const bf16x8*)(lb + (size_t)k * KB); l1[k] = *(const bf16x8*)(lb + (size_t)(KK + k) * KB); }
            mfma_Ba0(ya[0], mf[0], wb[0][0]);
            mfma_v0(ya[1], mf[0], wb[1][0]);
#pragma unroll
            for (int k = 1; k < KK; ++k) {
                mfma_Ba(ya[0], mf[k], wb[0][k]);
                mfma_v(ya[1], mf[k], wb[1][k]);
            }
            mfma_v0(ya[2], mf[0], l0[0]);
            mfma_v0(ya[3], mf[0], l1[0]);
#pragma unroll
            for (int k = 1; k < KK; ++k) {
                mfma_v(ya[2], mf[k], l0[k]);
                mfma_v(ya[3], mf[k], l1[k]);
            }
            mfma_fence4(ya[0], ya[1], ya[2], ya[3]);
            bf16x8 yp0 = pack8(ya[0], ya[1]), yp1 = pack8(ya[2], ya[3]);
            STAMP(4);
            u32x4 qx[4], qm0, qm1, qs;
            {
                unsigned spins = 0;
                const unsigned ox = R_SMX + (unsigned)(s * 8192 + ((4 * wave * 8 + (i & 7)) * 4 + g) * 16);
                const unsigned om = R_SMM + (unsigned)(s * 8192 + (((4 * wave + hh2) * 2) * 8 + r2) * 32 + q2 * 16);
                const unsigned os = R_SST + (unsigned)(s * 2048 + ((4 * wave + hh2) * 8 + r2) * 16);
                for (int d = 0; d < D2; ++d) __builtin_amdgcn_s_sleep(1);
                for (;;) {
#pragma unroll
                    for (int hh = 0; hh < 4; ++hh) qx[hh] = __builtin_amdgcn_raw_buffer_load_b128(rs, ox + hh * 512, 0, 16);
                    qm0 = __builtin_amdgcn_raw_buffer_load_b128(rs, om, 0, 16);
                    qm1 = __builtin_amdgcn_raw_buffer_load_b128(rs, om + 256, 0, 16);
                    qs = __builtin_amdgcn_raw_buffer_load_b128(rs, os, 0, 16);
                    unsigned mx = max(max(max(umax4(qx[0]), umax4(qx[1])), max(umax4(qx[2]), umax4(qx[3]))), max(max(umax4(qm0), umax4(qm1)), umax4(qs)));
                    if (__all(mx != SENT)) break;
                    if (spin_fail(spins, tmo)) break;
                }
            }
            STAMP(5);
            f32x4 dd[8];
#pragma unroll
            for (int hh = 0; hh < 4; ++hh) {
                const u32x4 u = __builtin_bit_cast(u32x4, uf[hh]);
                u32x2 ulo = {u.x, u.y}, uhi = {u.z, u.w}, elo = {qx[hh].x, qx[hh].y}, ehi = {qx[hh].z, qx[hh].w};
                mfma16_v0(dd[2 * hh], __builtin_bit_cast(bf16x4, ulo), __builtin_bit_cast(bf16x4, elo));
                mfma16_v0(dd[2 * hh + 1], __builtin_bit_cast(bf16x4, uhi), __builtin_bit_cast(bf16x4, ehi));
            }
            f32x4 ms = __builtin_bit_cast(f32x4, qm0) + __builtin_bit_cast(f32x4, qm1);
            const float m0 = __uint_as_float(qs.x), s0 = __uint_as_float(qs.y), m1 = __uint_as_float(qs.z), s1 = __uint_as_float(qs.w);
            float mx = fmaxf(fmaxf(ms[0], ms[1]), fmaxf(ms[2], ms[3]));
            mx = fmaxf(mx, qswap(mx));
            const float Mc = fmaxf(mx, fmaxf(m0, m1)) * cs;
            float e0 = fexp2(ms[0] * cs - Mc), e1 = fexp2(ms[1] * cs - Mc), e2 = fexp2(ms[2] * cs - Mc), e3 = fexp2(ms[3] * cs - Mc);
            float f0 = fexp2(m0 * cs - Mc), f1 = fexp2(m1 * cs - Mc);
            float sm = (e0 + e1) + (e2 + e3);
            sm += qswap(sm);
            const float inv = __builtin_amdgcn_rcpf(sm + f0 * s0 + f1 * s1);
            char* pw = lds + CH_LDS_PW + wave * 1024;
            *(u32x2*)(pw + ((hh2 * 8 + r2) * 2 + q2) * 8) = (u32x2){pack2(e0 * inv, e1 * inv), pack2(e2 * inv, e3 * inv)};
            if (q2 == 0) *(u32x2*)(pw + 512 + (r2 * 4 + hh2) * 8) = (u32x2){__float_as_uint(f0 * inv), __float_as_uint(f1 * inv)};
            __builtin_amdgcn_fence(__ATOMIC_RELEASE, "wavefront");
            __builtin_amdgcn_wave_barrier();
            __builtin_amdgcn_fence(__ATOMIC_ACQUIRE, "wavefront");
            u32x2 pa[4];
#pragma unroll
            for (int hh = 0; hh < 4; ++hh) pa[hh] = *(const u32x2*)(pw + ((hh * 8 + (i & 7)) * 2 + (g & 1)) * 8);
            f32x4 fa = *(const f32x4*)(pw + 512 + (i & 7) * 32), fb = *(const f32x4*)(pw + 512 + (i & 7) * 32 + 16);
            if (g >= 2) {
#pragma unroll
                for (int hh = 0; hh < 4; ++hh) pa[hh] = (u32x2){0u, 0u};
            }
            mfma_fence4(dd[0], dd[1], dd[2], dd[3]);
            mfma_fence4(dd[4], dd[5], dd[6], dd[7]);
            f32x4 acc = dd[0] * fa[0] + dd[1] * fa[1] + dd[2] * fa[2] + dd[3] * fa[3] + dd[4] * fb[0] + dd[5] * fb[1] + dd[6] * fb[2] + dd[7] * fb[3];
            u32x4 p01 = {pa[0].x, pa[0].y, pa[1].x, pa[1].y}, p23 = {pa[2].x, pa[2].y, pa[3].x, pa[3].y};
            mfma_v(acc, yp0, __builtin_bit_cast(bf16x8, p01));
            mfma_v(acc, yp1, __builtin_bit_cast(bf16x8, p23));
            mfma_fence1(acc);
            *(f32x4*)(lds + CH_LDS_RB + wave * 1024 + lane * 16) = acc;
        }
        STAMP(6);
        __syncthreads();
        if (hasB) {
            if (wave == 0) {
                f32x4 v0 = *(const f32x4*)(lds + CH_LDS_RB + lane * 16);
                f32x4 v1 = *(const f32x4*)(lds + CH_LDS_RB + 1024 + lane * 16);
                f32x4 v2 = *(const f32x4*)(lds + CH_LDS_RB + 2048 + lane * 16);
                f32x4 v3 = *(const f32x4*)(lds + CH_LDS_RB + 3072 + lane * 16);
                f32x4 v = (v0 + v1) + (v2 + v3);
                if (i < 8) {
                    u32x2 pk = {pack2(v[0], v[1]), pack2(v[2], v[3])};
                    __builtin_amdgcn_raw_buffer_store_b64(pk, rs, (unsigned)((s + 1) * 7168 + (tB >> 1) * 512 + g * 128 + i * 16 + (tB & 1) * 8), 0, 16);
                }
            }
        }
    }
}

__global__ __launch_bounds__(256, 1) void k_memrows(char* __restrict__ ws) {
    const int wave = threadIdx.x >> 6, lane = threadIdx.x & 63;
    const int unit = blockIdx.x;
    const int tile0 = unit * 4;
    const int i = lane & 15, g = lane >> 4;
    bf16x8 w[4][KK];
    {
        const char* wp = ws + OFF_WF + (size_t)tile0 * KK * KB + lane * 16;
#pragma unroll
        for (int t = 0; t < 4; ++t)
#pragma unroll
            for (int kk = 0; kk < KK; ++kk) w[t][kk] = ldfrag(wp + (size_t)(t * KK + kk) * KB);
    }
    const int type = tile0 < 448 ? 0 : 1;
    for (int sp = wave; sp < NSEG / 2; sp += 4) {
        f32x4 acc[4];
#pragma unroll
        for (int t = 0; t < 4; ++t) acc[t] = (f32x4){0.f, 0.f, 0.f, 0.f};
        const char* mp = ws + OFF_MEMF + (size_t)(2 * sp + (i >> 3)) * 7168 + g * 128 + (i & 7) * 16;
        if (type == 0) {
#pragma unroll
            for (int kk = 0; kk < KK; ++kk) {
                bf16x8 xf = ldfrag(mp + kk * 512);
#pragma unroll
                for (int t = 0; t < 4; ++t) acc[t] = mfma32(w[t][kk], xf, acc[t]);
            }
            const int h = tile0 / 28, t0 = tile0 % 28;
#pragma unroll
            for (int p = 0; p < 2; ++p)
                *(bf16x8*)(ws + OFF_T2F + ((size_t)h * NSEG + 2 * sp + (i >> 3)) * 7168 + ((t0 >> 1) + p) * 512 + g * 128 + (i & 7) * 16) = pack8(acc[2 * p], acc[2 * p + 1]);
        } else {
#pragma unroll
            for (int kk = 0; kk < KK; ++kk) {
                bf16x8 xf = ldfrag(mp + kk * 512);
#pragma unroll
                for (int t = 0; t < 4; ++t) acc[t] = mfma32(xf, w[t][kk], acc[t]);
            }
            const int h = (tile0 - 448) / 28, t0 = (tile0 - 448) % 28;
#pragma unroll
            for (int t = 0; t < 4; ++t)
                *(bf16x4*)(ws + OFF_YF + (((size_t)(2 * sp + (g >> 1)) * NT16 + t0 + t) * 8 + (h >> 1)) * 512 + (g & 1) * 256 + i * 16 + (h & 1) * 8) = pack4(acc[t]);
        }
    }
}

__global__ __launch_bounds__(256) void k_f1(char* __restrict__ ws) {
    const int wave = threadIdx.x >> 6, lane = threadIdx.x & 63;
    const int u = blockIdx.x * 4 + wave;
    const int s = u >> 4, h = u & 15;
    const int i = lane & 15, g = lane >> 4;
    f32x4 sc[2][3];
#pragma unroll
    for (int q = 0; q < 2; ++q)
#pragma unroll
        for (int k = 0; k < 3; ++k) sc[q][k] = (f32x4){0.f, 0.f, 0.f, 0.f};
    const char* xp = ws + OFF_XNF + (size_t)(2 * s) * KK * KB + lane * 16;
    const char* gp = ws + OFF_GTF + ((size_t)h * NRT + 2 * s) * KK * KB + lane * 16;
    const char* tp = ws + OFF_T2F + ((size_t)h * NSEG + s) * 7168 + g * 128 + (i & 7) * 16;
#pragma unroll
    for (int kk = 0; kk < KK; ++kk) {
        bf16x8 g0 = ldfrag(gp + (size_t)kk * KB), g1 = ldfrag(gp + (size_t)(KK + kk) * KB), t2 = ldfrag(tp + kk * 512);
#pragma unroll
        for (int q = 0; q < 2; ++q) {
            bf16x8 xf = ldfrag(xp + (size_t)(q * KK + kk) * KB);
            sc[q][0] = mfma32(g0, xf, sc[q][0]);
            sc[q][1] = mfma32(g1, xf, sc[q][1]);
            sc[q][2] = mfma32(t2, xf, sc[q][2]);
        }
    }
    const float cs = 0.0625f * 1.4426950408889634f;
    const float NEG = -3.0e38f;
#pragma unroll
    for (int q = 0; q < 2; ++q) {
        const int qi = 16 * q + i;
        float xs[8], ms[4];
#pragma unroll
        for (int j = 0; j < 8; ++j) {
            const int key = 16 * (j >> 2) + 4 * g + (j & 3);
            xs[j] = (key <= qi) ? sc[q][j >> 2][j & 3] : NEG;
        }
#pragma unroll
        for (int j = 0; j < 4; ++j) ms[j] = (g < 2) ? sc[q][2][j] : NEG;
        float m = fmaxf(fmaxf(fmaxf(xs[0], xs[1]), fmaxf(xs[2], xs[3])), fmaxf(fmaxf(xs[4], xs[5]), fmaxf(xs[6], xs[7])));
        m = fmaxf(m, fmaxf(fmaxf(ms[0], ms[1]), fmaxf(ms[2], ms[3])));
        m = fmaxf(m, __shfl_xor(m, 16));
        m = fmaxf(m, __shfl_xor(m, 32));
        float sum = 0.f;
#pragma unroll
        for (int j = 0; j < 8; ++j) { const int key = 16 * (j >> 2) + 4 * g + (j & 3); xs[j] = (key <= qi) ? exp2f((xs[j] - m) * cs) : 0.f; sum += xs[j]; }
#pragma unroll
        for (int j = 0; j < 4; ++j) { ms[j] = (g < 2) ? exp2f((ms[j] - m) * cs) : 0.f; sum += ms[j]; }
        sum += __shfl_xor(sum, 16);
        sum += __shfl_xor(sum, 32);
        const float inv = 1.f / sum;
        u32x4 px = {pack2(xs[0] * inv, xs[1] * inv), pack2(xs[2] * inv, xs[3] * inv), pack2(xs[4] * inv, xs[5] * inv), pack2(xs[6] * inv, xs[7] * inv)};
        *(u32x4*)(ws + OFF_PXF + (((size_t)s * 2 + q) * NH + h) * KB + lane * 16) = px;
        u32x2 pmv = {pack2(ms[0] * inv, ms[1] * inv), pack2(ms[2] * inv, ms[3] * inv)};
        *(u32x2*)(ws + OFF_PMF + (((size_t)s * 2 + q) * 8 + (h >> 1)) * KB + lane * 16 + (h & 1) * 8) = pmv;
    }
}

__global__ __launch_bounds__(256) void k_f2(const unsigned short* __restrict__ x, const unsigned short* __restrict__ b2, const char* __restrict__ ws, unsigned short* __restrict__ out) {
    const int wave = threadIdx.x >> 6, lane = threadIdx.x & 63;
    const int s = blockIdx.x / 7, t = (blockIdx.x % 7) * 4 + wave;
    const int i = lane & 15, g = lane >> 4;
    f32x4 acc[2] = {{0.f, 0.f, 0.f, 0.f}, {0.f, 0.f, 0.f, 0.f}};
    {
        const char* up = ws + OFF_UF + ((size_t)s * NT16 + t) * NH * KB + lane * 16;
        const char* pp = ws + OFF_PXF + (size_t)s * 2 * NH * KB + lane * 16;
#pragma unroll 4
        for (int h = 0; h < NH; ++h) {
            bf16x8 a = ldfrag(up + (size_t)h * KB);
            acc[0] = mfma32(a, ldfrag(pp + (size_t)h * KB), acc[0]);
            acc[1] = mfma32(a, ldfrag(pp + (size_t)(NH + h) * KB), acc[1]);
        }
    }
    {
        const char* yp = ws + OFF_YF + ((size_t)s * NT16 + t) * 8 * 512 + (g & 1) * 256 + i * 16;
        const char* pp = ws + OFF_PMF + (size_t)s * 2 * 8 * KB + lane * 16;
#pragma unroll 4
        for (int hp = 0; hp < 8; ++hp) {
            bf16x8 a = ldfrag(yp + hp * 512);
            acc[0] = mfma32(a, ldfrag(pp + (size_t)hp * KB), acc[0]);
            acc[1] = mfma32(a, ldfrag(pp + (size_t)(8 + hp) * KB), acc[1]);
        }
    }
    {
        const char* wp = ws + OFF_W2F + (size_t)t * KF * KB + lane * 16;
        const char* hp = ws + OFF_HF + (size_t)(2 * s) * KF * KB + lane * 16;
#pragma unroll 4
        for (int kf = 0; kf < KF; ++kf) {
            bf16x8 a = ldfrag(wp + (size_t)kf * KB);
            acc[0] = mfma32(a, ldfrag(hp + (size_t)kf * KB), acc[0]);
            acc[1] = mfma32(a, ldfrag(hp + (size_t)(KF + kf) * KB), acc[1]);
        }
    }
    u32x2 bb = *(const u32x2*)(b2 + 16 * t + 4 * g);
    const float bv[4] = {bf_lo(bb.x), bf_hi(bb.x), bf_lo(bb.y), bf_hi(bb.y)};
#pragma unroll
    for (int q = 0; q < 2; ++q) {
        const size_t o = (size_t)(32 * s + 16 * q + i) * DM + 16 * t + 4 * g;
        u32x2 xv = *(const u32x2*)(x + o);
        u32x2 r = {pack2(bf_lo(xv.x) + acc[q][0] + bv[0], bf_hi(xv.x) + acc[q][1] + bv[1]), pack2(bf_lo(xv.y) + acc[q][2] + bv[2], bf_hi(xv.y) + acc[q][3] + bv[3])};
        *(u32x2*)(out + o) = r;
    }
}

extern "C" void kernel_launch(void* const* d_in, const int* in_sizes, int n_in,
                              void* d_out, int out_size, void* d_ws, size_t ws_size,
                              hipStream_t stream) {
    const unsigned short* x = (const unsigned short*)d_in[0];
    const unsigned short* mem0 = (const unsigned short*)d_in[1];
    const unsigned short* Wq = (const unsigned short*)d_in[2];
    const unsigned short* Wk = (const unsigned short*)d_in[3];
    const unsigned short* Wv = (const unsigned short*)d_in[4];
    const unsigned short* Wo = (const unsigned short*)d_in[5];
    const unsigned short* w1 = (const unsigned short*)d_in[8];
    const unsigned short* b1 = (const unsigned short*)d_in[9];
    const unsigned short* w2 = (const unsigned short*)d_in[10];
    const unsigned short* b2 = (const unsigned short*)d_in[11];
    char* ws = (char*)d_ws;
    unsigned short* out = (unsigned short*)d_out;
    hipFuncSetAttribute((const void*)k_chain, hipFuncAttributeMaxDynamicSharedMemorySize, CH_LDS_SIZE);
    k_ln<<<512, 256, 0, stream>>>(x, mem0, ws);
    k_ab<<<336, 256, 0, stream>>>(Wq, Wk, Wv, Wo, ws);
    k_w12<<<784, 256, 0, stream>>>(w1, w2, ws);
    k_work<<<252, 256, 0, stream>>>(b1, ws);
    k_chain<<<256, 256, CH_LDS_SIZE, stream>>>(ws);
    k_memrows<<<224, 256, 0, stream>>>(ws);
    k_f1<<<256, 256, 0, stream>>>(ws);
    k_f2<<<448, 256, 0, stream>>>(x, b2, ws, out);
}
```

```cpp
#include <hip/hip_runtime.h>
#include <hip/hip_bf16.h>

typedef __attribute__((ext_vector_type(8))) short bf16x8;
typedef __attribute__((ext_vector_type(4))) short bf16x4;
typedef __attribute__((ext_vector_type(4))) float f32x4;
typedef __attribute__((ext_vector_type(4))) unsigned u32x4;
typedef __attribute__((ext_vector_type(2))) unsigned u32x2;
typedef __attribute__((ext_vector_type(2))) __bf16 bf2_t;
#define DI __device__ __forceinline__

constexpr int DM = 448, KK = 14, NT16 = 28, NH = 16, NSEG = 64, NRT = 128, FFD = 1792, KF = 56;
constexpr size_t KB = 1024;
constexpr size_t OFF_XNF = 0;
constexpr size_t OFF_WF = OFF_XNF + (size_t)NRT * KK * KB;
constexpr size_t OFF_ANF = OFF_WF + (size_t)1008 * KK * KB;
constexpr size_t OFF_W2F = OFF_ANF + (size_t)448 * KK * KB;
constexpr size_t OFF_GTF = OFF_W2F + (size_t)28 * KF * KB;
constexpr size_t OFF_UF = OFF_GTF + (size_t)NH * NRT * KK * KB;
constexpr size_t OFF_HF = OFF_UF + (size_t)NSEG * NT16 * NH * KB;
constexpr size_t OFF_MEMF = OFF_HF + (size_t)NRT * KF * KB;
constexpr size_t OFF_SMX = OFF_MEMF + (size_t)NSEG * 7168;
constexpr int E2B = 640, E2S = 32 * E2B;
constexpr size_t OFF_T2F = OFF_SMX + (size_t)NSEG * E2S;
constexpr size_t OFF_YF = OFF_T2F + (size_t)NH * NSEG * 7168;
constexpr size_t OFF_PXF = OFF_YF + (size_t)NSEG * NT16 * 8 * 512;
constexpr size_t OFF_PMF = OFF_PXF + (size_t)NSEG * 2 * NH * KB;
constexpr size_t OFF_TMO = OFF_PMF + (size_t)NSEG * 2 * 8 * KB;
constexpr size_t WS_END = OFF_TMO + 4096;
constexpr size_t OFF_DBG = WS_END;
#define STAMP(k)

constexpr unsigned SENT = 0xFFFFFFFFu;
constexpr unsigned kSpinCap = 1u << 16;

DI unsigned pack2(float a, float b) { bf2_t v = {(__bf16)a, (__bf16)b}; return __builtin_bit_cast(unsigned, v); }
DI float bf_lo(unsigned u) { return __uint_as_float(u << 16); }
DI float bf_hi(unsigned u) { return __uint_as_float(u & 0xFFFF0000u); }
DI float bf2f(unsigned short v) { return __uint_as_float((unsigned)v << 16); }
DI f32x4 mfma32(bf16x8 a, bf16x8 b, f32x4 c) { return __builtin_amdgcn_mfma_f32_16x16x32_bf16(a, b, c, 0, 0, 0); }
DI f32x4 mfma16(bf16x4 a, bf16x4 b, f32x4 c) { return __builtin_amdgcn_mfma_f32_16x16x16bf16_1k(a, b, c, 0, 0, 0); }
DI bf16x8 ldfrag(const char* p) { return *(const bf16x8*)p; }
DI bf16x8 pack8(f32x4 a, f32x4 b) {
    u32x4 r = {pack2(a[0], a[1]), pack2(a[2], a[3]), pack2(b[0], b[1]), pack2(b[2], b[3])};
    return __builtin_bit_cast(bf16x8, r);
}
DI bf16x4 pack4(f32x4 a) { u32x2 r = {pack2(a[0], a[1]), pack2(a[2], a[3])}; return __builtin_bit_cast(bf16x4, r); }

DI bool spin_fail(unsigned& spins, unsigned* tmo) {
    if (++spins > kSpinCap) { __hip_atomic_store(tmo, 1u, __ATOMIC_RELAXED, __HIP_MEMORY_SCOPE_AGENT); return true; }
    if ((spins & 63u) == 0u) return __builtin_amdgcn_readfirstlane(__hip_atomic_load(tmo, __ATOMIC_RELAXED, __HIP_MEMORY_SCOPE_AGENT)) != 0u;
    return false;
}

__global__ __launch_bounds__(256) void k_ln(const unsigned short* __restrict__ x, const unsigned short* __restrict__ mem0, char* __restrict__ ws) {
    const int wave = threadIdx.x >> 6, lane = threadIdx.x & 63;
    const int gw = blockIdx.x * 4 + wave;
    {
        const int row = gw;
        const unsigned short* xr = x + (size_t)row * DM;
        float v[2][4];
        float sum = 0.f;
#pragma unroll
        for (int it = 0; it < 2; ++it) {
            int grp = lane + it * 64;
            if (grp < 112) {
                u32x2 u = *(const u32x2*)(xr + grp * 4);
                v[it][0] = bf_lo(u.x); v[it][1] = bf_hi(u.x); v[it][2] = bf_lo(u.y); v[it][3] = bf_hi(u.y);
            } else { v[it][0] = v[it][1] = v[it][2] = v[it][3] = 0.f; }
            sum += v[it][0] + v[it][1] + v[it][2] + v[it][3];
        }
#pragma unroll
        for (int o = 32; o >= 1; o >>= 1) sum += __shfl_xor(sum, o);
        const float mean = sum * (1.f / DM);
        float sq = 0.f;
#pragma unroll
        for (int it = 0; it < 2; ++it) {
            int grp = lane + it * 64;
            if (grp < 112) {
#pragma unroll
                for (int j = 0; j < 4; ++j) { float d = v[it][j] - mean; sq += d * d; }
            }
        }
#pragma unroll
        for (int o = 32; o >= 1; o >>= 1) sq += __shfl_xor(sq, o);
        const float rstd = 1.f / sqrtf(sq * (1.f / DM) + 1e-5f);
        const int rt = row >> 4, i = row & 15;
#pragma unroll
        for (int it = 0; it < 2; ++it) {
            int grp = lane + it * 64;
            if (grp < 112) {
                int d = grp * 4, kk = d >> 5, dd = d & 31, half = dd >> 4, g = (dd & 15) >> 2;
                u32x2 o = {pack2((v[it][0] - mean) * rstd, (v[it][1] - mean) * rstd), pack2((v[it][2] - mean) * rstd, (v[it][3] - mean) * rstd)};
                *(u32x2*)(ws + OFF_XNF + ((size_t)(rt * KK + kk) * 64 + (i + 16 * g)) * 16 + half * 8) = o;
            }
        }
    }
    {
        const size_t nb = (size_t)(NSEG - 1) * 7168 + (size_t)NSEG * E2S;
        const size_t n16 = nb / 16;
        u32x4 sv = {SENT, SENT, SENT, SENT};
        for (size_t idx = (size_t)blockIdx.x * 256 + threadIdx.x; idx < n16; idx += (size_t)gridDim.x * 256)
            *(u32x4*)(ws + OFF_MEMF + 7168 + idx * 16) = sv;
    }
    if (blockIdx.x == 0) {
        for (int e = threadIdx.x; e < 8 * 112; e += 256) {
            int r = e / 112, grp = e % 112;
            int d = grp * 4, t = d >> 4, g = (d & 15) >> 2;
            u32x2 u = *(const u32x2*)(mem0 + r * DM + d);
            *(u32x2*)(ws + OFF_MEMF + t * 256 + r * 32 + g * 8) = u;
        }
        if (threadIdx.x == 0) *(unsigned*)(ws + OFF_TMO) = 0;
    }
}

__global__ __launch_bounds__(256) void k_ab(const unsigned short* __restrict__ Wq, const unsigned short* __restrict__ Wk,
                                            const unsigned short* __restrict__ Wv, const unsigned short* __restrict__ Wo, char* __restrict__ ws) {
    const int wave = threadIdx.x >> 6, lane = threadIdx.x & 63;
    const int u = blockIdx.x * 4 + wave;
    const int type = u / 448, h = (u % 448) / 28, tau = u % 28;
    const int i = lane & 15, g = lane >> 4;
    bf16x8 fix[8];
    const unsigned short* loopW;
    if (type == 0) {
        const unsigned short* p = Wq + (size_t)(16 * tau + i) * 4096 + h * 256 + 8 * g;
#pragma unroll
        for (int ke = 0; ke < 8; ++ke) fix[ke] = *(const bf16x8*)(p + ke * 32);
        loopW = Wk;
    } else if (type == 1) {
        const unsigned short* p = Wk + (size_t)(16 * tau + i) * 4096 + h * 256 + 8 * g;
#pragma unroll
        for (int ke = 0; ke < 8; ++ke) fix[ke] = *(const bf16x8*)(p + ke * 32);
        loopW = Wq;
    } else {
#pragma unroll
        for (int ke = 0; ke < 8; ++ke)
#pragma unroll
            for (int j = 0; j < 8; ++j)
                fix[ke][j] = (short)Wo[((size_t)h * 256 + ke * 32 + 8 * g + j) * DM + 16 * tau + i];
        loopW = Wv;
    }
    char* dst = (type == 0) ? ws + OFF_WF + (size_t)(h * 28 + tau) * KK * KB
              : (type == 1) ? ws + OFF_ANF + (size_t)(h * 28 + tau) * KK * KB
                            : ws + OFF_WF + (size_t)(448 + h * 28 + tau) * KK * KB;
    for (int kk = 0; kk < KK; ++kk) {
        f32x4 acc[2] = {{0.f, 0.f, 0.f, 0.f}, {0.f, 0.f, 0.f, 0.f}};
#pragma unroll
        for (int tt = 0; tt < 2; ++tt) {
            const unsigned short* p = loopW + (size_t)(32 * kk + 16 * tt + i) * 4096 + h * 256 + 8 * g;
#pragma unroll
            for (int ke = 0; ke < 8; ++ke) acc[tt] = mfma32(*(const bf16x8*)(p + ke * 32), fix[ke], acc[tt]);
        }
        *(bf16x8*)(dst + (size_t)kk * KB + lane * 16) = pack8(acc[0], acc[1]);
    }
}

__global__ __launch_bounds__(256) void k_w12(const unsigned short* __restrict__ w1, const unsigned short* __restrict__ w2, char* __restrict__ ws) {
    const int wave = threadIdx.x >> 6, lane = threadIdx.x & 63;
    const int f = blockIdx.x * 4 + wave;
    const int i = lane & 15, g = lane >> 4;
    bf16x8 v;
    if (f < 112 * KK) {
        int tf = f / KK, kk = f % KK;
#pragma unroll
        for (int j = 0; j < 8; ++j) v[j] = (short)w1[(size_t)(32 * kk + 16 * (j >> 2) + 4 * g + (j & 3)) * FFD + 16 * tf + i];
        *(bf16x8*)(ws + OFF_WF + ((size_t)(896 + tf) * KK + kk) * KB + lane * 16) = v;
    } else {
        int f2 = f - 112 * KK;
        int t = f2 / KF, kf = f2 % KF;
#pragma unroll
        for (int j = 0; j < 8; ++j) v[j] = (short)w2[(size_t)(32 * kf + 16 * (j >> 2) + 4 * g + (j & 3)) * DM + 16 * t + i];
        *(bf16x8*)(ws + OFF_W2F + ((size_t)t * KF + kf) * KB + lane * 16) = v;
    }
}

DI float gelu_tanh(float z) {
    float u = 0.7978845608028654f * (z + 0.044715f * z * z * z);
    float e = __expf(2.f * u);
    float t = 1.f - 2.f / (1.f + e);
    return 0.5f * z * (1.f + t);
}

__global__ __launch_bounds__(256, 1) void k_work(const unsigned short* __restrict__ b1, char* __restrict__ ws) {
    const int wave = threadIdx.x >> 6, lane = threadIdx.x & 63;
    const int unit = blockIdx.x;
    const int tile0 = unit * 4;
    const int i = lane & 15, g = lane >> 4;
    bf16x8 w[4][KK];
    {
        const char* wp = ws + OFF_WF + (size_t)tile0 * KK * KB + lane * 16;
#pragma unroll
        for (int t = 0; t < 4; ++t)
#pragma unroll
            for (int kk = 0; kk < KK; ++kk) w[t][kk] = ldfrag(wp + (size_t)(t * KK + kk) * KB);
    }
    const int type = tile0 < 448 ? 0 : (tile0 < 896 ? 1 : 2);
    for (int s = wave; s < NSEG; s += 4) {
        f32x4 acc[2][4];
#pragma unroll
        for (int q = 0; q < 2; ++q)
#pragma unroll
            for (int t = 0; t < 4; ++t) acc[q][t] = (f32x4){0.f, 0.f, 0.f, 0.f};
        const char* xp = ws + OFF_XNF + (size_t)(2 * s) * KK * KB + lane * 16;
        if (type == 1) {
#pragma unroll
            for (int kk = 0; kk < KK; ++kk)
#pragma unroll
                for (int q = 0; q < 2; ++q) {
                    bf16x8 xf = ldfrag(xp + (size_t)(q * KK + kk) * KB);
#pragma unroll
                    for (int t = 0; t < 4; ++t) acc[q][t] = mfma32(xf, w[t][kk], acc[q][t]);
                }
            const int h = (tile0 - 448) / 28, t0 = (tile0 - 448) % 28;
#pragma unroll
            for (int t = 0; t < 4; ++t)
                *(bf16x8*)(ws + OFF_UF + (((size_t)s * NT16 + t0 + t) * NH + h) * KB + lane * 16) = pack8(acc[0][t], acc[1][t]);
        } else {
#pragma unroll
            for (int kk = 0; kk < KK; ++kk)
#pragma unroll
                for (int q = 0; q < 2; ++q) {
                    bf16x8 xf = ldfrag(xp + (size_t)(q * KK + kk) * KB);
#pragma unroll
                    for (int t = 0; t < 4; ++t) acc[q][t] = mfma32(w[t][kk], xf, acc[q][t]);
                }
            if (type == 0) {
                const int h = tile0 / 28, t0 = tile0 % 28;
#pragma unroll
                for (int q = 0; q < 2; ++q)
#pragma unroll
                    for (int p = 0; p < 2; ++p)
                        *(bf16x8*)(ws + OFF_GTF + (((size_t)h * NRT + 2 * s + q) * KK + (t0 >> 1) + p) * KB + lane * 16) = pack8(acc[q][2 * p], acc[q][2 * p + 1]);
            } else {
                const int tf0 = tile0 - 896;
#pragma unroll
                for (int t = 0; t < 4; ++t) {
                    u32x2 bb = *(const u32x2*)(b1 + 16 * (tf0 + t) + 4 * g);
                    float bv[4] = {bf_lo(bb.x), bf_hi(bb.x), bf_lo(bb.y), bf_hi(bb.y)};
#pragma unroll
                    for (int q = 0; q < 2; ++q)
#pragma unroll
                        for (int r = 0; r < 4; ++r) acc[q][t][r] = gelu_tanh(acc[q][t][r] + bv[r]);
                }
#pragma unroll
                for (int q = 0; q < 2; ++q)
#pragma unroll
                    for (int p = 0; p < 2; ++p)
                        *(bf16x8*)(ws + OFF_HF + ((size_t)(2 * s + q) * KF + (tf0 >> 1) + p) * KB + lane * 16) = pack8(acc[q][2 * p], acc[q][2 * p + 1]);
            }
        }
    }
}

constexpr int CH_LDS_B = 0;
constexpr int CH_LDS_MEM = 112 * 1024;
constexpr int CH_LDS_RA = CH_LDS_MEM + 7168;
constexpr int CH_LDS_RB = CH_LDS_RA + 8192;
constexpr int CH_LDS_PW = CH_LDS_RB + 4096;
constexpr int CH_LDS_SIZE = CH_LDS_PW + 4096;

DI float xmax16(float v) { auto r = __builtin_amdgcn_permlane16_swap(__float_as_uint(v), __float_as_uint(v), false, false); return fmaxf(__uint_as_float(r[0]), __uint_as_float(r[1])); }
DI float xmax32(float v) { auto r = __builtin_amdgcn_permlane32_swap(__float_as_uint(v), __float_as_uint(v), false, false); return fmaxf(__uint_as_float(r[0]), __uint_as_float(r[1])); }
DI float xadd16(float v) { auto r = __builtin_amdgcn_permlane16_swap(__float_as_uint(v), __float_as_uint(v), false, false); return __uint_as_float(r[0]) + __uint_as_float(r[1]); }
DI float xadd32(float v) { auto r = __builtin_amdgcn_permlane32_swap(__float_as_uint(v), __float_as_uint(v), false, false); return __uint_as_float(r[0]) + __uint_as_float(r[1]); }
DI float qswap(float v) { return __uint_as_float(__builtin_amdgcn_update_dpp(0, __float_as_uint(v), 0xB1, 0xF, 0xF, true)); }
DI unsigned umax4(u32x4 v) { return max(max(v.x, v.y), max(v.z, v.w)); }
DI float fexp2(float x) { return __builtin_amdgcn_exp2f(x); }

DI void mfma_Aa0(f32x4& acc, const bf16x8& w, const bf16x8& m) { asm volatile("s_nop 1\n\tv_mfma_f32_16x16x32_bf16 %0, %1, %2, 0" : "=&v"(acc) : "a"(w), "v"(m)); }
DI void mfma_Aa(f32x4& acc, const bf16x8& w, const bf16x8& m) { asm volatile("s_nop 1\n\tv_mfma_f32_16x16x32_bf16 %0, %1, %2, %0" : "+v"(acc) : "a"(w), "v"(m)); }
DI void mfma_Ba0(f32x4& acc, const bf16x8& m, const bf16x8& w) { asm volatile("s_nop 1\n\tv_mfma_f32_16x16x32_bf16 %0, %1, %2, 0" : "=&v"(acc) : "v"(m), "a"(w)); }
DI void mfma_Ba(f32x4& acc, const bf16x8& m, const bf16x8& w) { asm volatile("s_nop 1\n\tv_mfma_f32_16x16x32_bf16 %0, %1, %2, %0" : "+v"(acc) : "v"(m), "a"(w)); }
DI void mfma_v0(f32x4& acc, const bf16x8& a, const bf16x8& b) { asm volatile("s_nop 1\n\tv_mfma_f32_16x16x32_bf16 %0, %1, %2, 0" : "=&v"(acc) : "v"(a), "v"(b)); }
DI void mfma_v(f32x4& acc, const bf16x8& a, const bf16x8& b) { asm volatile("s_nop 1\n\tv_mfma_f32_16x16x32_bf16 %0, %1, %2, %0" : "+v"(acc) : "v"(a), "v"(b)); }
DI void mfma16_v0(f32x4& acc, const bf16x4& a, const bf16x4& b) { asm volatile("s_nop 1\n\tv_mfma_f32_16x16x16_bf16 %0, %1, %2, 0" : "=&v"(acc) : "v"(a), "v"(b)); }
DI void mfma16_v(f32x4& acc, const bf16x4& a, const bf16x4& b) { asm volatile("s_nop 1\n\tv_mfma_f32_16x16x16_bf16 %0, %1, %2, %0" : "+v"(acc) : "v"(a), "v"(b)); }
DI void mfma_fence4(f32x4& a, f32x4& b, f32x4& c, f32x4& d) { asm volatile("s_nop 7\n\ts_nop 1" : "+v"(a), "+v"(b), "+v"(c), "+v"(d)); }
DI void pin(f32x4& a) { asm volatile("" : "+v"(a)); }
DI void mfma_fence1(f32x4& a) { asm volatile("s_nop 7\n\ts_nop 1" : "+v"(a)); }

#define SINGLEPOLL
#define DP 5
#define D1 3
#define D2 3

__global__ __launch_bounds__(256, 1) void k_chain(char* __restrict__ ws) {
    extern __shared__ __attribute__((aligned(16))) char lds[];
    if (blockIdx.x & 7) return;
    const int c = blockIdx.x >> 3;
    const int tid = threadIdx.x, wave = __builtin_amdgcn_readfirstlane(tid >> 6), lane = tid & 63;
    const int i = lane & 15, g = lane >> 4;
    const int h = c >> 1, half = c & 1;
    const bool hasB = c < NT16;
    const int tB = hasB ? c : 0;
    unsigned* tmo = (unsigned*)(ws + OFF_TMO);
    const __amdgpu_buffer_rsrc_t rs = __builtin_amdgcn_make_buffer_rsrc(ws + OFF_MEMF, 0, (int)(OFF_T2F - OFF_MEMF), 0x00020000);
    constexpr unsigned R_SMX = (unsigned)(OFF_SMX - OFF_MEMF);
    const float cs = 0.0625f * 1.4426950408889634f;

    bf16x8 wa[3][KK], wsh[7], wb[2][KK];
    {
        const char* ap = ws + OFF_ANF + (size_t)(h * 28 + 14 * half) * KK * KB + lane * 16;
#pragma unroll
        for (int t = 0; t < 3; ++t)
#pragma unroll
            for (int kk = 0; kk < KK; ++kk) wa[t][kk] = ldfrag(ap + (size_t)((3 * wave + t) * KK + kk) * KB);
#pragma unroll
        for (int k = 0; k < 7; ++k) wsh[k] = ldfrag(ap + (size_t)((12 + (wave >> 1)) * KK + 7 * (wave & 1) + k) * KB);
        const char* bp = ws + OFF_WF + (size_t)(448 + tB) * KK * KB + lane * 16;
#pragma unroll
        for (int hh = 0; hh < 2; ++hh)
#pragma unroll
            for (int kk = 0; kk < KK; ++kk) wb[hh][kk] = ldfrag(bp + (size_t)((4 * wave + hh) * 28 * KK + kk) * KB);
#pragma unroll
        for (int hh = 0; hh < 2; ++hh)
#pragma unroll
            for (int kk = 0; kk < KK; ++kk)
                *(bf16x8*)(lds + CH_LDS_B + (size_t)((wave * 2 + hh) * KK + kk) * KB + lane * 16) = ldfrag(bp + (size_t)((4 * wave + 2 + hh) * 28 * KK + kk) * KB);
    }
    const int gk0 = wave < 2 ? 4 * wave : 8 + 3 * (wave - 2);
    const int gkn = wave < 2 ? 4 : 3;
    bf16x8 gf[4], uf[4];
    {
        const char* gp = ws + OFF_GTF + (((size_t)h * NRT + half) * KK + gk0) * KB + lane * 16;
#pragma unroll
        for (int k = 0; k < 4; ++k) gf[k] = ldfrag(gp + (size_t)(k < gkn ? k : 0) * KB);
    }
    __syncthreads();
    const char* mp = lds + CH_LDS_MEM + g * 128 + (i & 7) * 16;
    const int hh2 = lane >> 4, r2 = (lane >> 1) & 7, q2 = lane & 1;

    for (int s = 0; s < NSEG - 1; ++s) {
        STAMP(0);
        {
            u32x4 x0, x1, a0, a1, b0, b1, c0, c1;
            unsigned spins = 0;
            const unsigned o0 = (unsigned)(s * 7168 + tid * 16), o1 = o0 + (tid < 192 ? 4096 : 0);
#define E1_ISSUE(u, v) do { u = __builtin_amdgcn_raw_buffer_load_b128(rs, o0, 0, 16); v = __builtin_amdgcn_raw_buffer_load_b128(rs, o1, 0, 16); __builtin_amdgcn_sched_barrier(0); } while (0)
#define E1_OK(u, v) __all(max(umax4(u), umax4(v)) != SENT)
            for (int d = 0; d < D1; ++d) __builtin_amdgcn_s_sleep(1);
            for (;;) { E1_ISSUE(a0, a1); if (E1_OK(a0, a1)) break; if (spin_fail(spins, tmo)) break; }
            x0 = a0; x1 = a1;
            {
                const int t0 = tid >> 4, rr = (tid >> 1) & 7, gp = tid & 1;
                char* d0 = lds + CH_LDS_MEM + (t0 >> 1) * 512 + gp * 256 + rr * 16 + (t0 & 1) * 8;
                *(u32x2*)(d0) = (u32x2){x0.x, x0.y};
                *(u32x2*)(d0 + 128) = (u32x2){x0.z, x0.w};
                if (tid < 192) {
                    *(u32x2*)(d0 + 8 * 512) = (u32x2){x1.x, x1.y};
                    *(u32x2*)(d0 + 8 * 512 + 128) = (u32x2){x1.z, x1.w};
                }
            }
        }
        {
            const char* up = ws + OFF_UF + (((size_t)s * NT16 + tB) * NH + 4 * wave) * KB + lane * 16;
#pragma unroll
            for (int k = 0; k < 4; ++k) uf[k] = ldfrag(up + (size_t)k * KB);
        }
        STAMP(1);
        __syncthreads();
        STAMP(2);
        bf16x8 mf[KK];
#pragma unroll
        for (int kk = 0; kk < KK; ++kk) mf[kk] = *(const bf16x8*)(mp + kk * 512);
        {
            bf16x8 mg[4];
            bf16x4 mk[4];
#pragma unroll
            for (int k = 0; k < 4; ++k) mg[k] = *(const bf16x8*)(mp + (gk0 + (k < gkn ? k : 0)) * 512);
#pragma unroll
            for (int t = 0; t < 4; ++t) {
                const int tl = (t < 3) ? 3 * wave + t : 12 + (wave >> 1);
                const int tg = 14 * half + tl;
                mk[t] = *(const bf16x4*)(mp + (tg >> 1) * 512 + (tg & 1) * 8);
            }
            f32x4 ta[4];
            f32x4 sx, sm;
            if (wave & 1) {
                mfma_Aa0(ta[3], wsh[0], mf[7]);
#pragma unroll
                for (int k = 1; k < 7; ++k) mfma_Aa(ta[3], wsh[k], mf[7 + k]);
            } else {
                mfma_Aa0(ta[3], wsh[0], mf[0]);
#pragma unroll
                for (int k = 1; k < 7; ++k) mfma_Aa(ta[3], wsh[k], mf[k]);
            }
            mfma_Aa0(ta[0], wa[0][0], mf[0]);
#pragma unroll
            for (int kk = 1; kk < KK; ++kk) mfma_Aa(ta[0], wa[0][kk], mf[kk]);
            mfma_Aa0(ta[1], wa[1][0], mf[0]);
#pragma unroll
            for (int kk = 1; kk < 4; ++kk) mfma_Aa(ta[1], wa[1][kk], mf[kk]);
            pin(ta[3]); pin(ta[0]);
            mfma16_v0(sm, mk[3], pack4(ta[3]));
            mfma16_v(sm, mk[0], pack4(ta[0]));
#pragma unroll
            for (int kk = 4; kk < KK; ++kk) mfma_Aa(ta[1], wa[1][kk], mf[kk]);
            mfma_Aa0(ta[2], wa[2][0], mf[0]);
#pragma unroll
            for (int kk = 1; kk < 4; ++kk) mfma_Aa(ta[2], wa[2][kk], mf[kk]);
            pin(ta[1]);
            mfma16_v(sm, mk[1], pack4(ta[1]));
#pragma unroll
            for (int kk = 4; kk < KK; ++kk) mfma_Aa(ta[2], wa[2][kk], mf[kk]);
            mfma_v0(sx, gf[0], mg[0]);
            mfma_v(sx, gf[1], mg[1]);
            mfma_v(sx, gf[2], mg[2]);
            if (wave < 2) mfma_v(sx, gf[3], mg[3]);
            pin(ta[2]);
            mfma16_v(sm, mk[2], pack4(ta[2]));
            mfma_fence4(sm, sx, ta[0], ta[1]);
            *(f32x4*)(lds + CH_LDS_RA + (wave * 2 + 0) * 1024 + lane * 16) = sx;
            *(f32x4*)(lds + CH_LDS_RA + (wave * 2 + 1) * 1024 + lane * 16) = sm;
        }
        STAMP(3);
        __syncthreads();
        if (wave < 2) {
            f32x4 v0 = *(const f32x4*)(lds + CH_LDS_RA + (0 + wave) * 1024 + lane * 16);
            f32x4 v1 = *(const f32x4*)(lds + CH_LDS_RA + (2 + wave) * 1024 + lane * 16);
            f32x4 v2 = *(const f32x4*)(lds + CH_LDS_RA + (4 + wave) * 1024 + lane * 16);
            f32x4 v3 = *(const f32x4*)(lds + CH_LDS_RA + (6 + wave) * 1024 + lane * 16);
            f32x4 v = (v0 + v1) + (v2 + v3);
            if (wave == 0) {
                float m = fmaxf(fmaxf(v[0], v[1]), fmaxf(v[2], v[3]));
                m = xmax16(m); m = xmax32(m);
                const float mc = m * cs;
                float e0 = fexp2(v[0] * cs - mc), e1 = fexp2(v[1] * cs - mc), e2 = fexp2(v[2] * cs - mc), e3 = fexp2(v[3] * cs - mc);
                u32x2 pk = {pack2(e0, e1), pack2(e2, e3)};
                if (i < 8) __builtin_amdgcn_raw_buffer_store_b64(pk, rs, R_SMX + (unsigned)(s * E2S + c * E2B + i * 32 + g * 8), 0, 16);
                float sum = (bf_lo(pk.x) + bf_hi(pk.x)) + (bf_lo(pk.y) + bf_hi(pk.y));
                sum = xadd16(sum); sum = xadd32(sum);
                u32x2 st = {__float_as_uint(m), __float_as_uint(sum)};
                if (lane < 8) __builtin_amdgcn_raw_buffer_store_b64(st, rs, R_SMX + (unsigned)(s * E2S + c * E2B + 512 + i * 8), 0, 16);
            } else {
                if (i < 8 && g < 2)
                    __builtin_amdgcn_raw_buffer_store_b128(__builtin_bit_cast(u32x4, v), rs, R_SMX + (unsigned)(s * E2S + c * E2B + 256 + i * 32 + g * 16), 0, 16);
            }
        }
        {
            const char* gp = ws + OFF_GTF + (((size_t)h * NRT + 2 * (s + 1) + half) * KK + gk0) * KB + lane * 16;
#pragma unroll
            for (int k = 0; k < 4; ++k) gf[k] = ldfrag(gp + (size_t)(k < gkn ? k : 0) * KB);
        }
        if (hasB) {
            f32x4 ya[4];
            const char* lb = lds + CH_LDS_B + (size_t)(wave * 2) * KK * KB + lane * 16;
            mfma_Ba0(ya[0], mf[0], wb[0][0]);
            mfma_v0(ya[1], mf[0], wb[1][0]);
#pragma unroll
            for (int k = 1; k < KK; ++k) {
                mfma_Ba(ya[0], mf[k], wb[0][k]);
                mfma_v(ya[1], mf[k], wb[1][k]);
            }
            STAMP(4);
            u32x4 qx[4], qm0, qm1, qs;
            const unsigned ox = R_SMX + (unsigned)(s * E2S + (8 * wave) * E2B + (i & 7) * 32 + g * 8);
            const unsigned om = R_SMX + (unsigned)(s * E2S + (8 * wave + 2 * hh2) * E2B + 256 + r2 * 32 + q2 * 16);
            const unsigned os = R_SMX + (unsigned)(s * E2S + (8 * wave + 2 * hh2) * E2B + 512 + r2 * 8);
            {
                bf16x8 lq[2][4];
#pragma unroll
                for (int k = 0; k < 2; ++k) { lq[0][2 * k] = *(const bf16x8*)(lb + (size_t)k * KB); lq[0][2 * k + 1] = *(const bf16x8*)(lb + (size_t)(KK + k) * KB); }
#pragma unroll
                for (int gq = 0; gq < 7; ++gq) {
                    if (gq < 6) {
#pragma unroll
                        for (int k = 0; k < 2; ++k) {
                            lq[(gq + 1) & 1][2 * k] = *(const bf16x8*)(lb + (size_t)(2 * gq + 2 + k) * KB);
                            lq[(gq + 1) & 1][2 * k + 1] = *(const bf16x8*)(lb + (size_t)(KK + 2 * gq + 2 + k) * KB);
                        }
                    }
                    __builtin_amdgcn_sched_barrier(0);
                    if (gq == 0) { mfma_v0(ya[2], mf[0], lq[0][0]); mfma_v0(ya[3], mf[0], lq[0][1]); }
                    else { mfma_v(ya[2], mf[2 * gq], lq[gq & 1][0]); mfma_v(ya[3], mf[2 * gq], lq[gq & 1][1]); }
                    mfma_v(ya[2], mf[2 * gq + 1], lq[gq & 1][2]); mfma_v(ya[3], mf[2 * gq + 1], lq[gq & 1][3]);
                }
            }
            mfma_fence4(ya[0], ya[1], ya[2], ya[3]);
            bf16x8 yp0 = pack8(ya[0], ya[1]), yp1 = pack8(ya[2], ya[3]);
            __builtin_amdgcn_sched_barrier(0);
            {
                u32x4 pa_[7], pb_[7], pc_[7];
#define E2_ISSUE(P) do { _Pragma("unroll") for (int hh = 0; hh < 4; ++hh) { u32x2 lo_ = __builtin_amdgcn_raw_buffer_load_b64(rs, ox + (2 * hh) * E2B, 0, 16), hi_ = __builtin_amdgcn_raw_buffer_load_b64(rs, ox + (2 * hh + 1) * E2B, 0, 16); P[hh] = (u32x4){lo_.x, lo_.y, hi_.x, hi_.y}; } \
                    P[4] = __builtin_amdgcn_raw_buffer_load_b128(rs, om, 0, 16); P[5] = __builtin_amdgcn_raw_buffer_load_b128(rs, om + E2B, 0, 16); \
                    { u32x2 lo_ = __builtin_amdgcn_raw_buffer_load_b64(rs, os, 0, 16), hi_ = __builtin_amdgcn_raw_buffer_load_b64(rs, os + E2B, 0, 16); P[6] = (u32x4){lo_.x, lo_.y, hi_.x, hi_.y}; } __builtin_amdgcn_sched_barrier(0); } while (0)
#define E2_OK(P) __all(max(max(max(umax4(P[0]), umax4(P[1])), max(umax4(P[2]), umax4(P[3]))), max(max(umax4(P[4]), umax4(P[5])), umax4(P[6]))) != SENT)
#define E2_TAKE(P) do { qx[0] = P[0]; qx[1] = P[1]; qx[2] = P[2]; qx[3] = P[3]; qm0 = P[4]; qm1 = P[5]; qs = P[6]; } while (0)
                unsigned spins = 0;
                for (int d = 0; d < D2; ++d) __builtin_amdgcn_s_sleep(1);
                for (;;) { E2_ISSUE(pa_); if (E2_OK(pa_)) break; if (spin_fail(spins, tmo)) break; }
                E2_TAKE(pa_);
            }
            STAMP(5);
            f32x4 dd[8];
#pragma unroll
            for (int hh = 0; hh < 4; ++hh) {
                const u32x4 u = __builtin_bit_cast(u32x4, uf[hh]);
                u32x2 ulo = {u.x, u.y}, uhi = {u.z, u.w}, elo = {qx[hh].x, qx[hh].y}, ehi = {qx[hh].z, qx[hh].w};
                mfma16_v0(dd[2 * hh], __builtin_bit_cast(bf16x4, ulo), __builtin_bit_cast(bf16x4, elo));
                mfma16_v0(dd[2 * hh + 1], __builtin_bit_cast(bf16x4, uhi), __builtin_bit_cast(bf16x4, ehi));
            }
            f32x4 ms = __builtin_bit_cast(f32x4, qm0) + __builtin_bit_cast(f32x4, qm1);
            const float m0 = __uint_as_float(qs.x), s0 = __uint_as_float(qs.y), m1 = __uint_as_float(qs.z), s1 = __uint_as_float(qs.w);
            float mx = fmaxf(fmaxf(ms[0], ms[1]), fmaxf(ms[2], ms[3]));
            mx = fmaxf(mx, qswap(mx));
            const float Mc = fmaxf(mx, fmaxf(m0, m1)) * cs;
            float e0 = fexp2(ms[0] * cs - Mc), e1 = fexp2(ms[1] * cs - Mc), e2 = fexp2(ms[2] * cs - Mc), e3 = fexp2(ms[3] * cs - Mc);
            float f0 = fexp2(m0 * cs - Mc), f1 = fexp2(m1 * cs - Mc);
            float sm = (e0 + e1) + (e2 + e3);
            sm += qswap(sm);
            const float inv = __builtin_amdgcn_rcpf(sm + f0 * s0 + f1 * s1);
            char* pw = lds + CH_LDS_PW + wave * 1024;
            *(u32x2*)(pw + ((hh2 * 8 + r2) * 2 + q2) * 8) = (u32x2){pack2(e0 * inv, e1 * inv), pack2(e2 * inv, e3 * inv)};
            if (q2 == 0) *(u32x2*)(pw + 512 + (r2 * 4 + hh2) * 8) = (u32x2){__float_as_uint(f0 * inv), __float_as_uint(f1 * inv)};
            __builtin_amdgcn_fence(__ATOMIC_RELEASE, "wavefront");
            __builtin_amdgcn_wave_barrier();
            __builtin_amdgcn_fence(__ATOMIC_ACQUIRE, "wavefront");
            u32x2 pa[4];
#pragma unroll
            for (int hh = 0; hh < 4; ++hh) pa[hh] = *(const u32x2*)(pw + ((hh * 8 + (i & 7)) * 2 + (g & 1)) * 8);
            f32x4 fa = *(const f32x4*)(pw + 512 + (i & 7) * 32), fb = *(const f32x4*)(pw + 512 + (i & 7) * 32 + 16);
            if (g >= 2) {
#pragma unroll
                for (int hh = 0; hh < 4; ++hh) pa[hh] = (u32x2){0u, 0u};
            }
            mfma_fence4(dd[0], dd[1], dd[2], dd[3]);
            mfma_fence4(dd[4], dd[5], dd[6], dd[7]);
            f32x4 acc = dd[0] * fa[0] + dd[1] * fa[1] + dd[2] * fa[2] + dd[3] * fa[3] + dd[4] * fb[0] + dd[5] * fb[1] + dd[6] * fb[2] + dd[7] * fb[3];
            u32x4 p01 = {pa[0].x, pa[0].y, pa[1].x, pa[1].y}, p23 = {pa[2].x, pa[2].y, pa[3].x, pa[3].y};
            mfma_v(acc, yp0, __builtin_bit_cast(bf16x8, p01));
            mfma_v(acc, yp1, __builtin_bit_cast(bf16x8, p23));
            mfma_fence1(acc);
            *(f32x4*)(lds + CH_LDS_RB + wave * 1024 + lane * 16) = acc;
        }
        STAMP(6);
        __syncthreads();
        if (hasB) {
            if (wave == 0) {
                f32x4 v0 = *(const f32x4*)(lds + CH_LDS_RB + lane * 16);
                f32x4 v1 = *(const f32x4*)(lds + CH_LDS_RB + 1024 + lane * 16);
                f32x4 v2 = *(const f32x4*)(lds + CH_LDS_RB + 2048 + lane * 16);
                f32x4 v3 = *(const f32x4*)(lds + CH_LDS_RB + 3072 + lane * 16);
                f32x4 v = (v0 + v1) + (v2 + v3);
                if (i < 8) {
                    u32x2 pk = {pack2(v[0], v[1]), pack2(v[2], v[3])};
                    __builtin_amdgcn_raw_buffer_store_b64(pk, rs, (unsigned)((s + 1) * 7168 + tB * 256 + i * 32 + g * 8), 0, 16);
                }
            }
        }
    }
}

__global__ __launch_bounds__(256, 1) void k_memrows(char* __restrict__ ws) {
    const int wave = threadIdx.x >> 6, lane = threadIdx.x & 63;
    const int unit = blockIdx.x;
    const int tile0 = unit * 4;
    const int i = lane & 15, g = lane >> 4;
    bf16x8 w[4][KK];
    {
        const char* wp = ws + OFF_WF + (size_t)tile0 * KK * KB + lane * 16;
#pragma unroll
        for (int t = 0; t < 4; ++t)
#pragma unroll
            for (int kk = 0; kk < KK; ++kk) w[t][kk] = ldfrag(wp + (size_t)(t * KK + kk) * KB);
    }
    const int type = tile0 < 448 ? 0 : 1;
    for (int sp = wave; sp < NSEG / 2; sp += 4) {
        f32x4 acc[4];
#pragma unroll
        for (int t = 0; t < 4; ++t) acc[t] = (f32x4){0.f, 0.f, 0.f, 0.f};
        const char* mp = ws + OFF_MEMF + (size_t)(2 * sp + (i >> 3)) * 7168 + (i & 7) * 32 + g * 8;
        if (type == 0) {
#pragma unroll
            for (int kk = 0; kk < KK; ++kk) {
                u32x2 lo_ = *(const u32x2*)(mp + kk * 512), hi_ = *(const u32x2*)(mp + kk * 512 + 256);
                u32x4 xx_ = {lo_.x, lo_.y, hi_.x, hi_.y};
                bf16x8 xf = __builtin_bit_cast(bf16x8, xx_);
#pragma unroll
                for (int t = 0; t < 4; ++t) acc[t] = mfma32(w[t][kk], xf, acc[t]);
            }
            const int h = tile0 / 28, t0 = tile0 % 28;
#pragma unroll
            for (int p = 0; p < 2; ++p)
                *(bf16x8*)(ws + OFF_T2F + ((size_t)h * NSEG + 2 * sp + (i >> 3)) * 7168 + ((t0 >> 1) + p) * 512 + g * 128 + (i & 7) * 16) = pack8(acc[2 * p], acc[2 * p + 1]);
        } else {
#pragma unroll
            for (int kk = 0; kk < KK; ++kk) {
                u32x2 lo_ = *(const u32x2*)(mp + kk * 512), hi_ = *(const u32x2*)(mp + kk * 512 + 256);
                u32x4 xx_ = {lo_.x, lo_.y, hi_.x, hi_.y};
                bf16x8 xf = __builtin_bit_cast(bf16x8, xx_);
#pragma unroll
                for (int t = 0; t < 4; ++t) acc[t] = mfma32(xf, w[t][kk], acc[t]);
            }
            const int h = (tile0 - 448) / 28, t0 = (tile0 - 448) % 28;
#pragma unroll
            for (int t = 0; t < 4; ++t)
                *(bf16x4*)(ws + OFF_YF + (((size_t)(2 * sp + (g >> 1)) * NT16 + t0 + t) * 8 + (h >> 1)) * 512 + (g & 1) * 256 + i * 16 + (h & 1) * 8) = pack4(acc[t]);
        }
    }
}

__global__ __launch_bounds__(256) void k_f1(char* __restrict__ ws) {
    const int wave = threadIdx.x >> 6, lane = threadIdx.x & 63;
    const int u = blockIdx.x * 4 + wave;
    const int s = u >> 4, h = u & 15;
    const int i = lane & 15, g = lane >> 4;
    f32x4 sc[2][3];
#pragma unroll
    for (int q = 0; q < 2; ++q)
#pragma unroll
        for (int k = 0; k < 3; ++k) sc[q][k] = (f32x4){0.f, 0.f, 0.f, 0.f};
    const char* xp = ws + OFF_XNF + (size_t)(2 * s) * KK * KB + lane * 16;
    const char* gp = ws + OFF_GTF + ((size_t)h * NRT + 2 * s) * KK * KB + lane * 16;
    const char* tp = ws + OFF_T2F + ((size_t)h * NSEG + s) * 7168 + g * 128 + (i & 7) * 16;
#pragma unroll
    for (int kk = 0; kk < KK; ++kk) {
        bf16x8 g0 = ldfrag(gp + (size_t)kk * KB), g1 = ldfrag(gp + (size_t)(KK + kk) * KB), t2 = ldfrag(tp + kk * 512);
#pragma unroll
        for (int q = 0; q < 2; ++q) {
            bf16x8 xf = ldfrag(xp + (size_t)(q * KK + kk) * KB);
            sc[q][0] = mfma32(g0, xf, sc[q][0]);
            sc[q][1] = mfma32(g1, xf, sc[q][1]);
            sc[q][2] = mfma32(t2, xf, sc[q][2]);
        }
    }
    const float cs = 0.0625f * 1.4426950408889634f;
    const float NEG = -3.0e38f;
#pragma unroll
    for (int q = 0; q < 2; ++q) {
        const int qi = 16 * q + i;
        float xs[8], ms[4];
#pragma unroll
        for (int j = 0; j < 8; ++j) {
            const int key = 16 * (j >> 2) + 4 * g + (j & 3);
            xs[j] = (key <= qi) ? sc[q][j >> 2][j & 3] : NEG;
        }
#pragma unroll
        for (int j = 0; j < 4; ++j) ms[j] = (g < 2) ? sc[q][2][j] : NEG;
        float m = fmaxf(fmaxf(fmaxf(xs[0], xs[1]), fmaxf(xs[2], xs[3])), fmaxf(fmaxf(xs[4], xs[5]), fmaxf(xs[6], xs[7])));
        m = fmaxf(m, fmaxf(fmaxf(ms[0], ms[1]), fmaxf(ms[2], ms[3])));
        m = fmaxf(m, __shfl_xor(m, 16));
        m = fmaxf(m, __shfl_xor(m, 32));
        float sum = 0.f;
#pragma unroll
        for (int j = 0; j < 8; ++j) { const int key = 16 * (j >> 2) + 4 * g + (j & 3); xs[j] = (key <= qi) ? exp2f((xs[j] - m) * cs) : 0.f; sum += xs[j]; }
#pragma unroll
        for (int j = 0; j < 4; ++j) { ms[j] = (g < 2) ? exp2f((ms[j] - m) * cs) : 0.f; sum += ms[j]; }
        sum += __shfl_xor(sum, 16);
        sum += __shfl_xor(sum, 32);
        const float inv = 1.f / sum;
        u32x4 px = {pack2(xs[0] * inv, xs[1] * inv), pack2(xs[2] * inv, xs[3] * inv), pack2(xs[4] * inv, xs[5] * inv), pack2(xs[6] * inv, xs[7] * inv)};
        *(u32x4*)(ws + OFF_PXF + (((size_t)s * 2 + q) * NH + h) * KB + lane * 16) = px;
        u32x2 pmv = {pack2(ms[0] * inv, ms[1] * inv), pack2(ms[2] * inv, ms[3] * inv)};
        *(u32x2*)(ws + OFF_PMF + (((size_t)s * 2 + q) * 8 + (h >> 1)) * KB + lane * 16 + (h & 1) * 8) = pmv;
    }
}

__global__ __launch_bounds__(256) void k_f2(const unsigned short* __restrict__ x, const unsigned short* __restrict__ b2, const char* __restrict__ ws, unsigned short* __restrict__ out) {
    const int wave = threadIdx.x >> 6, lane = threadIdx.x & 63;
    const int s = blockIdx.x / 7, t = (blockIdx.x % 7) * 4 + wave;
    const int i = lane & 15, g = lane >> 4;
    f32x4 acc[2] = {{0.f, 0.f, 0.f, 0.f}, {0.f, 0.f, 0.f, 0.f}};
    {
        const char* up = ws + OFF_UF + ((size_t)s * NT16 + t) * NH * KB + lane * 16;
        const char* pp = ws + OFF_PXF + (size_t)s * 2 * NH * KB + lane * 16;
#pragma unroll 4
        for (int h = 0; h < NH; ++h) {
            bf16x8 a = ldfrag(up + (size_t)h * KB);
            acc[0] = mfma32(a, ldfrag(pp + (size_t)h * KB), acc[0]);
            acc[1] = mfma32(a, ldfrag(pp + (size_t)(NH + h) * KB), acc[1]);
        }
    }
    {
        const char* yp = ws + OFF_YF + ((size_t)s * NT16 + t) * 8 * 512 + (g & 1) * 256 + i * 16;
        const char* pp = ws + OFF_PMF + (size_t)s * 2 * 8 * KB + lane * 16;
#pragma unroll 4
        for (int hp = 0; hp < 8; ++hp) {
            bf16x8 a = ldfrag(yp + hp * 512);
            acc[0] = mfma32(a, ldfrag(pp + (size_t)hp * KB), acc[0]);
            acc[1] = mfma32(a, ldfrag(pp + (size_t)(8 + hp) * KB), acc[1]);
        }
    }
    {
        const char* wp = ws + OFF_W2F + (size_t)t * KF * KB + lane * 16;
        const char* hp = ws + OFF_HF + (size_t)(2 * s) * KF * KB + lane * 16;
#pragma unroll 4
        for (int kf = 0; kf < KF; ++kf) {
            bf16x8 a = ldfrag(wp + (size_t)kf * KB);
            acc[0] = mfma32(a, ldfrag(hp + (size_t)kf * KB), acc[0]);
            acc[1] = mfma32(a, ldfrag(hp + (size_t)(KF + kf) * KB), acc[1]);
        }
    }
    u32x2 bb = *(const u32x2*)(b2 + 16 * t + 4 * g);
    const float bv[4] = {bf_lo(bb.x), bf_hi(bb.x), bf_lo(bb.y), bf_hi(bb.y)};
#pragma unroll
    for (int q = 0; q < 2; ++q) {
        const size_t o = (size_t)(32 * s + 16 * q + i) * DM + 16 * t + 4 * g;
        u32x2 xv = *(const u32x2*)(x + o);
        u32x2 r = {pack2(bf_lo(xv.x) + acc[q][0] + bv[0], bf_hi(xv.x) + acc[q][1] + bv[1]), pack2(bf_lo(xv.y) + acc[q][2] + bv[2], bf_hi(xv.y) + acc[q][3] + bv[3])};
        *(u32x2*)(out + o) = r;
    }
}

extern "C" void kernel_launch(void* const* d_in, const int* in_sizes, int n_in,
                              void* d_out, int out_size, void* d_ws, size_t ws_size,
                              hipStream_t stream) {
    const unsigned short* x = (const unsigned short*)d_in[0];
    const unsigned short* mem0 = (const unsigned short*)d_in[1];
    const unsigned short* Wq = (const unsigned short*)d_in[2];
    const unsigned short* Wk = (const unsigned short*)d_in[3];
    const unsigned short* Wv = (const unsigned short*)d_in[4];
    const unsigned short* Wo = (const unsigned short*)d_in[5];
    const unsigned short* w1 = (const unsigned short*)d_in[8];
    const unsigned short* b1 = (const unsigned short*)d_in[9];
    const unsigned short* w2 = (const unsigned short*)d_in[10];
    const unsigned short* b2 = (const unsigned short*)d_in[11];
    char* ws = (char*)d_ws;
    unsigned short* out = (unsigned short*)d_out;
    hipFuncSetAttribute((const void*)k_chain, hipFuncAttributeMaxDynamicSharedMemorySize, CH_LDS_SIZE);
    k_ln<<<512, 256, 0, stream>>>(x, mem0, ws);
    k_ab<<<336, 256, 0, stream>>>(Wq, Wk, Wv, Wo, ws);
    k_w12<<<784, 256, 0, stream>>>(w1, w2, ws);
    k_work<<<252, 256, 0, stream>>>(b1, ws);
    k_chain<<<256, 256, CH_LDS_SIZE, stream>>>(ws);
    k_memrows<<<224, 256, 0, stream>>>(ws);
    k_f1<<<256, 256, 0, stream>>>(ws);
    k_f2<<<448, 256, 0, stream>>>(x, b2, ws, out);
}
```

```cpp
#include <hip/hip_runtime.h>
#include <hip/hip_bf16.h>

typedef __attribute__((ext_vector_type(8))) short bf16x8;
typedef __attribute__((ext_vector_type(4))) short bf16x4;
typedef __attribute__((ext_vector_type(4))) float f32x4;
typedef __attribute__((ext_vector_type(4))) unsigned u32x4;
typedef __attribute__((ext_vector_type(2))) unsigned u32x2;
typedef __attribute__((ext_vector_type(2))) __bf16 bf2_t;
#define DI __device__ __forceinline__

constexpr int DM = 448, KK = 14, NT16 = 28, NH = 16, NSEG = 64, NRT = 128, FFD = 1792, KF = 56;
constexpr size_t KB = 1024;
constexpr size_t OFF_XNF = 0;
constexpr size_t OFF_WF = OFF_XNF + (size_t)NRT * KK * KB;
constexpr size_t OFF_ANF = OFF_WF + (size_t)1008 * KK * KB;
constexpr size_t OFF_W2F = OFF_ANF + (size_t)448 * KK * KB;
constexpr size_t OFF_GTF = OFF_W2F + (size_t)28 * KF * KB;
constexpr size_t OFF_UF = OFF_GTF + (size_t)NH * NRT * KK * KB;
constexpr size_t OFF_FO = OFF_UF + (size_t)NSEG * NT16 * NH * KB;
constexpr size_t OFF_MEMF = OFF_FO + (size_t)2048 * DM * 4;
constexpr size_t OFF_SMX = OFF_MEMF + (size_t)NSEG * 7168;
constexpr int E2B = 640, E2S = 32 * E2B;
constexpr size_t OFF_T2F = OFF_SMX + (size_t)NSEG * E2S;
constexpr size_t OFF_YF = OFF_T2F + (size_t)NH * NSEG * 7168;
constexpr size_t OFF_PXF = OFF_YF + (size_t)NSEG * NT16 * 8 * 512;
constexpr size_t OFF_PMF = OFF_PXF + (size_t)NSEG * 2 * NH * KB;
constexpr size_t OFF_CNT = OFF_PMF + (size_t)NSEG * 2 * 8 * KB;
constexpr int CNT_G = 0, CNT_T = 64, CNT_P = 96, CNT_F = 160, CNT_TMO = 224, CNT_N = 232;
constexpr size_t OFF_TMO = OFF_CNT + (size_t)CNT_TMO * 128;
constexpr size_t WS_END = OFF_CNT + (size_t)CNT_N * 128;
constexpr size_t OFF_DBG = WS_END;
#define STAMP(k)

constexpr unsigned SENT = 0xFFFFFFFFu;
constexpr unsigned kSpinCap = 1u << 16;

DI unsigned pack2(float a, float b) { bf2_t v = {(__bf16)a, (__bf16)b}; return __builtin_bit_cast(unsigned, v); }
DI float bf_lo(unsigned u) { return __uint_as_float(u << 16); }
DI float bf_hi(unsigned u) { return __uint_as_float(u & 0xFFFF0000u); }
DI f32x4 mfma32(bf16x8 a, bf16x8 b, f32x4 c) { return __builtin_amdgcn_mfma_f32_16x16x32_bf16(a, b, c, 0, 0, 0); }
DI bf16x8 ldfrag(const char* p) { return *(const bf16x8*)p; }
DI bf16x8 pack8(f32x4 a, f32x4 b) {
    u32x4 r = {pack2(a[0], a[1]), pack2(a[2], a[3]), pack2(b[0], b[1]), pack2(b[2], b[3])};
    return __builtin_bit_cast(bf16x8, r);
}
DI bf16x4 pack4(f32x4 a) { u32x2 r = {pack2(a[0], a[1]), pack2(a[2], a[3])}; return __builtin_bit_cast(bf16x4, r); }
DI bool spin_fail(unsigned& spins, unsigned* tmo, unsigned code = 1u) {
    if (++spins > kSpinCap) { unsigned exp_ = 0u; __hip_atomic_compare_exchange_strong(tmo, &exp_, code, __ATOMIC_RELAXED, __ATOMIC_RELAXED, __HIP_MEMORY_SCOPE_AGENT); return true; }
    if ((spins & 63u) == 0u) return __builtin_amdgcn_readfirstlane(__hip_atomic_load(tmo, __ATOMIC_RELAXED, __HIP_MEMORY_SCOPE_AGENT)) != 0u;
    return false;
}
typedef __amdgpu_buffer_rsrc_t rsrc_t;
DI bf16x8 ld8c(rsrc_t rs, size_t off) { return __builtin_bit_cast(bf16x8, __builtin_amdgcn_raw_buffer_load_b128(rs, (unsigned)off, 0, 16)); }
DI u32x4 ld4c(rsrc_t rs, size_t off) { return __builtin_amdgcn_raw_buffer_load_b128(rs, (unsigned)off, 0, 16); }
DI u32x2 ld2c(rsrc_t rs, size_t off) { return __builtin_amdgcn_raw_buffer_load_b64(rs, (unsigned)off, 0, 16); }
DI unsigned ld1c(rsrc_t rs, size_t off) { return __builtin_amdgcn_raw_buffer_load_b32(rs, (unsigned)off, 0, 16); }
DI void st8c(rsrc_t rs, size_t off, bf16x8 v) { __builtin_amdgcn_raw_buffer_store_b128(__builtin_bit_cast(u32x4, v), rs, (unsigned)off, 0, 16); }
DI void st4c(rsrc_t rs, size_t off, u32x4 v) { __builtin_amdgcn_raw_buffer_store_b128(v, rs, (unsigned)off, 0, 16); }
DI void st2c(rsrc_t rs, size_t off, u32x2 v) { __builtin_amdgcn_raw_buffer_store_b64(v, rs, (unsigned)off, 0, 16); }
DI void signal_cnt(char* ws, int idx, int lane) {
    asm volatile("s_waitcnt vmcnt(0)" ::: "memory");
    if (lane == 0) __hip_atomic_fetch_add((unsigned*)(ws + OFF_CNT + (size_t)idx * 128), 1u, __ATOMIC_RELAXED, __HIP_MEMORY_SCOPE_AGENT);
}
DI void wait_cnt(char* ws, int idx, unsigned target) {
    unsigned spins = 0;
    unsigned* p = (unsigned*)(ws + OFF_CNT + (size_t)idx * 128);
    for (;;) {
        unsigned v = __builtin_amdgcn_readfirstlane(__hip_atomic_load(p, __ATOMIC_RELAXED, __HIP_MEMORY_SCOPE_AGENT));
        if (v >= target) break;
        __builtin_amdgcn_s_sleep(8);
        if (spin_fail(spins, (unsigned*)(ws + OFF_TMO), 0x300u + idx)) break;
    }
    asm volatile("" ::: "memory");
}

DI float gelu_tanh(float z) {
    float u = 0.7978845608028654f * (z + 0.044715f * z * z * z);
    float e = __expf(2.f * u);
    float t = 1.f - 2.f / (1.f + e);
    return 0.5f * z * (1.f + t);
}

DI float xmax16(float v) { auto r = __builtin_amdgcn_permlane16_swap(__float_as_uint(v), __float_as_uint(v), false, false); return fmaxf(__uint_as_float(r[0]), __uint_as_float(r[1])); }
DI float xmax32(float v) { auto r = __builtin_amdgcn_permlane32_swap(__float_as_uint(v), __float_as_uint(v), false, false); return fmaxf(__uint_as_float(r[0]), __uint_as_float(r[1])); }
DI float xadd16(float v) { auto r = __builtin_amdgcn_permlane16_swap(__float_as_uint(v), __float_as_uint(v), false, false); return __uint_as_float(r[0]) + __uint_as_float(r[1]); }
DI float xadd32(float v) { auto r = __builtin_amdgcn_permlane32_swap(__float_as_uint(v), __float_as_uint(v), false, false); return __uint_as_float(r[0]) + __uint_as_float(r[1]); }
DI float qswap(float v) { return __uint_as_float(__builtin_amdgcn_update_dpp(0, __float_as_uint(v), 0xB1, 0xF, 0xF, true)); }
DI unsigned umax4(u32x4 v) { return max(max(v.x, v.y), max(v.z, v.w)); }
DI float fexp2(float x) { return __builtin_amdgcn_exp2f(x); }

DI void mfma_Aa0(f32x4& acc, const bf16x8& w, const bf16x8& m) { asm volatile("s_nop 1\n\tv_mfma_f32_16x16x32_bf16 %0, %1, %2, 0" : "=&v"(acc) : "a"(w), "v"(m)); }
DI void mfma_Aa(f32x4& acc, const bf16x8& w, const bf16x8& m) { asm volatile("s_nop 1\n\tv_mfma_f32_16x16x32_bf16 %0, %1, %2, %0" : "+v"(acc) : "a"(w), "v"(m)); }
DI void mfma_Ba0(f32x4& acc, const bf16x8& m, const bf16x8& w) { asm volatile("s_nop 1\n\tv_mfma_f32_16x16x32_bf16 %0, %1, %2, 0" : "=&v"(acc) : "v"(m), "a"(w)); }
DI void mfma_Ba(f32x4& acc, const bf16x8& m, const bf16x8& w) { asm volatile("s_nop 1\n\tv_mfma_f32_16x16x32_bf16 %0, %1, %2, %0" : "+v"(acc) : "v"(m), "a"(w)); }
DI void mfma_v0(f32x4& acc, const bf16x8& a, const bf16x8& b) { asm volatile("s_nop 1\n\tv_mfma_f32_16x16x32_bf16 %0, %1, %2, 0" : "=&v"(acc) : "v"(a), "v"(b)); }
DI void mfma_v(f32x4& acc, const bf16x8& a, const bf16x8& b) { asm volatile("s_nop 1\n\tv_mfma_f32_16x16x32_bf16 %0, %1, %2, %0" : "+v"(acc) : "v"(a), "v"(b)); }
DI void mfma16_v0(f32x4& acc, const bf16x4& a, const bf16x4& b) { asm volatile("s_nop 1\n\tv_mfma_f32_16x16x16_bf16 %0, %1, %2, 0" : "=&v"(acc) : "v"(a), "v"(b)); }
DI void mfma16_v(f32x4& acc, const bf16x4& a, const bf16x4& b) { asm volatile("s_nop 1\n\tv_mfma_f32_16x16x16_bf16 %0, %1, %2, %0" : "+v"(acc) : "v"(a), "v"(b)); }
DI void mfma_fence4(f32x4& a, f32x4& b, f32x4& c, f32x4& d) { asm volatile("s_nop 7\n\ts_nop 1" : "+v"(a), "+v"(b), "+v"(c), "+v"(d)); }
DI void pin(f32x4& a) { asm volatile("" : "+v"(a)); }
DI void mfma_fence1(f32x4& a) { asm volatile("s_nop 7\n\ts_nop 1" : "+v"(a)); }

constexpr int PB_A = 0, PB_B = 392, PB_W = PB_B + 224, PB_LN = PB_W + 784, PB_END = PB_LN + 128;

__global__ __launch_bounds__(256) void k_prep(const unsigned short* __restrict__ x, const unsigned short* __restrict__ mem0,
                                              const unsigned short* __restrict__ Wq, const unsigned short* __restrict__ Wk,
                                              const unsigned short* __restrict__ Wv, const unsigned short* __restrict__ Wo,
                                              const unsigned short* __restrict__ w1, const unsigned short* __restrict__ w2, char* __restrict__ ws) {
    __shared__ __attribute__((aligned(16))) unsigned short wo_t[256 * 64];
    const int wave = threadIdx.x >> 6, lane = threadIdx.x & 63;
    const int i = lane & 15, g = lane >> 4;
    const int b = blockIdx.x;
    if (b < PB_B) {
        const int u = b * 4 + wave;
        const int h = u / 98, ap = (u % 98) / 7, bq = u % 7;
        bf16x8 qf[2][8], kf[4][8];
#pragma unroll
        for (int a = 0; a < 2; ++a) {
            const unsigned short* p = Wq + (size_t)(32 * ap + 16 * a + i) * 4096 + h * 256 + 8 * g;
#pragma unroll
            for (int ke = 0; ke < 8; ++ke) qf[a][ke] = *(const bf16x8*)(p + ke * 32);
        }
#pragma unroll
        for (int bb = 0; bb < 4; ++bb) {
            const unsigned short* p = Wk + (size_t)(64 * bq + 16 * bb + i) * 4096 + h * 256 + 8 * g;
#pragma unroll
            for (int ke = 0; ke < 8; ++ke) kf[bb][ke] = *(const bf16x8*)(p + ke * 32);
        }
#pragma unroll
        for (int a = 0; a < 2; ++a)
#pragma unroll
            for (int bp = 0; bp < 2; ++bp) {
                f32x4 d0 = {0.f, 0.f, 0.f, 0.f}, d1 = {0.f, 0.f, 0.f, 0.f};
#pragma unroll
                for (int ke = 0; ke < 8; ++ke) { d0 = mfma32(kf[2 * bp][ke], qf[a][ke], d0); d1 = mfma32(kf[2 * bp + 1][ke], qf[a][ke], d1); }
                *(bf16x8*)(ws + OFF_WF + ((size_t)(h * 28 + 2 * ap + a) * KK + 2 * bq + bp) * KB + lane * 16) = pack8(d0, d1);
            }
#pragma unroll
        for (int bb = 0; bb < 4; ++bb) {
            f32x4 d0 = {0.f, 0.f, 0.f, 0.f}, d1 = {0.f, 0.f, 0.f, 0.f};
#pragma unroll
            for (int ke = 0; ke < 8; ++ke) { d0 = mfma32(qf[0][ke], kf[bb][ke], d0); d1 = mfma32(qf[1][ke], kf[bb][ke], d1); }
            *(bf16x8*)(ws + OFF_ANF + ((size_t)(h * 28 + 4 * bq + bb) * KK + ap) * KB + lane * 16) = pack8(d0, d1);
        }
    } else if (b < PB_W) {
        const int u = b - PB_B;
        const int h = u / 14, cg = (u % 14) >> 1, kh = u & 1;
        for (int e = threadIdx.x; e < 256 * 8; e += 256) {
            const int row = e >> 3, ch = e & 7;
            *(u32x4*)(wo_t + row * 64 + ch * 8) = *(const u32x4*)(Wo + ((size_t)h * 256 + row) * DM + 64 * cg + ch * 8);
        }
        __syncthreads();
        bf16x8 fix[8];
#pragma unroll
        for (int ke = 0; ke < 8; ++ke)
#pragma unroll
            for (int j = 0; j < 8; ++j) fix[ke][j] = (short)wo_t[(ke * 32 + 8 * g + j) * 64 + 16 * wave + i];
        char* dst = ws + OFF_WF + (size_t)(448 + h * 28 + 4 * cg + wave) * KK * KB + lane * 16;
        bf16x8 cur[2][8];
        {
            const unsigned short* p = Wv + (size_t)(32 * (7 * kh) + i) * 4096 + h * 256 + 8 * g;
#pragma unroll
            for (int tt = 0; tt < 2; ++tt)
#pragma unroll
                for (int ke = 0; ke < 8; ++ke) cur[tt][ke] = *(const bf16x8*)(p + (size_t)tt * 16 * 4096 + ke * 32);
        }
        for (int k = 0; k < 7; ++k) {
            const int kk = 7 * kh + k;
            bf16x8 nxt[2][8];
            {
                const int kn = (k < 6) ? kk + 1 : kk;
                const unsigned short* p = Wv + (size_t)(32 * kn + i) * 4096 + h * 256 + 8 * g;
#pragma unroll
                for (int tt = 0; tt < 2; ++tt)
#pragma unroll
                    for (int ke = 0; ke < 8; ++ke) nxt[tt][ke] = *(const bf16x8*)(p + (size_t)tt * 16 * 4096 + ke * 32);
            }
            f32x4 d0 = {0.f, 0.f, 0.f, 0.f}, d1 = {0.f, 0.f, 0.f, 0.f};
#pragma unroll
            for (int ke = 0; ke < 8; ++ke) { d0 = mfma32(cur[0][ke], fix[ke], d0); d1 = mfma32(cur[1][ke], fix[ke], d1); }
            *(bf16x8*)(dst + (size_t)kk * KB) = pack8(d0, d1);
#pragma unroll
            for (int tt = 0; tt < 2; ++tt)
#pragma unroll
                for (int ke = 0; ke < 8; ++ke) cur[tt][ke] = nxt[tt][ke];
        }
    } else if (b < PB_LN) {
        const int f = (b - PB_W) * 4 + wave;
        bf16x8 v;
        if (f < 112 * KK) {
            int tf = f / KK, kk = f % KK;
#pragma unroll
            for (int j = 0; j < 8; ++j) v[j] = (short)w1[(size_t)(32 * kk + 16 * (j >> 2) + 4 * g + (j & 3)) * FFD + 16 * tf + i];
            *(bf16x8*)(ws + OFF_WF + ((size_t)(896 + tf) * KK + kk) * KB + lane * 16) = v;
        } else {
            int f2 = f - 112 * KK;
            int t = f2 / KF, kf = f2 % KF;
#pragma unroll
            for (int j = 0; j < 8; ++j) v[j] = (short)w2[(size_t)(32 * kf + 16 * (j >> 2) + 4 * g + (j & 3)) * DM + 16 * t + i];
            *(bf16x8*)(ws + OFF_W2F + ((size_t)t * KF + kf) * KB + lane * 16) = v;
        }
    } else {
        const int lb = b - PB_LN;
        float v[4][2][4];
#pragma unroll
        for (int rr = 0; rr < 4; ++rr) {
            const unsigned short* xr = x + (size_t)(lb * 16 + wave * 4 + rr) * DM;
#pragma unroll
            for (int it = 0; it < 2; ++it) {
                int grp = lane + it * 64;
                u32x2 u = {0u, 0u};
                if (grp < 112) u = *(const u32x2*)(xr + grp * 4);
                v[rr][it][0] = bf_lo(u.x); v[rr][it][1] = bf_hi(u.x); v[rr][it][2] = bf_lo(u.y); v[rr][it][3] = bf_hi(u.y);
            }
        }
#pragma unroll
        for (int rr = 0; rr < 4; ++rr) {
            float sum = 0.f;
#pragma unroll
            for (int it = 0; it < 2; ++it) sum += v[rr][it][0] + v[rr][it][1] + v[rr][it][2] + v[rr][it][3];
#pragma unroll
            for (int o = 32; o >= 1; o >>= 1) sum += __shfl_xor(sum, o);
            const float mean = sum * (1.f / DM);
            float sq = 0.f;
#pragma unroll
            for (int it = 0; it < 2; ++it) {
                if (lane + it * 64 < 112) {
#pragma unroll
                    for (int j = 0; j < 4; ++j) { float d = v[rr][it][j] - mean; sq += d * d; }
                }
            }
#pragma unroll
            for (int o = 32; o >= 1; o >>= 1) sq += __shfl_xor(sq, o);
            const float rstd = 1.f / sqrtf(sq * (1.f / DM) + 1e-5f);
            const int ii = wave * 4 + rr;
#pragma unroll
            for (int it = 0; it < 2; ++it) {
                int grp = lane + it * 64;
                if (grp < 112) {
                    int d = grp * 4, kk = d >> 5, dd = d & 31, half = dd >> 4, gg = (dd & 15) >> 2;
                    u32x2 o = {pack2((v[rr][it][0] - mean) * rstd, (v[rr][it][1] - mean) * rstd), pack2((v[rr][it][2] - mean) * rstd, (v[rr][it][3] - mean) * rstd)};
                    *(u32x2*)(ws + OFF_XNF + ((size_t)(lb * KK + kk) * 64 + (ii + 16 * gg)) * 16 + half * 8) = o;
                }
            }
        }
        {
            const size_t n16 = ((size_t)(NSEG - 1) * 7168 + (size_t)NSEG * E2S) / 16;
            u32x4 sv = {SENT, SENT, SENT, SENT};
            for (size_t idx = (size_t)lb * 256 + threadIdx.x; idx < n16; idx += (size_t)128 * 256)
                *(u32x4*)(ws + OFF_MEMF + 7168 + idx * 16) = sv;
        }
        if (lb == 0) {
            for (int e = threadIdx.x; e < 8 * 112; e += 256) {
                int r = e / 112, grp = e % 112;
                int d = grp * 4, t = d >> 4, gg = (d & 15) >> 2;
                u32x2 u = *(const u32x2*)(mem0 + r * DM + d);
                *(u32x2*)(ws + OFF_MEMF + t * 256 + r * 32 + gg * 8) = u;
            }
        }
        if (lb == 1) {
            if (threadIdx.x < CNT_N) *(unsigned*)(ws + OFF_CNT + (size_t)threadIdx.x * 128) = 0u;
        }
    }
}

constexpr int CH_LDS_B = 0;
constexpr int CH_LDS_MEM = 112 * 1024;
constexpr int CH_LDS_RA = CH_LDS_MEM + 7168;
constexpr int CH_LDS_RB = CH_LDS_RA + 8192;
constexpr int CH_LDS_PW = CH_LDS_RB + 4096;
constexpr int CH_LDS_SIZE = CH_LDS_PW + 4096;

#define D1 5
#define D2 0

DI void chain_role(char* __restrict__ ws, char* lds, const rsrc_t rs) {
    const int c = blockIdx.x >> 3;
    const int tid = threadIdx.x, wave = __builtin_amdgcn_readfirstlane(tid >> 6), lane = tid & 63;
    const int i = lane & 15, g = lane >> 4;
    const int h = c >> 1, half = c & 1;
    const bool hasB = c < NT16;
    const int tB = hasB ? c : 0;
    unsigned* tmo = (unsigned*)(ws + OFF_TMO);
    const float cs = 0.0625f * 1.4426950408889634f;

    bf16x8 wa[3][KK], wsh[7], wb[2][KK];
    {
        const char* ap = ws + OFF_ANF + (size_t)(h * 28 + 14 * half) * KK * KB + lane * 16;
#pragma unroll
        for (int t = 0; t < 3; ++t)
#pragma unroll
            for (int kk = 0; kk < KK; ++kk) wa[t][kk] = ldfrag(ap + (size_t)((3 * wave + t) * KK + kk) * KB);
#pragma unroll
        for (int k = 0; k < 7; ++k) wsh[k] = ldfrag(ap + (size_t)((12 + (wave >> 1)) * KK + 7 * (wave & 1) + k) * KB);
        const char* bp = ws + OFF_WF + (size_t)(448 + tB) * KK * KB + lane * 16;
#pragma unroll
        for (int hh = 0; hh < 2; ++hh)
#pragma unroll
            for (int kk = 0; kk < KK; ++kk) wb[hh][kk] = ldfrag(bp + (size_t)((4 * wave + hh) * 28 * KK + kk) * KB);
#pragma unroll
        for (int hh = 0; hh < 2; ++hh)
#pragma unroll
            for (int kk = 0; kk < KK; ++kk)
                *(bf16x8*)(lds + CH_LDS_B + (size_t)((wave * 2 + hh) * KK + kk) * KB + lane * 16) = ldfrag(bp + (size_t)((4 * wave + 2 + hh) * 28 * KK + kk) * KB);
    }
    const int gk0 = wave < 2 ? 4 * wave : 8 + 3 * (wave - 2);
    const int gkn = wave < 2 ? 4 : 3;
    bf16x8 gf[4], uf[4];
    wait_cnt(ws, CNT_G + 0, 224);
    __syncthreads();
    {
        const size_t gp = OFF_GTF + (((size_t)h * NRT + half) * KK + gk0) * KB + lane * 16;
#pragma unroll
        for (int k = 0; k < 4; ++k) gf[k] = ld8c(rs, gp + (size_t)(k < gkn ? k : 0) * KB);
    }
    const char* mp = lds + CH_LDS_MEM + g * 128 + (i & 7) * 16;
    const int hh2 = lane >> 4, r2 = (lane >> 1) & 7, q2 = lane & 1;

    for (int s = 0; s < NSEG - 1; ++s) {
        STAMP(0);
        {
            u32x4 x0, x1;
            unsigned spins = 0;
            const size_t o0 = OFF_MEMF + (size_t)(s * 7168 + tid * 16), o1 = o0 + (tid < 192 ? 4096 : 0);
            const size_t oc = OFF_CNT + (size_t)(CNT_G + s + 1) * 128;
            for (int d = 0; d < D1; ++d) __builtin_amdgcn_s_sleep(1);
            for (;;) {
                asm volatile("" ::: "memory");
                x0 = ld4c(rs, o0);
                x1 = ld4c(rs, o1);
                unsigned cv = ld1c(rs, oc);
                if (__all((max(umax4(x0), umax4(x1)) != SENT) && (cv >= 224u))) break;
                if (spin_fail(spins, tmo, 0x100u + s)) break;
            }
            const int t0 = tid >> 4, rr = (tid >> 1) & 7, gp = tid & 1;
            char* d0 = lds + CH_LDS_MEM + (t0 >> 1) * 512 + gp * 256 + rr * 16 + (t0 & 1) * 8;
            *(u32x2*)(d0) = (u32x2){x0.x, x0.y};
            *(u32x2*)(d0 + 128) = (u32x2){x0.z, x0.w};
            if (tid < 192) {
                *(u32x2*)(d0 + 8 * 512) = (u32x2){x1.x, x1.y};
                *(u32x2*)(d0 + 8 * 512 + 128) = (u32x2){x1.z, x1.w};
            }
        }
        {
            const size_t up = OFF_UF + (((size_t)s * NT16 + tB) * NH + 4 * wave) * KB + lane * 16;
#pragma unroll
            for (int k = 0; k < 4; ++k) uf[k] = ld8c(rs, up + (size_t)k * KB);
        }
        STAMP(1);
        __syncthreads();
        STAMP(2);
        bf16x8 mf[KK];
#pragma unroll
        for (int kk = 0; kk < KK; ++kk) mf[kk] = *(const bf16x8*)(mp + kk * 512);
        {
            bf16x8 mg[4];
            bf16x4 mk[4];
#pragma unroll
            for (int k = 0; k < 4; ++k) mg[k] = *(const bf16x8*)(mp + (gk0 + (k < gkn ? k : 0)) * 512);
#pragma unroll
            for (int t = 0; t < 4; ++t) {
                const int tl = (t < 3) ? 3 * wave + t : 12 + (wave >> 1);
                const int tg = 14 * half + tl;
                mk[t] = *(const bf16x4*)(mp + (tg >> 1) * 512 + (tg & 1) * 8);
            }
            f32x4 ta[4];
            f32x4 sx, sm;
            if (wave & 1) {
                mfma_Aa0(ta[3], wsh[0], mf[7]);
#pragma unroll
                for (int k = 1; k < 7; ++k) mfma_Aa(ta[3], wsh[k], mf[7 + k]);
            } else {
                mfma_Aa0(ta[3], wsh[0], mf[0]);
#pragma unroll
                for (int k = 1; k < 7; ++k) mfma_Aa(ta[3], wsh[k], mf[k]);
            }
            mfma_Aa0(ta[0], wa[0][0], mf[0]);
#pragma unroll
            for (int kk = 1; kk < KK; ++kk) mfma_Aa(ta[0], wa[0][kk], mf[kk]);
            mfma_Aa0(ta[1], wa[1][0], mf[0]);
#pragma unroll
            for (int kk = 1; kk < 4; ++kk) mfma_Aa(ta[1], wa[1][kk], mf[kk]);
            pin(ta[3]); pin(ta[0]);
            mfma16_v0(sm, mk[3], pack4(ta[3]));
            mfma16_v(sm, mk[0], pack4(ta[0]));
#pragma unroll
            for (int kk = 4; kk < KK; ++kk) mfma_Aa(ta[1], wa[1][kk], mf[kk]);
            mfma_Aa0(ta[2], wa[2][0], mf[0]);
#pragma unroll
            for (int kk = 1; kk < 4; ++kk) mfma_Aa(ta[2], wa[2][kk], mf[kk]);
            pin(ta[1]);
            mfma16_v(sm, mk[1], pack4(ta[1]));
#pragma unroll
            for (int kk = 4; kk < KK; ++kk) mfma_Aa(ta[2], wa[2][kk], mf[kk]);
            mfma_v0(sx, gf[0], mg[0]);
            mfma_v(sx, gf[1], mg[1]);
            mfma_v(sx, gf[2], mg[2]);
            if (wave < 2) mfma_v(sx, gf[3], mg[3]);
            pin(ta[2]);
            mfma16_v(sm, mk[2], pack4(ta[2]));
            mfma_fence4(sm, sx, ta[0], ta[1]);
            *(f32x4*)(lds + CH_LDS_RA + (wave * 2 + 0) * 1024 + lane * 16) = sx;
            *(f32x4*)(lds + CH_LDS_RA + (wave * 2 + 1) * 1024 + lane * 16) = sm;
        }
        STAMP(3);
        __syncthreads();
        const size_t e2w = OFF_SMX + (size_t)s * E2S + (size_t)c * E2B;
        if (wave < 2) {
            f32x4 v0 = *(const f32x4*)(lds + CH_LDS_RA + (0 + wave) * 1024 + lane * 16);
            f32x4 v1 = *(const f32x4*)(lds + CH_LDS_RA + (2 + wave) * 1024 + lane * 16);
            f32x4 v2 = *(const f32x4*)(lds + CH_LDS_RA + (4 + wave) * 1024 + lane * 16);
            f32x4 v3 = *(const f32x4*)(lds + CH_LDS_RA + (6 + wave) * 1024 + lane * 16);
            f32x4 v = (v0 + v1) + (v2 + v3);
            if (wave == 0) {
                float m = fmaxf(fmaxf(v[0], v[1]), fmaxf(v[2], v[3]));
                m = xmax16(m); m = xmax32(m);
                const float mc = m * cs;
                float e0 = fexp2(v[0] * cs - mc), e1 = fexp2(v[1] * cs - mc), e2 = fexp2(v[2] * cs - mc), e3 = fexp2(v[3] * cs - mc);
                u32x2 pk = {pack2(e0, e1), pack2(e2, e3)};
                if (i < 8) st2c(rs, e2w + i * 32 + g * 8, pk);
                float sum = (bf_lo(pk.x) + bf_hi(pk.x)) + (bf_lo(pk.y) + bf_hi(pk.y));
                sum = xadd16(sum); sum = xadd32(sum);
                u32x2 st = {__float_as_uint(m), __float_as_uint(sum)};
                if (lane < 8) st2c(rs, e2w + 512 + i * 8, st);
            } else {
                if (i < 8 && g < 2) st4c(rs, e2w + 256 + i * 32 + g * 16, __builtin_bit_cast(u32x4, v));
            }
        }
        {
            const size_t gp = OFF_GTF + (((size_t)h * NRT + 2 * (s + 1) + half) * KK + gk0) * KB + lane * 16;
#pragma unroll
            for (int k = 0; k < 4; ++k) gf[k] = ld8c(rs, gp + (size_t)(k < gkn ? k : 0) * KB);
        }
        if (hasB) {
            f32x4 ya[4];
            const char* lb = lds + CH_LDS_B + (size_t)(wave * 2) * KK * KB + lane * 16;
            mfma_Ba0(ya[0], mf[0], wb[0][0]);
            mfma_v0(ya[1], mf[0], wb[1][0]);
#pragma unroll
            for (int k = 1; k < KK; ++k) {
                mfma_Ba(ya[0], mf[k], wb[0][k]);
                mfma_v(ya[1], mf[k], wb[1][k]);
            }
            STAMP(4);
            u32x4 qx[4], qm0, qm1, qs;
            const size_t e2r = OFF_SMX + (size_t)s * E2S;
            const size_t ox = e2r + (size_t)(8 * wave) * E2B + (i & 7) * 32 + g * 8;
            const size_t om = e2r + (size_t)(8 * wave + 2 * hh2) * E2B + 256 + r2 * 32 + q2 * 16;
            const size_t os = e2r + (size_t)(8 * wave + 2 * hh2) * E2B + 512 + r2 * 8;
            {
                bf16x8 lq[2][4];
#pragma unroll
                for (int k = 0; k < 2; ++k) { lq[0][2 * k] = *(const bf16x8*)(lb + (size_t)k * KB); lq[0][2 * k + 1] = *(const bf16x8*)(lb + (size_t)(KK + k) * KB); }
#pragma unroll
                for (int gq = 0; gq < 7; ++gq) {
                    if (gq < 6) {
#pragma unroll
                        for (int k = 0; k < 2; ++k) {
                            lq[(gq + 1) & 1][2 * k] = *(const bf16x8*)(lb + (size_t)(2 * gq + 2 + k) * KB);
                            lq[(gq + 1) & 1][2 * k + 1] = *(const bf16x8*)(lb + (size_t)(KK + 2 * gq + 2 + k) * KB);
                        }
                    }
                    __builtin_amdgcn_sched_barrier(0);
                    if (gq == 0) { mfma_v0(ya[2], mf[0], lq[0][0]); mfma_v0(ya[3], mf[0], lq[0][1]); }
                    else { mfma_v(ya[2], mf[2 * gq], lq[gq & 1][0]); mfma_v(ya[3], mf[2 * gq], lq[gq & 1][1]); }
                    mfma_v(ya[2], mf[2 * gq + 1], lq[gq & 1][2]); mfma_v(ya[3], mf[2 * gq + 1], lq[gq & 1][3]);
                }
            }
            mfma_fence4(ya[0], ya[1], ya[2], ya[3]);
            bf16x8 yp0 = pack8(ya[0], ya[1]), yp1 = pack8(ya[2], ya[3]);
            __builtin_amdgcn_sched_barrier(0);
            {
                unsigned spins = 0;
                for (int d = 0; d < D2; ++d) __builtin_amdgcn_s_sleep(1);
                for (;;) {
                    asm volatile("" ::: "memory");
#pragma unroll
                    for (int hh = 0; hh < 4; ++hh) {
                        u32x2 lo_ = ld2c(rs, ox + (size_t)(2 * hh) * E2B), hi_ = ld2c(rs, ox + (size_t)(2 * hh + 1) * E2B);
                        qx[hh] = (u32x4){lo_.x, lo_.y, hi_.x, hi_.y};
                    }
                    qm0 = ld4c(rs, om);
                    qm1 = ld4c(rs, om + E2B);
                    { u32x2 lo_ = ld2c(rs, os), hi_ = ld2c(rs, os + E2B); qs = (u32x4){lo_.x, lo_.y, hi_.x, hi_.y}; }
                    unsigned mx = max(max(max(umax4(qx[0]), umax4(qx[1])), max(umax4(qx[2]), umax4(qx[3]))), max(max(umax4(qm0), umax4(qm1)), umax4(qs)));
                    if (__all(mx != SENT)) break;
                    if (spin_fail(spins, tmo, 0x200u + s)) break;
                }
            }
            STAMP(5);
            f32x4 dd[8];
#pragma unroll
            for (int hh = 0; hh < 4; ++hh) {
                const u32x4 u = __builtin_bit_cast(u32x4, uf[hh]);
                u32x2 ulo = {u.x, u.y}, uhi = {u.z, u.w}, elo = {qx[hh].x, qx[hh].y}, ehi = {qx[hh].z, qx[hh].w};
                mfma16_v0(dd[2 * hh], __builtin_bit_cast(bf16x4, ulo), __builtin_bit_cast(bf16x4, elo));
                mfma16_v0(dd[2 * hh + 1], __builtin_bit_cast(bf16x4, uhi), __builtin_bit_cast(bf16x4, ehi));
            }
            f32x4 ms = __builtin_bit_cast(f32x4, qm0) + __builtin_bit_cast(f32x4, qm1);
            const float m0 = __uint_as_float(qs.x), s0 = __uint_as_float(qs.y), m1 = __uint_as_float(qs.z), s1 = __uint_as_float(qs.w);
            float mx = fmaxf(fmaxf(ms[0], ms[1]), fmaxf(ms[2], ms[3]));
            mx = fmaxf(mx, qswap(mx));
            const float Mc = fmaxf(mx, fmaxf(m0, m1)) * cs;
            float e0 = fexp2(ms[0] * cs - Mc), e1 = fexp2(ms[1] * cs - Mc), e2 = fexp2(ms[2] * cs - Mc), e3 = fexp2(ms[3] * cs - Mc);
            float f0 = fexp2(m0 * cs - Mc), f1 = fexp2(m1 * cs - Mc);
            float sm = (e0 + e1) + (e2 + e3);
            sm += qswap(sm);
            const float inv = __builtin_amdgcn_rcpf(sm + f0 * s0 + f1 * s1);
            char* pw = lds + CH_LDS_PW + wave * 1024;
            *(u32x2*)(pw + ((hh2 * 8 + r2) * 2 + q2) * 8) = (u32x2){pack2(e0 * inv, e1 * inv), pack2(e2 * inv, e3 * inv)};
            if (q2 == 0) *(u32x2*)(pw + 512 + (r2 * 4 + hh2) * 8) = (u32x2){__float_as_uint(f0 * inv), __float_as_uint(f1 * inv)};
            __builtin_amdgcn_fence(__ATOMIC_RELEASE, "wavefront");
            __builtin_amdgcn_wave_barrier();
            __builtin_amdgcn_fence(__ATOMIC_ACQUIRE, "wavefront");
            u32x2 pa[4];
#pragma unroll
            for (int hh = 0; hh < 4; ++hh) pa[hh] = *(const u32x2*)(pw + ((hh * 8 + (i & 7)) * 2 + (g & 1)) * 8);
            f32x4 fa = *(const f32x4*)(pw + 512 + (i & 7) * 32), fb = *(const f32x4*)(pw + 512 + (i & 7) * 32 + 16);
            if (g >= 2) {
#pragma unroll
                for (int hh = 0; hh < 4; ++hh) pa[hh] = (u32x2){0u, 0u};
            }
            mfma_fence4(dd[0], dd[1], dd[2], dd[3]);
            mfma_fence4(dd[4], dd[5], dd[6], dd[7]);
            f32x4 acc = dd[0] * fa[0] + dd[1] * fa[1] + dd[2] * fa[2] + dd[3] * fa[3] + dd[4] * fb[0] + dd[5] * fb[1] + dd[6] * fb[2] + dd[7] * fb[3];
            u32x4 p01 = {pa[0].x, pa[0].y, pa[1].x, pa[1].y}, p23 = {pa[2].x, pa[2].y, pa[3].x, pa[3].y};
            mfma_v(acc, yp0, __builtin_bit_cast(bf16x8, p01));
            mfma_v(acc, yp1, __builtin_bit_cast(bf16x8, p23));
            mfma_fence1(acc);
            *(f32x4*)(lds + CH_LDS_RB + wave * 1024 + lane * 16) = acc;
        }
        STAMP(6);
        __syncthreads();
        if (hasB && wave == 0) {
            f32x4 v0 = *(const f32x4*)(lds + CH_LDS_RB + lane * 16);
            f32x4 v1 = *(const f32x4*)(lds + CH_LDS_RB + 1024 + lane * 16);
            f32x4 v2 = *(const f32x4*)(lds + CH_LDS_RB + 2048 + lane * 16);
            f32x4 v3 = *(const f32x4*)(lds + CH_LDS_RB + 3072 + lane * 16);
            f32x4 v = (v0 + v1) + (v2 + v3);
            if (i < 8) {
                u32x2 pk = {pack2(v[0], v[1]), pack2(v[2], v[3])};
                st2c(rs, OFF_MEMF + (size_t)((s + 1) * 7168 + tB * 256 + i * 32 + g * 8), pk);
            }
        }
    }
}

template <int TYPE>
DI void w1_phase(char* __restrict__ ws, const rsrc_t rs, int tile0, int wave, int lane) {
    bf16x8 w[4][KK];
    {
        const char* wp = ws + OFF_WF + (size_t)tile0 * KK * KB + lane * 16;
#pragma unroll
        for (int t = 0; t < 4; ++t)
#pragma unroll
            for (int kk = 0; kk < KK; ++kk) w[t][kk] = ldfrag(wp + (size_t)(t * KK + kk) * KB);
    }
    const int h = (tile0 - 448 * TYPE) / 28, t0 = (tile0 - 448 * TYPE) % 28;
    bf16x8 xa[KK], xb[KK];
    {
        const char* xp = ws + OFF_XNF + (size_t)(2 * wave) * KK * KB + lane * 16;
#pragma unroll
        for (int kk = 0; kk < KK; ++kk) xa[kk] = ldfrag(xp + (size_t)kk * KB);
    }
    for (int s = wave; s < NSEG; s += 4) {
        f32x4 acc[2][4];
        {
            const char* xp = ws + OFF_XNF + (size_t)(2 * s + 1) * KK * KB + lane * 16;
#pragma unroll
            for (int kk = 0; kk < KK; ++kk) xb[kk] = ldfrag(xp + (size_t)kk * KB);
        }
        __builtin_amdgcn_sched_barrier(0);
#pragma unroll
        for (int t = 0; t < 4; ++t) { if (TYPE == 0) mfma_Aa0(acc[0][t], w[t][0], xa[0]); else mfma_Ba0(acc[0][t], xa[0], w[t][0]); }
#pragma unroll
        for (int kk = 1; kk < KK; ++kk)
#pragma unroll
            for (int t = 0; t < 4; ++t) { if (TYPE == 0) mfma_Aa(acc[0][t], w[t][kk], xa[kk]); else mfma_Ba(acc[0][t], xa[kk], w[t][kk]); }
        __builtin_amdgcn_sched_barrier(0);
        {
            const int sn = (s + 4 < NSEG) ? s + 4 : s;
            const char* xp = ws + OFF_XNF + (size_t)(2 * sn) * KK * KB + lane * 16;
#pragma unroll
            for (int kk = 0; kk < KK; ++kk) xa[kk] = ldfrag(xp + (size_t)kk * KB);
        }
        __builtin_amdgcn_sched_barrier(0);
#pragma unroll
        for (int t = 0; t < 4; ++t) { if (TYPE == 0) mfma_Aa0(acc[1][t], w[t][0], xb[0]); else mfma_Ba0(acc[1][t], xb[0], w[t][0]); }
#pragma unroll
        for (int kk = 1; kk < KK; ++kk)
#pragma unroll
            for (int t = 0; t < 4; ++t) { if (TYPE == 0) mfma_Aa(acc[1][t], w[t][kk], xb[kk]); else mfma_Ba(acc[1][t], xb[kk], w[t][kk]); }
        mfma_fence4(acc[0][0], acc[0][1], acc[0][2], acc[0][3]);
        mfma_fence4(acc[1][0], acc[1][1], acc[1][2], acc[1][3]);
        if (TYPE == 0) {
#pragma unroll
            for (int q = 0; q < 2; ++q)
#pragma unroll
                for (int p = 0; p < 2; ++p)
                    st8c(rs, OFF_GTF + (((size_t)h * NRT + 2 * s + q) * KK + (t0 >> 1) + p) * KB + lane * 16, pack8(acc[q][2 * p], acc[q][2 * p + 1]));
        } else {
#pragma unroll
            for (int t = 0; t < 4; ++t)
                st8c(rs, OFF_UF + (((size_t)s * NT16 + t0 + t) * NH + h) * KB + lane * 16, pack8(acc[0][t], acc[1][t]));
        }
        signal_cnt(ws, CNT_G + s, lane);
    }
}

DI void ffn_task(char* __restrict__ ws, char* lds, const rsrc_t rs, const unsigned short* __restrict__ x, const unsigned short* __restrict__ b1,
                 const unsigned short* __restrict__ b2, int s, int wave, int lane) {
    const int i = lane & 15, g = lane >> 4;
    {
        bf16x8 xf[2][KK];
        const char* xp = ws + OFF_XNF + (size_t)(2 * s) * KK * KB + lane * 16;
#pragma unroll
        for (int q = 0; q < 2; ++q)
#pragma unroll
            for (int kk = 0; kk < KK; ++kk) xf[q][kk] = ldfrag(xp + (size_t)(q * KK + kk) * KB);
        const char* wp = ws + OFF_WF + (size_t)(896 + 28 * wave) * KK * KB + lane * 16;
        for (int pr = 0; pr < 14; ++pr) {
            bf16x8 wf[2][KK];
#pragma unroll
            for (int t = 0; t < 2; ++t)
#pragma unroll
                for (int kk = 0; kk < KK; ++kk) wf[t][kk] = ldfrag(wp + (size_t)((2 * pr + t) * KK + kk) * KB);
            f32x4 acc[2][2];
#pragma unroll
            for (int q = 0; q < 2; ++q)
#pragma unroll
                for (int t = 0; t < 2; ++t) acc[q][t] = (f32x4){0.f, 0.f, 0.f, 0.f};
#pragma unroll
            for (int kk = 0; kk < KK; ++kk)
#pragma unroll
                for (int q = 0; q < 2; ++q)
#pragma unroll
                    for (int t = 0; t < 2; ++t) acc[q][t] = mfma32(wf[t][kk], xf[q][kk], acc[q][t]);
#pragma unroll
            for (int t = 0; t < 2; ++t) {
                u32x2 bb = *(const u32x2*)(b1 + 16 * (28 * wave + 2 * pr + t) + 4 * g);
                float bv[4] = {bf_lo(bb.x), bf_hi(bb.x), bf_lo(bb.y), bf_hi(bb.y)};
#pragma unroll
                for (int q = 0; q < 2; ++q)
#pragma unroll
                    for (int r = 0; r < 4; ++r) acc[q][t][r] = gelu_tanh(acc[q][t][r] + bv[r]);
            }
#pragma unroll
            for (int q = 0; q < 2; ++q)
                *(bf16x8*)(lds + (size_t)(q * KF + 14 * wave + pr) * KB + lane * 16) = pack8(acc[q][0], acc[q][1]);
        }
    }
    __syncthreads();
    {
        f32x4 acc[2][7];
#pragma unroll
        for (int q = 0; q < 2; ++q)
#pragma unroll
            for (int t = 0; t < 7; ++t) acc[q][t] = (f32x4){0.f, 0.f, 0.f, 0.f};
        const char* wp = ws + OFF_W2F + (size_t)(7 * wave) * KF * KB + lane * 16;
#pragma unroll 4
        for (int kf = 0; kf < KF; ++kf) {
            bf16x8 h0 = *(const bf16x8*)(lds + (size_t)kf * KB + lane * 16), h1 = *(const bf16x8*)(lds + (size_t)(KF + kf) * KB + lane * 16);
#pragma unroll
            for (int t = 0; t < 7; ++t) {
                bf16x8 wf = ldfrag(wp + (size_t)(t * KF + kf) * KB);
                acc[0][t] = mfma32(wf, h0, acc[0][t]);
                acc[1][t] = mfma32(wf, h1, acc[1][t]);
            }
        }
#pragma unroll
        for (int t = 0; t < 7; ++t) {
            const int tc = 7 * wave + t;
            u32x2 bb = *(const u32x2*)(b2 + 16 * tc + 4 * g);
#pragma unroll
            for (int q = 0; q < 2; ++q) {
                const size_t o = (size_t)(32 * s + 16 * q + i) * DM + 16 * tc + 4 * g;
                u32x2 xv = *(const u32x2*)(x + o);
                f32x4 r = {acc[q][t][0] + bf_lo(bb.x) + bf_lo(xv.x), acc[q][t][1] + bf_hi(bb.x) + bf_hi(xv.x), acc[q][t][2] + bf_lo(bb.y) + bf_lo(xv.y), acc[q][t][3] + bf_hi(bb.y) + bf_hi(xv.y)};
                st4c(rs, OFF_FO + o * 4, __builtin_bit_cast(u32x4, r));
            }
        }
        signal_cnt(ws, CNT_F + s, lane);
    }
    __syncthreads();
}

DI void w3_task(char* __restrict__ ws, const rsrc_t rs, int tile0, int sp, int lane) {
    const int i = lane & 15, g = lane >> 4;
    bf16x8 w[4][KK];
    {
        const char* wp = ws + OFF_WF + (size_t)tile0 * KK * KB + lane * 16;
#pragma unroll
        for (int t = 0; t < 4; ++t)
#pragma unroll
            for (int kk = 0; kk < KK; ++kk) w[t][kk] = ldfrag(wp + (size_t)(t * KK + kk) * KB);
    }
    bf16x8 xf[KK];
    {
        const size_t mp = OFF_MEMF + (size_t)(2 * sp + (i >> 3)) * 7168 + (i & 7) * 32 + g * 8;
        unsigned spins = 0;
        for (;;) {
            asm volatile("" ::: "memory");
            unsigned mx = 0;
#pragma unroll
            for (int kk = 0; kk < KK; ++kk) {
                u32x2 lo_ = ld2c(rs, mp + kk * 512), hi_ = ld2c(rs, mp + kk * 512 + 256);
                u32x4 xx_ = {lo_.x, lo_.y, hi_.x, hi_.y};
                mx = max(mx, umax4(xx_));
                xf[kk] = __builtin_bit_cast(bf16x8, xx_);
            }
            if (__all(mx != SENT)) break;
            __builtin_amdgcn_s_sleep(32);
            if (spin_fail(spins, (unsigned*)(ws + OFF_TMO), 0x500u + sp)) break;
        }
    }
    f32x4 acc[4];
#pragma unroll
    for (int t = 0; t < 4; ++t) acc[t] = (f32x4){0.f, 0.f, 0.f, 0.f};
    if (tile0 < 448) {
#pragma unroll
        for (int kk = 0; kk < KK; ++kk)
#pragma unroll
            for (int t = 0; t < 4; ++t) acc[t] = mfma32(w[t][kk], xf[kk], acc[t]);
        const int h = tile0 / 28, t0 = tile0 % 28;
#pragma unroll
        for (int p = 0; p < 2; ++p)
            st8c(rs, OFF_T2F + ((size_t)h * NSEG + 2 * sp + (i >> 3)) * 7168 + ((t0 >> 1) + p) * 512 + g * 128 + (i & 7) * 16, pack8(acc[2 * p], acc[2 * p + 1]));
    } else {
#pragma unroll
        for (int kk = 0; kk < KK; ++kk)
#pragma unroll
            for (int t = 0; t < 4; ++t) acc[t] = mfma32(xf[kk], w[t][kk], acc[t]);
        const int h = (tile0 - 448) / 28, t0 = (tile0 - 448) % 28;
#pragma unroll
        for (int t = 0; t < 4; ++t)
            st2c(rs, OFF_YF + (((size_t)(2 * sp + (g >> 1)) * NT16 + t0 + t) * 8 + (h >> 1)) * 512 + (g & 1) * 256 + i * 16 + (h & 1) * 8, __builtin_bit_cast(u32x2, pack4(acc[t])));
    }
    signal_cnt(ws, CNT_T + sp, lane);
}

DI void f1_task(char* __restrict__ ws, const rsrc_t rs, int s, int h, int lane) {
    const int i = lane & 15, g = lane >> 4;
    wait_cnt(ws, CNT_T + (s >> 1), 224);
    f32x4 sc[2][3];
#pragma unroll
    for (int q = 0; q < 2; ++q)
#pragma unroll
        for (int k = 0; k < 3; ++k) sc[q][k] = (f32x4){0.f, 0.f, 0.f, 0.f};
    const char* xp = ws + OFF_XNF + (size_t)(2 * s) * KK * KB + lane * 16;
    const size_t gp = OFF_GTF + ((size_t)h * NRT + 2 * s) * KK * KB + lane * 16;
    const size_t tp = OFF_T2F + ((size_t)h * NSEG + s) * 7168 + g * 128 + (i & 7) * 16;
    bf16x8 g0[KK], g1[KK], t2[KK];
#pragma unroll
    for (int kk = 0; kk < KK; ++kk) { g0[kk] = ld8c(rs, gp + (size_t)kk * KB); g1[kk] = ld8c(rs, gp + (size_t)(KK + kk) * KB); t2[kk] = ld8c(rs, tp + kk * 512); }
#pragma unroll
    for (int kk = 0; kk < KK; ++kk) {
#pragma unroll
        for (int q = 0; q < 2; ++q) {
            bf16x8 xf = ldfrag(xp + (size_t)(q * KK + kk) * KB);
            sc[q][0] = mfma32(g0[kk], xf, sc[q][0]);
            sc[q][1] = mfma32(g1[kk], xf, sc[q][1]);
            sc[q][2] = mfma32(t2[kk], xf, sc[q][2]);
        }
    }
    const float cs = 0.0625f * 1.4426950408889634f;
    const float NEG = -3.0e38f;
#pragma unroll
    for (int q = 0; q < 2; ++q) {
        const int qi = 16 * q + i;
        float xs[8], ms[4];
#pragma unroll
        for (int j = 0; j < 8; ++j) {
            const int key = 16 * (j >> 2) + 4 * g + (j & 3);
            xs[j] = (key <= qi) ? sc[q][j >> 2][j & 3] : NEG;
        }
#pragma unroll
        for (int j = 0; j < 4; ++j) ms[j] = (g < 2) ? sc[q][2][j] : NEG;
        float m = fmaxf(fmaxf(fmaxf(xs[0], xs[1]), fmaxf(xs[2], xs[3])), fmaxf(fmaxf(xs[4], xs[5]), fmaxf(xs[6], xs[7])));
        m = fmaxf(m, fmaxf(fmaxf(ms[0], ms[1]), fmaxf(ms[2], ms[3])));
        m = xmax16(m); m = xmax32(m);
        const float mc = m * cs;
        float sum = 0.f;
#pragma unroll
        for (int j = 0; j < 8; ++j) { const int key = 16 * (j >> 2) + 4 * g + (j & 3); xs[j] = (key <= qi) ? fexp2(xs[j] * cs - mc) : 0.f; sum += xs[j]; }
#pragma unroll
        for (int j = 0; j < 4; ++j) { ms[j] = (g < 2) ? fexp2(ms[j] * cs - mc) : 0.f; sum += ms[j]; }
        sum = xadd16(sum); sum = xadd32(sum);
        const float inv = 1.f / sum;
        u32x4 px = {pack2(xs[0] * inv, xs[1] * inv), pack2(xs[2] * inv, xs[3] * inv), pack2(xs[4] * inv, xs[5] * inv), pack2(xs[6] * inv, xs[7] * inv)};
        st4c(rs, OFF_PXF + (((size_t)s * 2 + q) * NH + h) * KB + lane * 16, px);
        u32x2 pmv = {pack2(ms[0] * inv, ms[1] * inv), pack2(ms[2] * inv, ms[3] * inv)};
        st2c(rs, OFF_PMF + (((size_t)s * 2 + q) * 8 + (h >> 1)) * KB + lane * 16 + (h & 1) * 8, pmv);
    }
    signal_cnt(ws, CNT_P + s, lane);
}

DI void f2_task(char* __restrict__ ws, const rsrc_t rs, unsigned short* __restrict__ out, int s, int tg, int lane) {
    const int i = lane & 15, g = lane >> 4;
    wait_cnt(ws, CNT_P + s, 16);
    wait_cnt(ws, CNT_F + s, 4);
    f32x4 acc[2][4];
#pragma unroll
    for (int q = 0; q < 2; ++q)
#pragma unroll
        for (int t = 0; t < 4; ++t)
            acc[q][t] = __builtin_bit_cast(f32x4, ld4c(rs, OFF_FO + ((size_t)(32 * s + 16 * q + i) * DM + 16 * (4 * tg + t) + 4 * g) * 4));
    {
        const size_t up = OFF_UF + ((size_t)s * NT16 + 4 * tg) * NH * KB + lane * 16;
        const size_t pp = OFF_PXF + (size_t)s * 2 * NH * KB + lane * 16;
#pragma unroll 4
        for (int h = 0; h < NH; ++h) {
            bf16x8 p0 = ld8c(rs, pp + (size_t)h * KB), p1 = ld8c(rs, pp + (size_t)(NH + h) * KB);
#pragma unroll
            for (int t = 0; t < 4; ++t) {
                bf16x8 a = ld8c(rs, up + (size_t)(t * NH + h) * KB);
                acc[0][t] = mfma32(a, p0, acc[0][t]);
                acc[1][t] = mfma32(a, p1, acc[1][t]);
            }
        }
    }
    {
        const size_t yp = OFF_YF + ((size_t)s * NT16 + 4 * tg) * 8 * 512 + (g & 1) * 256 + i * 16;
        const size_t pp = OFF_PMF + (size_t)s * 2 * 8 * KB + lane * 16;
#pragma unroll 4
        for (int hp = 0; hp < 8; ++hp) {
            bf16x8 p0 = ld8c(rs, pp + (size_t)hp * KB), p1 = ld8c(rs, pp + (size_t)(8 + hp) * KB);
#pragma unroll
            for (int t = 0; t < 4; ++t) {
                bf16x8 a = ld8c(rs, yp + (size_t)(t * 8 + hp) * 512);
                acc[0][t] = mfma32(a, p0, acc[0][t]);
                acc[1][t] = mfma32(a, p1, acc[1][t]);
            }
        }
    }
#pragma unroll
    for (int q = 0; q < 2; ++q)
#pragma unroll
        for (int t = 0; t < 4; ++t) {
            const size_t o = (size_t)(32 * s + 16 * q + i) * DM + 16 * (4 * tg + t) + 4 * g;
            u32x2 r = {pack2(acc[q][t][0], acc[q][t][1]), pack2(acc[q][t][2], acc[q][t][3])};
            *(u32x2*)(out + o) = r;
        }
}

__global__ __launch_bounds__(256, 1) void k_mega(const unsigned short* __restrict__ x, const unsigned short* __restrict__ b1, const unsigned short* __restrict__ b2,
                                                 char* __restrict__ ws, unsigned short* __restrict__ out) {
    extern __shared__ __attribute__((aligned(16))) char lds[];
    const rsrc_t rs = __builtin_amdgcn_make_buffer_rsrc(ws, 0, (int)WS_END, 0x00020000);
    if ((blockIdx.x & 7) == 0) { chain_role(ws, lds, rs); return; }
    const int wid = (blockIdx.x >> 3) * 7 + (blockIdx.x & 7) - 1;
    const int wave = __builtin_amdgcn_readfirstlane(threadIdx.x >> 6), lane = threadIdx.x & 63;
    const int tile0 = wid * 4;
    if (tile0 < 448) w1_phase<0>(ws, rs, tile0, wave, lane);
    else w1_phase<1>(ws, rs, tile0, wave, lane);
    if (wid >= 160) ffn_task(ws, lds, rs, x, b1, b2, wid - 160, wave, lane);
    const int gw = wid * 4 + wave;
    for (int b = 0; b < 34; ++b) {
        if (b < 32 && (b & 3) == wave) w3_task(ws, rs, tile0, b, lane);
        if (b >= 1 && b <= 32) {
            const int k = (gw + 896 * 2 - (b - 1) * 32) % 896;
            if (k < 32) f1_task(ws, rs, 2 * (b - 1) + (k >> 4), k & 15, lane);
        }
        if (b >= 2) {
            const int k = (gw + 896 * 2 - 448 - (b - 2) * 14) % 896;
            if (k < 14) f2_task(ws, rs, out, 2 * (b - 2) + (k / 7), k % 7, lane);
        }
    }
}

extern "C" void kernel_launch(void* const* d_in, const int* in_sizes, int n_in,
                              void* d_out, int out_size, void* d_ws, size_t ws_size,
                              hipStream_t stream) {
    const unsigned short* x = (const unsigned short*)d_in[0];
    const unsigned short* mem0 = (const unsigned short*)d_in[1];
    const unsigned short* Wq = (const unsigned short*)d_in[2];
    const unsigned short* Wk = (const unsigned short*)d_in[3];
    const unsigned short* Wv = (const unsigned short*)d_in[4];
    const unsigned short* Wo = (const unsigned short*)d_in[5];
    const unsigned short* w1 = (const unsigned short*)d_in[8];
    const unsigned short* b1 = (const unsigned short*)d_in[9];
    const unsigned short* w2 = (const unsigned short*)d_in[10];
    const unsigned short* b2 = (const unsigned short*)d_in[11];
    char* ws = (char*)d_ws;
    unsigned short* out = (unsigned short*)d_out;
    (void)hipFuncSetAttribute((const void*)k_mega, hipFuncAttributeMaxDynamicSharedMemorySize, CH_LDS_SIZE);
    k_prep<<<PB_END, 256, 0, stream>>>(x, mem0, Wq, Wk, Wv, Wo, w1, w2, ws);
    k_mega<<<256, 256, CH_LDS_SIZE, stream>>>(x, b1, b2, ws, out);
}
```

```cpp
#include <hip/hip_runtime.h>
#include <hip/hip_bf16.h>

typedef __attribute__((ext_vector_type(8))) short bf16x8;
typedef __attribute__((ext_vector_type(4))) short bf16x4;
typedef __attribute__((ext_vector_type(4))) float f32x4;
typedef __attribute__((ext_vector_type(4))) unsigned u32x4;
typedef __attribute__((ext_vector_type(2))) unsigned u32x2;
typedef __attribute__((ext_vector_type(2))) __bf16 bf2_t;
#define DI __device__ __forceinline__

constexpr int DM = 448, KK = 14, NT16 = 28, NH = 16, NSEG = 64, NRT = 128, FFD = 1792, KF = 56;
constexpr size_t KB = 1024;
constexpr size_t OFF_XNF = 0;
constexpr size_t OFF_WF = OFF_XNF + (size_t)NRT * KK * KB;
constexpr size_t OFF_ANF = OFF_WF + (size_t)1008 * KK * KB;
constexpr size_t OFF_W2F = OFF_ANF + (size_t)448 * KK * KB;
constexpr size_t OFF_GTF = OFF_W2F + (size_t)28 * KF * KB;
constexpr size_t OFF_UF = OFF_GTF + (size_t)NH * NRT * KK * KB;
constexpr size_t OFF_FO = OFF_UF + (size_t)NSEG * NT16 * NH * KB;
constexpr size_t OFF_MEMF = OFF_FO + (size_t)2048 * DM * 4;
constexpr size_t OFF_SMX = OFF_MEMF + (size_t)NSEG * 7168;
constexpr int E2B = 640, E2S = 32 * E2B;
constexpr size_t OFF_T2F = OFF_SMX + (size_t)NSEG * E2S;
constexpr size_t OFF_YF = OFF_T2F + (size_t)NH * NSEG * 7168;
constexpr size_t OFF_PXF = OFF_YF + (size_t)NSEG * NT16 * 8 * 512;
constexpr size_t OFF_PMF = OFF_PXF + (size_t)NSEG * 2 * NH * KB;
constexpr size_t OFF_CNT = OFF_PMF + (size_t)NSEG * 2 * 8 * KB;
constexpr int CNT_G = 0, CNT_P = 64, CNT_F = 128, CNT_TMO = 192, CNT_PROG = 193, CNT_T2 = 200, CNT_Y = CNT_T2 + 32 * 16, CNT_N = CNT_Y + 32 * 7;
constexpr size_t OFF_TMO = OFF_CNT + (size_t)CNT_TMO * 128;
constexpr size_t WS_END = OFF_CNT + (size_t)CNT_N * 128;
constexpr size_t OFF_DBG = WS_END;
#define STAMP(k)

constexpr unsigned SENT = 0xFFFFFFFFu;
constexpr unsigned kSpinCap = 1u << 16;

DI unsigned pack2(float a, float b) { bf2_t v = {(__bf16)a, (__bf16)b}; return __builtin_bit_cast(unsigned, v); }
DI float bf_lo(unsigned u) { return __uint_as_float(u << 16); }
DI float bf_hi(unsigned u) { return __uint_as_float(u & 0xFFFF0000u); }
DI f32x4 mfma32(bf16x8 a, bf16x8 b, f32x4 c) { return __builtin_amdgcn_mfma_f32_16x16x32_bf16(a, b, c, 0, 0, 0); }
DI bf16x8 ldfrag(const char* p) { return *(const bf16x8*)p; }
DI bf16x8 pack8(f32x4 a, f32x4 b) {
    u32x4 r = {pack2(a[0], a[1]), pack2(a[2], a[3]), pack2(b[0], b[1]), pack2(b[2], b[3])};
    return __builtin_bit_cast(bf16x8, r);
}
DI bf16x4 pack4(f32x4 a) { u32x2 r = {pack2(a[0], a[1]), pack2(a[2], a[3])}; return __builtin_bit_cast(bf16x4, r); }
DI bool spin_fail(unsigned& spins, unsigned* tmo, unsigned code = 1u) {
    if (++spins > kSpinCap) { unsigned exp_ = 0u; __hip_atomic_compare_exchange_strong(tmo, &exp_, code, __ATOMIC_RELAXED, __ATOMIC_RELAXED, __HIP_MEMORY_SCOPE_AGENT); return true; }
    if ((spins & 63u) == 0u) return __builtin_amdgcn_readfirstlane(__hip_atomic_load(tmo, __ATOMIC_RELAXED, __HIP_MEMORY_SCOPE_AGENT)) != 0u;
    return false;
}
typedef __amdgpu_buffer_rsrc_t rsrc_t;
DI bf16x8 ld8c(rsrc_t rs, size_t off) { return __builtin_bit_cast(bf16x8, __builtin_amdgcn_raw_buffer_load_b128(rs, (unsigned)off, 0, 16)); }
DI u32x4 ld4c(rsrc_t rs, size_t off) { return __builtin_amdgcn_raw_buffer_load_b128(rs, (unsigned)off, 0, 16); }
DI u32x2 ld2c(rsrc_t rs, size_t off) { return __builtin_amdgcn_raw_buffer_load_b64(rs, (unsigned)off, 0, 16); }
DI unsigned ld1c(rsrc_t rs, size_t off) { return __builtin_amdgcn_raw_buffer_load_b32(rs, (unsigned)off, 0, 16); }
DI void st8c(rsrc_t rs, size_t off, bf16x8 v) { __builtin_amdgcn_raw_buffer_store_b128(__builtin_bit_cast(u32x4, v), rs, (unsigned)off, 0, 16); }
DI void st4c(rsrc_t rs, size_t off, u32x4 v) { __builtin_amdgcn_raw_buffer_store_b128(v, rs, (unsigned)off, 0, 16); }
DI void st2c(rsrc_t rs, size_t off, u32x2 v) { __builtin_amdgcn_raw_buffer_store_b64(v, rs, (unsigned)off, 0, 16); }
DI void signal_cnt(char* ws, int idx, int lane) {
    asm volatile("s_waitcnt vmcnt(0)" ::: "memory");
    if (lane == 0) __hip_atomic_fetch_add((unsigned*)(ws + OFF_CNT + (size_t)idx * 128), 1u, __ATOMIC_RELAXED, __HIP_MEMORY_SCOPE_AGENT);
}
DI void wait_cnt(char* ws, int idx, unsigned target) {
    unsigned spins = 0;
    unsigned* p = (unsigned*)(ws + OFF_CNT + (size_t)idx * 128);
    for (;;) {
        unsigned v = __builtin_amdgcn_readfirstlane(__hip_atomic_load(p, __ATOMIC_RELAXED, __HIP_MEMORY_SCOPE_AGENT));
        if (v >= target) break;
        __builtin_amdgcn_s_sleep(8);
        if (spin_fail(spins, (unsigned*)(ws + OFF_TMO), 0x300u + idx)) break;
    }
    asm volatile("" ::: "memory");
}

DI void wait_cnt_slow(char* ws, int idx, unsigned target) {
    unsigned spins = 0;
    unsigned* p = (unsigned*)(ws + OFF_CNT + (size_t)idx * 128);
    for (;;) {
        unsigned v = __builtin_amdgcn_readfirstlane(__hip_atomic_load(p, __ATOMIC_RELAXED, __HIP_MEMORY_SCOPE_AGENT));
        if (v >= target) break;
        if (v + 1 < target) __builtin_amdgcn_s_sleep(100); else __builtin_amdgcn_s_sleep(12);
        if (spin_fail(spins, (unsigned*)(ws + OFF_TMO), 0x400u + idx)) break;
    }
    asm volatile("" ::: "memory");
}

DI float gelu_tanh(float z) {
    float u = 0.7978845608028654f * (z + 0.044715f * z * z * z);
    float e = __expf(2.f * u);
    float t = 1.f - 2.f / (1.f + e);
    return 0.5f * z * (1.f + t);
}

DI float xmax16(float v) { auto r = __builtin_amdgcn_permlane16_swap(__float_as_uint(v), __float_as_uint(v), false, false); return fmaxf(__uint_as_float(r[0]), __uint_as_float(r[1])); }
DI float xmax32(float v) { auto r = __builtin_amdgcn_permlane32_swap(__float_as_uint(v), __float_as_uint(v), false, false); return fmaxf(__uint_as_float(r[0]), __uint_as_float(r[1])); }
DI float xadd16(float v) { auto r = __builtin_amdgcn_permlane16_swap(__float_as_uint(v), __float_as_uint(v), false, false); return __uint_as_float(r[0]) + __uint_as_float(r[1]); }
DI float xadd32(float v) { auto r = __builtin_amdgcn_permlane32_swap(__float_as_uint(v), __float_as_uint(v), false, false); return __uint_as_float(r[0]) + __uint_as_float(r[1]); }
DI float qswap(float v) { return __uint_as_float(__builtin_amdgcn_update_dpp(0, __float_as_uint(v), 0xB1, 0xF, 0xF, true)); }
DI unsigned umax4(u32x4 v) { return max(max(v.x, v.y), max(v.z, v.w)); }
DI float fexp2(float x) { return __builtin_amdgcn_exp2f(x); }

DI void mfma_Aa0(f32x4& acc, const bf16x8& w, const bf16x8& m) { asm volatile("s_nop 1\n\tv_mfma_f32_16x16x32_bf16 %0, %1, %2, 0" : "=&v"(acc) : "a"(w), "v"(m)); }
DI void mfma_Aa(f32x4& acc, const bf16x8& w, const bf16x8& m) { asm volatile("s_nop 1\n\tv_mfma_f32_16x16x32_bf16 %0, %1, %2, %0" : "+v"(acc) : "a"(w), "v"(m)); }
DI void mfma_Ba0(f32x4& acc, const bf16x8& m, const bf16x8& w) { asm volatile("s_nop 1\n\tv_mfma_f32_16x16x32_bf16 %0, %1, %2, 0" : "=&v"(acc) : "v"(m), "a"(w)); }
DI void mfma_Ba(f32x4& acc, const bf16x8& m, const bf16x8& w) { asm volatile("s_nop 1\n\tv_mfma_f32_16x16x32_bf16 %0, %1, %2, %0" : "+v"(acc) : "v"(m), "a"(w)); }
DI void mfma_v0(f32x4& acc, const bf16x8& a, const bf16x8& b) { asm volatile("s_nop 1\n\tv_mfma_f32_16x16x32_bf16 %0, %1, %2, 0" : "=&v"(acc) : "v"(a), "v"(b)); }
DI void mfma_v(f32x4& acc, const bf16x8& a, const bf16x8& b) { asm volatile("s_nop 1\n\tv_mfma_f32_16x16x32_bf16 %0, %1, %2, %0" : "+v"(acc) : "v"(a), "v"(b)); }
DI void mfma16_v0(f32x4& acc, const bf16x4& a, const bf16x4& b) { asm volatile("s_nop 1\n\tv_mfma_f32_16x16x16_bf16 %0, %1, %2, 0" : "=&v"(acc) : "v"(a), "v"(b)); }
DI void mfma16_v(f32x4& acc, const bf16x4& a, const bf16x4& b) { asm volatile("s_nop 1\n\tv_mfma_f32_16x16x16_bf16 %0, %1, %2, %0" : "+v"(acc) : "v"(a), "v"(b)); }
DI void mfma_fence4(f32x4& a, f32x4& b, f32x4& c, f32x4& d) { asm volatile("s_nop 7\n\ts_nop 1" : "+v"(a), "+v"(b), "+v"(c), "+v"(d)); }
DI void pin(f32x4& a) { asm volatile("" : "+v"(a)); }
DI void mfma_fence1(f32x4& a) { asm volatile("s_nop 7\n\ts_nop 1" : "+v"(a)); }

constexpr int PREP_GRID = 256, PREP_NT = 448;

DI void ln_row(const unsigned short* __restrict__ x, char* __restrict__ ws, int row, int lane, const u32x2 (&u)[2]) {
    float v[2][4];
#pragma unroll
    for (int it = 0; it < 2; ++it) { v[it][0] = bf_lo(u[it].x); v[it][1] = bf_hi(u[it].x); v[it][2] = bf_lo(u[it].y); v[it][3] = bf_hi(u[it].y); }
    float sum = 0.f;
#pragma unroll
    for (int it = 0; it < 2; ++it) sum += v[it][0] + v[it][1] + v[it][2] + v[it][3];
#pragma unroll
    for (int o = 32; o >= 1; o >>= 1) sum += __shfl_xor(sum, o);
    const float mean = sum * (1.f / DM);
    float sq = 0.f;
#pragma unroll
    for (int it = 0; it < 2; ++it) {
        if (lane + it * 64 < 112) {
#pragma unroll
            for (int j = 0; j < 4; ++j) { float d = v[it][j] - mean; sq += d * d; }
        }
    }
#pragma unroll
    for (int o = 32; o >= 1; o >>= 1) sq += __shfl_xor(sq, o);
    const float rstd = 1.f / sqrtf(sq * (1.f / DM) + 1e-5f);
    const int rt = row >> 4, ii = row & 15;
#pragma unroll
    for (int it = 0; it < 2; ++it) {
        int grp = lane + it * 64;
        if (grp < 112) {
            int d = grp * 4, kk = d >> 5, dd = d & 31, half = dd >> 4, gg = (dd & 15) >> 2;
            u32x2 o = {pack2((v[it][0] - mean) * rstd, (v[it][1] - mean) * rstd), pack2((v[it][2] - mean) * rstd, (v[it][3] - mean) * rstd)};
            *(u32x2*)(ws + OFF_XNF + ((size_t)(rt * KK + kk) * 64 + (ii + 16 * gg)) * 16 + half * 8) = o;
        }
    }
}

DI bf16x8 w12_load(const unsigned short* __restrict__ w1, const unsigned short* __restrict__ w2, int f, int i, int g) {
    bf16x8 v;
    if (f < 112 * KK) {
        int tf = f / KK, kk = f % KK;
#pragma unroll
        for (int j = 0; j < 8; ++j) v[j] = (short)w1[(size_t)(32 * kk + 16 * (j >> 2) + 4 * g + (j & 3)) * FFD + 16 * tf + i];
    } else {
        int f2 = f - 112 * KK;
        int t = f2 / KF, kf = f2 % KF;
#pragma unroll
        for (int j = 0; j < 8; ++j) v[j] = (short)w2[(size_t)(32 * kf + 16 * (j >> 2) + 4 * g + (j & 3)) * DM + 16 * t + i];
    }
    return v;
}
DI void w12_store(char* __restrict__ ws, int f, int lane, bf16x8 v) {
    if (f < 112 * KK) *(bf16x8*)(ws + OFF_WF + ((size_t)896 * KK + f) * KB + lane * 16) = v;
    else *(bf16x8*)(ws + OFF_W2F + (size_t)(f - 112 * KK) * KB + lane * 16) = v;
}

__global__ __launch_bounds__(PREP_NT, 2) void k_prep(const unsigned short* __restrict__ x, const unsigned short* __restrict__ mem0,
                                              const unsigned short* __restrict__ Wq, const unsigned short* __restrict__ Wk,
                                              const unsigned short* __restrict__ Wv, const unsigned short* __restrict__ Wo,
                                              const unsigned short* __restrict__ w1, const unsigned short* __restrict__ w2, char* __restrict__ ws) {
    __shared__ __attribute__((aligned(16))) char sm[64 * 1024];
    const int wave = __builtin_amdgcn_readfirstlane(threadIdx.x >> 6), lane = threadIdx.x & 63;
    const int i = lane & 15, g = lane >> 4;
    const int b = blockIdx.x;
    const int gw = b * 7 + wave;
    const bool hasU = b < 224;
    const int xc = b & 7, k28 = b >> 3;
    const int h = 2 * xc + (k28 % 14) / 7, ch = k28 % 7;
    const bool isA = k28 < 14;
    bf16x8 st[4][8];
    bf16x8 shf[5];
    u32x4 wot[5];
    if (hasU) {
        const unsigned short* ms = isA ? Wk : Wv;
#pragma unroll
        for (int t = 0; t < 4; ++t) {
            const unsigned short* p = ms + (size_t)(64 * wave + 16 * t + i) * 4096 + h * 256 + 8 * g;
#pragma unroll
            for (int ke = 0; ke < 8; ++ke) st[t][ke] = *(const bf16x8*)(p + ke * 32);
        }
        if (isA) {
#pragma unroll
            for (int n = 0; n < 5; ++n) {
                const int f = min(wave + 7 * n, 31);
                shf[n] = *(const bf16x8*)(Wq + (size_t)(64 * ch + 16 * (f >> 3) + i) * 4096 + h * 256 + (f & 7) * 32 + 8 * g);
            }
        } else {
#pragma unroll
            for (int n = 0; n < 5; ++n) {
                const int e = min((int)threadIdx.x + n * PREP_NT, 2047), row = e >> 3, c8 = e & 7;
                wot[n] = *(const u32x4*)(Wo + ((size_t)h * 256 + row) * DM + 64 * ch + c8 * 8);
            }
        }
    }
    u32x2 xr[2][2];
#pragma unroll
    for (int n = 0; n < 2; ++n) {
        const int row = gw + n * 1792;
#pragma unroll
        for (int it = 0; it < 2; ++it) {
            const int grp = lane + it * 64;
            xr[n][it] = (u32x2){0u, 0u};
            if (row < 2048 && grp < 112) xr[n][it] = *(const u32x2*)(x + (size_t)row * DM + grp * 4);
        }
    }
    bf16x8 wv[2];
#pragma unroll
    for (int n = 0; n < 2; ++n) { const int f = gw + n * 1792; wv[n] = w12_load(w1, w2, f < 3136 ? f : 0, i, g); }
    {
        const size_t n16 = ((size_t)(NSEG - 1) * 7168 + (size_t)NSEG * E2S) / 16;
        u32x4 sv = {SENT, SENT, SENT, SENT};
        for (size_t idx = (size_t)b * PREP_NT + threadIdx.x; idx < n16; idx += (size_t)PREP_GRID * PREP_NT)
            *(u32x4*)(ws + OFF_MEMF + 7168 + idx * 16) = sv;
    }
    if (b == 255) {
        for (int e = threadIdx.x; e < 8 * 112; e += PREP_NT) {
            int r = e / 112, grp = e % 112;
            int d = grp * 4, t = d >> 4, gg = (d & 15) >> 2;
            u32x2 u = *(const u32x2*)(mem0 + r * DM + d);
            *(u32x2*)(ws + OFF_MEMF + t * 256 + r * 32 + gg * 8) = u;
        }
    }
    if (b == 254) {
        for (int e = threadIdx.x; e < CNT_N; e += PREP_NT) *(unsigned*)(ws + OFF_CNT + (size_t)e * 128) = 0u;
    }
    if (hasU) {
        if (isA) {
#pragma unroll
            for (int n = 0; n < 5; ++n) { const int f = wave + 7 * n; if (f < 32) *(bf16x8*)(sm + f * 1024 + lane * 16) = shf[n]; }
        } else {
#pragma unroll
            for (int n = 0; n < 5; ++n) { const int e = threadIdx.x + n * PREP_NT; if (e < 2048) *(u32x4*)(sm + 32768 + e * 16) = wot[n]; }
            __syncthreads();
            const unsigned short* wo_t = (const unsigned short*)(sm + 32768);
#pragma unroll
            for (int n = 0; n < 5; ++n) {
                const int f = wave + 7 * n;
                if (f < 32) {
                    bf16x8 v;
#pragma unroll
                    for (int j = 0; j < 8; ++j) v[j] = (short)wo_t[((f & 7) * 32 + 8 * g + j) * 64 + 16 * (f >> 3) + i];
                    *(bf16x8*)(sm + f * 1024 + lane * 16) = v;
                }
            }
        }
        __syncthreads();
        if (isA) {
#pragma unroll
            for (int ap = 0; ap < 2; ++ap) {
                bf16x8 qf[2][8];
#pragma unroll
                for (int a = 0; a < 2; ++a)
#pragma unroll
                    for (int ke = 0; ke < 8; ++ke) qf[a][ke] = *(const bf16x8*)(sm + ((2 * ap + a) * 8 + ke) * 1024 + lane * 16);
#pragma unroll
                for (int a = 0; a < 2; ++a)
#pragma unroll
                    for (int bp = 0; bp < 2; ++bp) {
                        f32x4 d0 = {0.f, 0.f, 0.f, 0.f}, d1 = {0.f, 0.f, 0.f, 0.f};
#pragma unroll
                        for (int ke = 0; ke < 8; ++ke) { d0 = mfma32(st[2 * bp][ke], qf[a][ke], d0); d1 = mfma32(st[2 * bp + 1][ke], qf[a][ke], d1); }
                        *(bf16x8*)(ws + OFF_WF + ((size_t)(h * 28 + 4 * ch + 2 * ap + a) * KK + 2 * wave + bp) * KB + lane * 16) = pack8(d0, d1);
                    }
#pragma unroll
                for (int bb = 0; bb < 4; ++bb) {
                    f32x4 d0 = {0.f, 0.f, 0.f, 0.f}, d1 = {0.f, 0.f, 0.f, 0.f};
#pragma unroll
                    for (int ke = 0; ke < 8; ++ke) { d0 = mfma32(qf[0][ke], st[bb][ke], d0); d1 = mfma32(qf[1][ke], st[bb][ke], d1); }
                    *(bf16x8*)(ws + OFF_ANF + ((size_t)(h * 28 + 4 * wave + bb) * KK + 2 * ch + ap) * KB + lane * 16) = pack8(d0, d1);
                }
            }
        } else {
#pragma unroll
            for (int ct = 0; ct < 4; ++ct) {
                bf16x8 fix[8];
#pragma unroll
                for (int ke = 0; ke < 8; ++ke) fix[ke] = *(const bf16x8*)(sm + (ct * 8 + ke) * 1024 + lane * 16);
#pragma unroll
                for (int dp = 0; dp < 2; ++dp) {
                    f32x4 d0 = {0.f, 0.f, 0.f, 0.f}, d1 = {0.f, 0.f, 0.f, 0.f};
#pragma unroll
                    for (int ke = 0; ke < 8; ++ke) { d0 = mfma32(st[2 * dp][ke], fix[ke], d0); d1 = mfma32(st[2 * dp + 1][ke], fix[ke], d1); }
                    *(bf16x8*)(ws + OFF_WF + ((size_t)(448 + h * 28 + 4 * ch + ct) * KK + 2 * wave + dp) * KB + lane * 16) = pack8(d0, d1);
                }
            }
        }
    }
    ln_row(x, ws, gw, lane, xr[0]);
    if (gw + 1792 < 2048) ln_row(x, ws, gw + 1792, lane, xr[1]);
    w12_store(ws, gw, lane, wv[0]);
    if (gw + 1792 < 3136) w12_store(ws, gw + 1792, lane, wv[1]);
}

constexpr int CH_LDS_B = 0;
constexpr int CH_LDS_MEM = 112 * 1024;
constexpr int CH_LDS_RA = CH_LDS_MEM + 7168;
constexpr int CH_LDS_RB = CH_LDS_RA + 8192;
constexpr int CH_LDS_PW = CH_LDS_RB + 4096;
constexpr int CH_LDS_SIZE = CH_LDS_PW + 4096;

#define E2POS 7
#define D1 5
#define D2 0

DI void chain_role(char* __restrict__ ws, char* lds, const rsrc_t rs) {
    const int c = blockIdx.x >> 3;
    const int tid = threadIdx.x, wave = __builtin_amdgcn_readfirstlane(tid >> 6), lane = tid & 63;
    const int i = lane & 15, g = lane >> 4;
    const int h = c >> 1, half = c & 1;
    const bool hasB = c < NT16;
    const int tB = hasB ? c : 0;
    unsigned* tmo = (unsigned*)(ws + OFF_TMO);
    const float cs = 0.0625f * 1.4426950408889634f;

    bf16x8 wa[3][KK], wsh[7], wb[2][KK];
    {
        const char* ap = ws + OFF_ANF + (size_t)(h * 28 + 14 * half) * KK * KB + lane * 16;
#pragma unroll
        for (int t = 0; t < 3; ++t)
#pragma unroll
            for (int kk = 0; kk < KK; ++kk) wa[t][kk] = ldfrag(ap + (size_t)((3 * wave + t) * KK + kk) * KB);
#pragma unroll
        for (int k = 0; k < 7; ++k) wsh[k] = ldfrag(ap + (size_t)((12 + (wave >> 1)) * KK + 7 * (wave & 1) + k) * KB);
    }
    const int gk0 = wave < 2 ? 4 * wave : 8 + 3 * (wave - 2);
    const int gkn = wave < 2 ? 4 : 3;
    bf16x8 gf[4], uf[4];
    {
        const char* bp = ws + OFF_WF + (size_t)(448 + tB) * KK * KB + lane * 16;
#pragma unroll
        for (int hh = 0; hh < 2; ++hh)
#pragma unroll
            for (int kk = 0; kk < KK; ++kk)
                *(bf16x8*)(lds + CH_LDS_B + (size_t)((wave * 2 + hh) * KK + kk) * KB + lane * 16) = ldfrag(bp + (size_t)((4 * wave + 2 + hh) * 28 * KK + kk) * KB);
#pragma unroll
        for (int hh = 0; hh < 2; ++hh)
#pragma unroll
            for (int kk = 0; kk < KK; ++kk) wb[hh][kk] = ldfrag(bp + (size_t)((4 * wave + hh) * 28 * KK + kk) * KB);
    }
    wait_cnt(ws, CNT_G + 0, 224);
    __syncthreads();
    {
        const size_t gp = OFF_GTF + (((size_t)h * NRT + half) * KK + gk0) * KB + lane * 16;
#pragma unroll
        for (int k = 0; k < 4; ++k) gf[k] = ld8c(rs, gp + (size_t)(k < gkn ? k : 0) * KB);
    }
    const char* mp = lds + CH_LDS_MEM + g * 128 + (i & 7) * 16;
    const int hh2 = lane >> 4, r2 = (lane >> 1) & 7, q2 = lane & 1;

    for (int s = 0; s < NSEG - 1; ++s) {
        STAMP(0);
        {
            u32x4 x0, x1;
            unsigned spins = 0;
            const size_t o0 = OFF_MEMF + (size_t)(s * 7168 + tid * 16), o1 = o0 + (tid < 192 ? 4096 : 0);
            const size_t oc = OFF_CNT + (size_t)(CNT_G + s + 1) * 128;
            for (int d = 0; d < D1; ++d) __builtin_amdgcn_s_sleep(1);
            for (;;) {
                asm volatile("" ::: "memory");
                x0 = ld4c(rs, o0);
                x1 = ld4c(rs, o1);
                unsigned cv = ld1c(rs, oc);
                if (__all((max(umax4(x0), umax4(x1)) != SENT) && (cv >= 224u))) break;
                if (spin_fail(spins, tmo, 0x100u + s)) break;
            }
            const int t0 = tid >> 4, rr = (tid >> 1) & 7, gp = tid & 1;
            char* d0 = lds + CH_LDS_MEM + (t0 >> 1) * 512 + gp * 256 + rr * 16 + (t0 & 1) * 8;
            *(u32x2*)(d0) = (u32x2){x0.x, x0.y};
            *(u32x2*)(d0 + 128) = (u32x2){x0.z, x0.w};
            if (tid < 192) {
                *(u32x2*)(d0 + 8 * 512) = (u32x2){x1.x, x1.y};
                *(u32x2*)(d0 + 8 * 512 + 128) = (u32x2){x1.z, x1.w};
            }
        }
        {
            const size_t up = OFF_UF + (((size_t)s * NT16 + tB) * NH + 4 * wave) * KB + lane * 16;
#pragma unroll
            for (int k = 0; k < 4; ++k) uf[k] = ld8c(rs, up + (size_t)k * KB);
        }
        STAMP(1);
        __syncthreads();
        STAMP(2);
        if (c == 31 && tid == 255) __hip_atomic_store((unsigned*)(ws + OFF_CNT + (size_t)CNT_PROG * 128), (unsigned)s, __ATOMIC_RELAXED, __HIP_MEMORY_SCOPE_AGENT);
        bf16x8 mf[KK];
#pragma unroll
        for (int kk = 0; kk < KK; ++kk) mf[kk] = *(const bf16x8*)(mp + kk * 512);
        {
            bf16x8 mg[4];
            bf16x4 mk[4];
#pragma unroll
            for (int k = 0; k < 4; ++k) mg[k] = *(const bf16x8*)(mp + (gk0 + (k < gkn ? k : 0)) * 512);
#pragma unroll
            for (int t = 0; t < 4; ++t) {
                const int tl = (t < 3) ? 3 * wave + t : 12 + (wave >> 1);
                const int tg = 14 * half + tl;
                mk[t] = *(const bf16x4*)(mp + (tg >> 1) * 512 + (tg & 1) * 8);
            }
            f32x4 ta[4];
            f32x4 sx, sm;
            if (wave & 1) {
                mfma_Aa0(ta[3], wsh[0], mf[7]);
#pragma unroll
                for (int k = 1; k < 7; ++k) mfma_Aa(ta[3], wsh[k], mf[7 + k]);
            } else {
                mfma_Aa0(ta[3], wsh[0], mf[0]);
#pragma unroll
                for (int k = 1; k < 7; ++k) mfma_Aa(ta[3], wsh[k], mf[k]);
            }
            mfma_Aa0(ta[0], wa[0][0], mf[0]);
#pragma unroll
            for (int kk = 1; kk < KK; ++kk) mfma_Aa(ta[0], wa[0][kk], mf[kk]);
            mfma_Aa0(ta[1], wa[1][0], mf[0]);
#pragma unroll
            for (int kk = 1; kk < 4; ++kk) mfma_Aa(ta[1], wa[1][kk], mf[kk]);
            pin(ta[3]); pin(ta[0]);
            mfma16_v0(sm, mk[3], pack4(ta[3]));
            mfma16_v(sm, mk[0], pack4(ta[0]));
#pragma unroll
            for (int kk = 4; kk < KK; ++kk) mfma_Aa(ta[1], wa[1][kk], mf[kk]);
            mfma_Aa0(ta[2], wa[2][0], mf[0]);
#pragma unroll
            for (int kk = 1; kk < 4; ++kk) mfma_Aa(ta[2], wa[2][kk], mf[kk]);
            pin(ta[1]);
            mfma16_v(sm, mk[1], pack4(ta[1]));
#pragma unroll
            for (int kk = 4; kk < KK; ++kk) mfma_Aa(ta[2], wa[2][kk], mf[kk]);
            mfma_v0(sx, gf[0], mg[0]);
            mfma_v(sx, gf[1], mg[1]);
            mfma_v(sx, gf[2], mg[2]);
            if (wave < 2) mfma_v(sx, gf[3], mg[3]);
            pin(ta[2]);
            mfma16_v(sm, mk[2], pack4(ta[2]));
            mfma_fence4(sm, sx, ta[0], ta[1]);
            *(f32x4*)(lds + CH_LDS_RA + (wave * 2 + 0) * 1024 + lane * 16) = sx;
            *(f32x4*)(lds + CH_LDS_RA + (wave * 2 + 1) * 1024 + lane * 16) = sm;
        }
        STAMP(3);
        __syncthreads();
        const size_t e2w = OFF_SMX + (size_t)s * E2S + (size_t)c * E2B;
        if (wave < 2) {
            f32x4 v0 = *(const f32x4*)(lds + CH_LDS_RA + (0 + wave) * 1024 + lane * 16);
            f32x4 v1 = *(const f32x4*)(lds + CH_LDS_RA + (2 + wave) * 1024 + lane * 16);
            f32x4 v2 = *(const f32x4*)(lds + CH_LDS_RA + (4 + wave) * 1024 + lane * 16);
            f32x4 v3 = *(const f32x4*)(lds + CH_LDS_RA + (6 + wave) * 1024 + lane * 16);
            f32x4 v = (v0 + v1) + (v2 + v3);
            if (wave == 0) {
                float m = fmaxf(fmaxf(v[0], v[1]), fmaxf(v[2], v[3]));
                m = xmax16(m); m = xmax32(m);
                const float mc = m * cs;
                float e0 = fexp2(v[0] * cs - mc), e1 = fexp2(v[1] * cs - mc), e2 = fexp2(v[2] * cs - mc), e3 = fexp2(v[3] * cs - mc);
                u32x2 pk = {pack2(e0, e1), pack2(e2, e3)};
                if (i < 8) st2c(rs, e2w + i * 32 + g * 8, pk);
                float sum = (bf_lo(pk.x) + bf_hi(pk.x)) + (bf_lo(pk.y) + bf_hi(pk.y));
                sum = xadd16(sum); sum = xadd32(sum);
                u32x2 st = {__float_as_uint(m), __float_as_uint(sum)};
                if (lane < 8) st2c(rs, e2w + 512 + i * 8, st);
            } else {
                if (i < 8 && g < 2) st4c(rs, e2w + 256 + i * 32 + g * 16, __builtin_bit_cast(u32x4, v));
            }
        }
        {
            const size_t gp = OFF_GTF + (((size_t)h * NRT + 2 * (s + 1) + half) * KK + gk0) * KB + lane * 16;
#pragma unroll
            for (int k = 0; k < 4; ++k) gf[k] = ld8c(rs, gp + (size_t)(k < gkn ? k : 0) * KB);
        }
        if (hasB) {
            f32x4 ya[4];
            const char* lb = lds + CH_LDS_B + (size_t)(wave * 2) * KK * KB + lane * 16;
            mfma_Ba0(ya[0], mf[0], wb[0][0]);
            mfma_v0(ya[1], mf[0], wb[1][0]);
#pragma unroll
            for (int k = 1; k < KK; ++k) {
                mfma_Ba(ya[0], mf[k], wb[0][k]);
                mfma_v(ya[1], mf[k], wb[1][k]);
            }
            STAMP(4);
            u32x4 qx[4], qm0, qm1, qs;
            const size_t e2r = OFF_SMX + (size_t)s * E2S;
            const size_t ox = e2r + (size_t)(8 * wave) * E2B + (i & 7) * 32 + g * 8;
            const size_t om = e2r + (size_t)(8 * wave + 2 * hh2) * E2B + 256 + r2 * 32 + q2 * 16;
            const size_t os = e2r + (size_t)(8 * wave + 2 * hh2) * E2B + 512 + r2 * 8;
#define E2_LOADS() do { _Pragma("unroll") for (int hh = 0; hh < 4; ++hh) { \
                        u32x2 lo_ = ld2c(rs, ox + (size_t)(2 * hh) * E2B), hi_ = ld2c(rs, ox + (size_t)(2 * hh + 1) * E2B); \
                        qx[hh] = (u32x4){lo_.x, lo_.y, hi_.x, hi_.y}; } \
                    qm0 = ld4c(rs, om); qm1 = ld4c(rs, om + E2B); \
                    { u32x2 lo_ = ld2c(rs, os), hi_ = ld2c(rs, os + E2B); qs = (u32x4){lo_.x, lo_.y, hi_.x, hi_.y}; } } while (0)
            {
                bf16x8 lq[2][4];
#pragma unroll
                for (int k = 0; k < 2; ++k) { lq[0][2 * k] = *(const bf16x8*)(lb + (size_t)k * KB); lq[0][2 * k + 1] = *(const bf16x8*)(lb + (size_t)(KK + k) * KB); }
#pragma unroll
                for (int gq = 0; gq < 7; ++gq) {
                    if (gq < 6) {
#pragma unroll
                        for (int k = 0; k < 2; ++k) {
                            lq[(gq + 1) & 1][2 * k] = *(const bf16x8*)(lb + (size_t)(2 * gq + 2 + k) * KB);
                            lq[(gq + 1) & 1][2 * k + 1] = *(const bf16x8*)(lb + (size_t)(KK + 2 * gq + 2 + k) * KB);
                        }
                    }
                    if (gq == E2POS) { asm volatile("" ::: "memory"); E2_LOADS(); }
                    __builtin_amdgcn_sched_barrier(0);
                    if (gq == 0) { mfma_v0(ya[2], mf[0], lq[0][0]); mfma_v0(ya[3], mf[0], lq[0][1]); }
                    else { mfma_v(ya[2], mf[2 * gq], lq[gq & 1][0]); mfma_v(ya[3], mf[2 * gq], lq[gq & 1][1]); }
                    mfma_v(ya[2], mf[2 * gq + 1], lq[gq & 1][2]); mfma_v(ya[3], mf[2 * gq + 1], lq[gq & 1][3]);
                }
            }
            mfma_fence4(ya[0], ya[1], ya[2], ya[3]);
            bf16x8 yp0 = pack8(ya[0], ya[1]), yp1 = pack8(ya[2], ya[3]);
            __builtin_amdgcn_sched_barrier(0);
            {
                unsigned spins = 0;
                if (E2POS >= 7) { for (int d = 0; d < D2; ++d) __builtin_amdgcn_s_sleep(1); asm volatile("" ::: "memory"); E2_LOADS(); }
                for (;;) {
                    unsigned mx = max(max(max(umax4(qx[0]), umax4(qx[1])), max(umax4(qx[2]), umax4(qx[3]))), max(max(umax4(qm0), umax4(qm1)), umax4(qs)));
                    if (__all(mx != SENT)) break;
                    if (spin_fail(spins, tmo, 0x200u + s)) break;
                    asm volatile("" ::: "memory");
                    E2_LOADS();
                }
            }
            STAMP(5);
            f32x4 dd[8];
#pragma unroll
            for (int hh = 0; hh < 4; ++hh) {
                const u32x4 u = __builtin_bit_cast(u32x4, uf[hh]);
                u32x2 ulo = {u.x, u.y}, uhi = {u.z, u.w}, elo = {qx[hh].x, qx[hh].y}, ehi = {qx[hh].z, qx[hh].w};
                mfma16_v0(dd[2 * hh], __builtin_bit_cast(bf16x4, ulo), __builtin_bit_cast(bf16x4, elo));
                mfma16_v0(dd[2 * hh + 1], __builtin_bit_cast(bf16x4, uhi), __builtin_bit_cast(bf16x4, ehi));
            }
            f32x4 ms = __builtin_bit_cast(f32x4, qm0) + __builtin_bit_cast(f32x4, qm1);
            const float m0 = __uint_as_float(qs.x), s0 = __uint_as_float(qs.y), m1 = __uint_as_float(qs.z), s1 = __uint_as_float(qs.w);
            float mx = fmaxf(fmaxf(ms[0], ms[1]), fmaxf(ms[2], ms[3]));
            mx = fmaxf(mx, qswap(mx));
            const float Mc = fmaxf(mx, fmaxf(m0, m1)) * cs;
            float e0 = fexp2(ms[0] * cs - Mc), e1 = fexp2(ms[1] * cs - Mc), e2 = fexp2(ms[2] * cs - Mc), e3 = fexp2(ms[3] * cs - Mc);
            float f0 = fexp2(m0 * cs - Mc), f1 = fexp2(m1 * cs - Mc);
            float sm = (e0 + e1) + (e2 + e3);
            sm += qswap(sm);
            const float inv = __builtin_amdgcn_rcpf(sm + f0 * s0 + f1 * s1);
            char* pw = lds + CH_LDS_PW + wave * 1024;
            *(u32x2*)(pw + ((hh2 * 8 + r2) * 2 + q2) * 8) = (u32x2){pack2(e0 * inv, e1 * inv), pack2(e2 * inv, e3 * inv)};
            if (q2 == 0) *(u32x2*)(pw + 512 + (r2 * 4 + hh2) * 8) = (u32x2){__float_as_uint(f0 * inv), __float_as_uint(f1 * inv)};
            __builtin_amdgcn_fence(__ATOMIC_RELEASE, "wavefront");
            __builtin_amdgcn_wave_barrier();
            __builtin_amdgcn_fence(__ATOMIC_ACQUIRE, "wavefront");
            u32x2 pa[4];
#pragma unroll
            for (int hh = 0; hh < 4; ++hh) pa[hh] = *(const u32x2*)(pw + ((hh * 8 + (i & 7)) * 2 + (g & 1)) * 8);
            f32x4 fa = *(const f32x4*)(pw + 512 + (i & 7) * 32), fb = *(const f32x4*)(pw + 512 + (i & 7) * 32 + 16);
            if (g >= 2) {
#pragma unroll
                for (int hh = 0; hh < 4; ++hh) pa[hh] = (u32x2){0u, 0u};
            }
            mfma_fence4(dd[0], dd[1], dd[2], dd[3]);
            mfma_fence4(dd[4], dd[5], dd[6], dd[7]);
            f32x4 acc = dd[0] * fa[0] + dd[1] * fa[1] + dd[2] * fa[2] + dd[3] * fa[3] + dd[4] * fb[0] + dd[5] * fb[1] + dd[6] * fb[2] + dd[7] * fb[3];
            u32x4 p01 = {pa[0].x, pa[0].y, pa[1].x, pa[1].y}, p23 = {pa[2].x, pa[2].y, pa[3].x, pa[3].y};
            mfma_v(acc, yp0, __builtin_bit_cast(bf16x8, p01));
            mfma_v(acc, yp1, __builtin_bit_cast(bf16x8, p23));
            mfma_fence1(acc);
            *(f32x4*)(lds + CH_LDS_RB + wave * 1024 + lane * 16) = acc;
        }
        STAMP(6);
        __syncthreads();
        if (hasB && wave == 0) {
            f32x4 v0 = *(const f32x4*)(lds + CH_LDS_RB + lane * 16);
            f32x4 v1 = *(const f32x4*)(lds + CH_LDS_RB + 1024 + lane * 16);
            f32x4 v2 = *(const f32x4*)(lds + CH_LDS_RB + 2048 + lane * 16);
            f32x4 v3 = *(const f32x4*)(lds + CH_LDS_RB + 3072 + lane * 16);
            f32x4 v = (v0 + v1) + (v2 + v3);
            if (i < 8) {
                u32x2 pk = {pack2(v[0], v[1]), pack2(v[2], v[3])};
                st2c(rs, OFF_MEMF + (size_t)((s + 1) * 7168 + tB * 256 + i * 32 + g * 8), pk);
            }
        }
    }
    if (c == 31) {
        unsigned spins = 0;
        const size_t o0 = OFF_MEMF + (size_t)((NSEG - 1) * 7168 + tid * 16), o1 = o0 + (tid < 192 ? 4096 : 0);
        for (;;) {
            asm volatile("" ::: "memory");
            u32x4 x0 = ld4c(rs, o0), x1 = ld4c(rs, o1);
            if (__all(max(umax4(x0), umax4(x1)) != SENT)) break;
            if (spin_fail(spins, tmo, 0x1FFu)) break;
        }
        __syncthreads();
        if (tid == 255) __hip_atomic_store((unsigned*)(ws + OFF_CNT + (size_t)CNT_PROG * 128), (unsigned)(NSEG - 1), __ATOMIC_RELAXED, __HIP_MEMORY_SCOPE_AGENT);
    }
}

template <int TYPE>
DI void w1_phase(char* __restrict__ ws, const rsrc_t rs, int tile0, int wave, int lane) {
    bf16x8 w[4][KK];
    {
        const char* wp = ws + OFF_WF + (size_t)tile0 * KK * KB + lane * 16;
#pragma unroll
        for (int t = 0; t < 4; ++t)
#pragma unroll
            for (int kk = 0; kk < KK; ++kk) w[t][kk] = ldfrag(wp + (size_t)(t * KK + kk) * KB);
    }
    const int h = (tile0 - 448 * TYPE) / 28, t0 = (tile0 - 448 * TYPE) % 28;
    bf16x8 xa[KK], xb[KK];
    {
        const char* xp = ws + OFF_XNF + (size_t)(2 * wave) * KK * KB + lane * 16;
#pragma unroll
        for (int kk = 0; kk < KK; ++kk) xa[kk] = ldfrag(xp + (size_t)kk * KB);
    }
    for (int s = wave; s < NSEG; s += 4) {
        f32x4 acc[2][4];
        {
            const char* xp = ws + OFF_XNF + (size_t)(2 * s + 1) * KK * KB + lane * 16;
#pragma unroll
            for (int kk = 0; kk < KK; ++kk) xb[kk] = ldfrag(xp + (size_t)kk * KB);
        }
        __builtin_amdgcn_sched_barrier(0);
#pragma unroll
        for (int t = 0; t < 4; ++t) { if (TYPE == 0) mfma_Aa0(acc[0][t], w[t][0], xa[0]); else mfma_Ba0(acc[0][t], xa[0], w[t][0]); }
#pragma unroll
        for (int kk = 1; kk < KK; ++kk)
#pragma unroll
            for (int t = 0; t < 4; ++t) { if (TYPE == 0) mfma_Aa(acc[0][t], w[t][kk], xa[kk]); else mfma_Ba(acc[0][t], xa[kk], w[t][kk]); }
        __builtin_amdgcn_sched_barrier(0);
        {
            const int sn = (s + 4 < NSEG) ? s + 4 : s;
            const char* xp = ws + OFF_XNF + (size_t)(2 * sn) * KK * KB + lane * 16;
#pragma unroll
            for (int kk = 0; kk < KK; ++kk) xa[kk] = ldfrag(xp + (size_t)kk * KB);
        }
        __builtin_amdgcn_sched_barrier(0);
#pragma unroll
        for (int t = 0; t < 4; ++t) { if (TYPE == 0) mfma_Aa0(acc[1][t], w[t][0], xb[0]); else mfma_Ba0(acc[1][t], xb[0], w[t][0]); }
#pragma unroll
        for (int kk = 1; kk < KK; ++kk)
#pragma unroll
            for (int t = 0; t < 4; ++t) { if (TYPE == 0) mfma_Aa(acc[1][t], w[t][kk], xb[kk]); else mfma_Ba(acc[1][t], xb[kk], w[t][kk]); }
        mfma_fence4(acc[0][0], acc[0][1], acc[0][2], acc[0][3]);
        mfma_fence4(acc[1][0], acc[1][1], acc[1][2], acc[1][3]);
        if (TYPE == 0) {
#pragma unroll
            for (int q = 0; q < 2; ++q)
#pragma unroll
                for (int p = 0; p < 2; ++p)
                    st8c(rs, OFF_GTF + (((size_t)h * NRT + 2 * s + q) * KK + (t0 >> 1) + p) * KB + lane * 16, pack8(acc[q][2 * p], acc[q][2 * p + 1]));
        } else {
#pragma unroll
            for (int t = 0; t < 4; ++t)
                st8c(rs, OFF_UF + (((size_t)s * NT16 + t0 + t) * NH + h) * KB + lane * 16, pack8(acc[0][t], acc[1][t]));
        }
        signal_cnt(ws, CNT_G + s, lane);
    }
}

DI void ffn_task(char* __restrict__ ws, char* lds, const rsrc_t rs, const unsigned short* __restrict__ x, const unsigned short* __restrict__ b1,
                 const unsigned short* __restrict__ b2, int s, int wave, int lane) {
    const int i = lane & 15, g = lane >> 4;
    {
        bf16x8 xf[2][KK];
        const char* xp = ws + OFF_XNF + (size_t)(2 * s) * KK * KB + lane * 16;
#pragma unroll
        for (int q = 0; q < 2; ++q)
#pragma unroll
            for (int kk = 0; kk < KK; ++kk) xf[q][kk] = ldfrag(xp + (size_t)(q * KK + kk) * KB);
        const char* wp = ws + OFF_WF + (size_t)(896 + 28 * wave) * KK * KB + lane * 16;
        for (int pr = 0; pr < 14; ++pr) {
            bf16x8 wf[2][KK];
#pragma unroll
            for (int t = 0; t < 2; ++t)
#pragma unroll
                for (int kk = 0; kk < KK; ++kk) wf[t][kk] = ldfrag(wp + (size_t)((2 * pr + t) * KK + kk) * KB);
            f32x4 acc[2][2];
#pragma unroll
            for (int q = 0; q < 2; ++q)
#pragma unroll
                for (int t = 0; t < 2; ++t) acc[q][t] = (f32x4){0.f, 0.f, 0.f, 0.f};
#pragma unroll
            for (int kk = 0; kk < KK; ++kk)
#pragma unroll
                for (int q = 0; q < 2; ++q)
#pragma unroll
                    for (int t = 0; t < 2; ++t) acc[q][t] = mfma32(wf[t][kk], xf[q][kk], acc[q][t]);
#pragma unroll
            for (int t = 0; t < 2; ++t) {
                u32x2 bb = *(const u32x2*)(b1 + 16 * (28 * wave + 2 * pr + t) + 4 * g);
                float bv[4] = {bf_lo(bb.x), bf_hi(bb.x), bf_lo(bb.y), bf_hi(bb.y)};
#pragma unroll
                for (int q = 0; q < 2; ++q)
#pragma unroll
                    for (int r = 0; r < 4; ++r) acc[q][t][r] = gelu_tanh(acc[q][t][r] + bv[r]);
            }
#pragma unroll
            for (int q = 0; q < 2; ++q)
                *(bf16x8*)(lds + (size_t)(q * KF + 14 * wave + pr) * KB + lane * 16) = pack8(acc[q][0], acc[q][1]);
        }
    }
    __syncthreads();
    {
        f32x4 acc[2][7];
#pragma unroll
        for (int q = 0; q < 2; ++q)
#pragma unroll
            for (int t = 0; t < 7; ++t) acc[q][t] = (f32x4){0.f, 0.f, 0.f, 0.f};
        const char* wp = ws + OFF_W2F + (size_t)(7 * wave) * KF * KB + lane * 16;
#pragma unroll 4
        for (int kf = 0; kf < KF; ++kf) {
            bf16x8 h0 = *(const bf16x8*)(lds + (size_t)kf * KB + lane * 16), h1 = *(const bf16x8*)(lds + (size_t)(KF + kf) * KB + lane * 16);
#pragma unroll
            for (int t = 0; t < 7; ++t) {
                bf16x8 wf = ldfrag(wp + (size_t)(t * KF + kf) * KB);
                acc[0][t] = mfma32(wf, h0, acc[0][t]);
                acc[1][t] = mfma32(wf, h1, acc[1][t]);
            }
        }
#pragma unroll
        for (int t = 0; t < 7; ++t) {
            const int tc = 7 * wave + t;
            u32x2 bb = *(const u32x2*)(b2 + 16 * tc + 4 * g);
#pragma unroll
            for (int q = 0; q < 2; ++q) {
                const size_t o = (size_t)(32 * s + 16 * q + i) * DM + 16 * tc + 4 * g;
                u32x2 xv = *(const u32x2*)(x + o);
                f32x4 r = {acc[q][t][0] + bf_lo(bb.x) + bf_lo(xv.x), acc[q][t][1] + bf_hi(bb.x) + bf_hi(xv.x), acc[q][t][2] + bf_lo(bb.y) + bf_lo(xv.y), acc[q][t][3] + bf_hi(bb.y) + bf_hi(xv.y)};
                st4c(rs, OFF_FO + o * 4, __builtin_bit_cast(u32x4, r));
            }
        }
        signal_cnt(ws, CNT_F + s, lane);
    }
    __syncthreads();
}

DI void w3_loop(char* __restrict__ ws, const rsrc_t rs, int tile0, int lane) {
    const int i = lane & 15, g = lane >> 4;
    bf16x8 w[4][KK];
    {
        const char* wp = ws + OFF_WF + (size_t)tile0 * KK * KB + lane * 16;
#pragma unroll
        for (int t = 0; t < 4; ++t)
#pragma unroll
            for (int kk = 0; kk < KK; ++kk) w[t][kk] = ldfrag(wp + (size_t)(t * KK + kk) * KB);
    }
    const bool isA = tile0 < 448;
    const int h = (isA ? tile0 : tile0 - 448) / 28, t0 = (isA ? tile0 : tile0 - 448) % 28;
    for (int sp = 0; sp < NSEG / 2; ++sp) {
        bf16x8 xf[KK];
        wait_cnt_slow(ws, CNT_PROG, (unsigned)(sp == NSEG / 2 - 1 ? 2 * sp : 2 * sp + 1));
        {
            const size_t mp = OFF_MEMF + (size_t)(2 * sp + (i >> 3)) * 7168 + (i & 7) * 32 + g * 8;
            unsigned spins = 0;
            for (;;) {
                asm volatile("" ::: "memory");
                unsigned mx = 0;
#pragma unroll
                for (int kk = 0; kk < KK; ++kk) {
                    u32x2 lo_ = ld2c(rs, mp + kk * 512), hi_ = ld2c(rs, mp + kk * 512 + 256);
                    u32x4 xx_ = {lo_.x, lo_.y, hi_.x, hi_.y};
                    mx = max(mx, umax4(xx_));
                    xf[kk] = __builtin_bit_cast(bf16x8, xx_);
                }
                if (__all(mx != SENT)) break;
                __builtin_amdgcn_s_sleep(16);
                if (spin_fail(spins, (unsigned*)(ws + OFF_TMO), 0x500u + sp)) break;
            }
        }
        f32x4 acc[4];
        if (isA) {
#pragma unroll
            for (int t = 0; t < 4; ++t) mfma_Aa0(acc[t], w[t][0], xf[0]);
#pragma unroll
            for (int kk = 1; kk < KK; ++kk)
#pragma unroll
                for (int t = 0; t < 4; ++t) mfma_Aa(acc[t], w[t][kk], xf[kk]);
            mfma_fence4(acc[0], acc[1], acc[2], acc[3]);
#pragma unroll
            for (int p = 0; p < 2; ++p)
                st8c(rs, OFF_T2F + ((size_t)h * NSEG + 2 * sp + (i >> 3)) * 7168 + ((t0 >> 1) + p) * 512 + g * 128 + (i & 7) * 16, pack8(acc[2 * p], acc[2 * p + 1]));
        } else {
#pragma unroll
            for (int t = 0; t < 4; ++t) mfma_Ba0(acc[t], xf[0], w[t][0]);
#pragma unroll
            for (int kk = 1; kk < KK; ++kk)
#pragma unroll
                for (int t = 0; t < 4; ++t) mfma_Ba(acc[t], xf[kk], w[t][kk]);
            mfma_fence4(acc[0], acc[1], acc[2], acc[3]);
#pragma unroll
            for (int t = 0; t < 4; ++t)
                st2c(rs, OFF_YF + (((size_t)(2 * sp + (g >> 1)) * NT16 + t0 + t) * 8 + (h >> 1)) * 512 + (g & 1) * 256 + i * 16 + (h & 1) * 8, __builtin_bit_cast(u32x2, pack4(acc[t])));
        }
        signal_cnt(ws, isA ? CNT_T2 + sp * 16 + h : CNT_Y + sp * 7 + (t0 >> 2), lane);
    }
}

DI void f1_task(char* __restrict__ ws, const rsrc_t rs, int s, int h, int lane) {
    const int i = lane & 15, g = lane >> 4;
    f32x4 sc[2][3];
#pragma unroll
    for (int q = 0; q < 2; ++q)
#pragma unroll
        for (int k = 0; k < 3; ++k) sc[q][k] = (f32x4){0.f, 0.f, 0.f, 0.f};
    const char* xp = ws + OFF_XNF + (size_t)(2 * s) * KK * KB + lane * 16;
    const size_t gp = OFF_GTF + ((size_t)h * NRT + 2 * s) * KK * KB + lane * 16;
    const size_t tp = OFF_T2F + ((size_t)h * NSEG + s) * 7168 + g * 128 + (i & 7) * 16;
    bf16x8 xf[2][KK];
    {
        wait_cnt(ws, CNT_G + s, 224);
        bf16x8 g0[KK], g1[KK];
#pragma unroll
        for (int kk = 0; kk < KK; ++kk) { g0[kk] = ld8c(rs, gp + (size_t)kk * KB); g1[kk] = ld8c(rs, gp + (size_t)(KK + kk) * KB); }
#pragma unroll
        for (int q = 0; q < 2; ++q)
#pragma unroll
            for (int kk = 0; kk < KK; ++kk) xf[q][kk] = ldfrag(xp + (size_t)(q * KK + kk) * KB);
#pragma unroll
        for (int kk = 0; kk < KK; ++kk)
#pragma unroll
            for (int q = 0; q < 2; ++q) {
                sc[q][0] = mfma32(g0[kk], xf[q][kk], sc[q][0]);
                sc[q][1] = mfma32(g1[kk], xf[q][kk], sc[q][1]);
            }
    }
    wait_cnt_slow(ws, CNT_PROG, (unsigned)(s | 1));
    wait_cnt(ws, CNT_T2 + (s >> 1) * 16 + h, 7);
    {
        bf16x8 t2[KK];
#pragma unroll
        for (int kk = 0; kk < KK; ++kk) t2[kk] = ld8c(rs, tp + kk * 512);
#pragma unroll
        for (int kk = 0; kk < KK; ++kk)
#pragma unroll
            for (int q = 0; q < 2; ++q) sc[q][2] = mfma32(t2[kk], xf[q][kk], sc[q][2]);
    }
    const float cs = 0.0625f * 1.4426950408889634f;
    const float NEG = -3.0e38f;
#pragma unroll
    for (int q = 0; q < 2; ++q) {
        const int qi = 16 * q + i;
        float xs[8], ms[4];
#pragma unroll
        for (int j = 0; j < 8; ++j) {
            const int key = 16 * (j >> 2) + 4 * g + (j & 3);
            xs[j] = (key <= qi) ? sc[q][j >> 2][j & 3] : NEG;
        }
#pragma unroll
        for (int j = 0; j < 4; ++j) ms[j] = (g < 2) ? sc[q][2][j] : NEG;
        float m = fmaxf(fmaxf(fmaxf(xs[0], xs[1]), fmaxf(xs[2], xs[3])), fmaxf(fmaxf(xs[4], xs[5]), fmaxf(xs[6], xs[7])));
        m = fmaxf(m, fmaxf(fmaxf(ms[0], ms[1]), fmaxf(ms[2], ms[3])));
        m = xmax16(m); m = xmax32(m);
        const float mc = m * cs;
        float sum = 0.f;
#pragma unroll
        for (int j = 0; j < 8; ++j) { const int key = 16 * (j >> 2) + 4 * g + (j & 3); xs[j] = (key <= qi) ? fexp2(xs[j] * cs - mc) : 0.f; sum += xs[j]; }
#pragma unroll
        for (int j = 0; j < 4; ++j) { ms[j] = (g < 2) ? fexp2(ms[j] * cs - mc) : 0.f; sum += ms[j]; }
        sum = xadd16(sum); sum = xadd32(sum);
        const float inv = 1.f / sum;
        u32x4 px = {pack2(xs[0] * inv, xs[1] * inv), pack2(xs[2] * inv, xs[3] * inv), pack2(xs[4] * inv, xs[5] * inv), pack2(xs[6] * inv, xs[7] * inv)};
        st4c(rs, OFF_PXF + (((size_t)s * 2 + q) * NH + h) * KB + lane * 16, px);
        u32x2 pmv = {pack2(ms[0] * inv, ms[1] * inv), pack2(ms[2] * inv, ms[3] * inv)};
        st2c(rs, OFF_PMF + (((size_t)s * 2 + q) * 8 + (h >> 1)) * KB + lane * 16 + (h & 1) * 8, pmv);
    }
    signal_cnt(ws, CNT_P + s, lane);
}

DI void f2_task(char* __restrict__ ws, const rsrc_t rs, unsigned short* __restrict__ out, int s, int tp2, int lane) {
    const int i = lane & 15, g = lane >> 4;
    wait_cnt(ws, CNT_G + s, 224);
    wait_cnt(ws, CNT_F + s, 4);
    f32x4 acc[2][2];
    bf16x8 ua[2][NH];
    {
        const size_t up = OFF_UF + ((size_t)s * NT16 + 2 * tp2) * NH * KB + lane * 16;
#pragma unroll
        for (int t = 0; t < 2; ++t)
#pragma unroll
            for (int h = 0; h < NH; ++h) ua[t][h] = ld8c(rs, up + (size_t)(t * NH + h) * KB);
    }
#pragma unroll
    for (int q = 0; q < 2; ++q)
#pragma unroll
        for (int t = 0; t < 2; ++t)
            acc[q][t] = __builtin_bit_cast(f32x4, ld4c(rs, OFF_FO + ((size_t)(32 * s + 16 * q + i) * DM + 16 * (2 * tp2 + t) + 4 * g) * 4));
    wait_cnt_slow(ws, CNT_PROG, (unsigned)(s | 1));
    wait_cnt(ws, CNT_Y + (s >> 1) * 7 + (tp2 >> 1), 16);
    wait_cnt(ws, CNT_P + s, 16);
    {
        const size_t pp = OFF_PXF + (size_t)s * 2 * NH * KB + lane * 16;
        bf16x8 p0[NH], p1[NH];
#pragma unroll
        for (int h = 0; h < NH; ++h) { p0[h] = ld8c(rs, pp + (size_t)h * KB); p1[h] = ld8c(rs, pp + (size_t)(NH + h) * KB); }
        const size_t yp = OFF_YF + ((size_t)s * NT16 + 2 * tp2) * 8 * 512 + (g & 1) * 256 + i * 16;
        const size_t pq = OFF_PMF + (size_t)s * 2 * 8 * KB + lane * 16;
        bf16x8 ya[2][8], m0[8], m1[8];
#pragma unroll
        for (int hp = 0; hp < 8; ++hp) {
            m0[hp] = ld8c(rs, pq + (size_t)hp * KB); m1[hp] = ld8c(rs, pq + (size_t)(8 + hp) * KB);
            ya[0][hp] = ld8c(rs, yp + (size_t)hp * 512); ya[1][hp] = ld8c(rs, yp + (size_t)(8 + hp) * 512);
        }
#pragma unroll
        for (int h = 0; h < NH; ++h)
#pragma unroll
            for (int t = 0; t < 2; ++t) {
                acc[0][t] = mfma32(ua[t][h], p0[h], acc[0][t]);
                acc[1][t] = mfma32(ua[t][h], p1[h], acc[1][t]);
            }
#pragma unroll
        for (int hp = 0; hp < 8; ++hp)
#pragma unroll
            for (int t = 0; t < 2; ++t) {
                acc[0][t] = mfma32(ya[t][hp], m0[hp], acc[0][t]);
                acc[1][t] = mfma32(ya[t][hp], m1[hp], acc[1][t]);
            }
    }
#pragma unroll
    for (int q = 0; q < 2; ++q)
#pragma unroll
        for (int t = 0; t < 2; ++t) {
            const size_t o = (size_t)(32 * s + 16 * q + i) * DM + 16 * (2 * tp2 + t) + 4 * g;
            u32x2 r = {pack2(acc[q][t][0], acc[q][t][1]), pack2(acc[q][t][2], acc[q][t][3])};
            *(u32x2*)(out + o) = r;
        }
}

__global__ __launch_bounds__(256, 1) void k_mega(const unsigned short* __restrict__ x, const unsigned short* __restrict__ b1, const unsigned short* __restrict__ b2,
                                                 char* __restrict__ ws, unsigned short* __restrict__ out) {
    extern __shared__ __attribute__((aligned(16))) char lds[];
    const rsrc_t rs = __builtin_amdgcn_make_buffer_rsrc(ws, 0, (int)WS_END, 0x00020000);
#define WSTAMP(k)
    WSTAMP(0);
    if ((blockIdx.x & 7) == 0) { chain_role(ws, lds, rs); WSTAMP(3); return; }
    const int wid = (blockIdx.x >> 3) * 7 + (blockIdx.x & 7) - 1;
    const int wave = __builtin_amdgcn_readfirstlane(threadIdx.x >> 6), lane = threadIdx.x & 63;
    const int tile0 = wid * 4;
    if (tile0 < 448) w1_phase<0>(ws, rs, tile0, wave, lane);
    else w1_phase<1>(ws, rs, tile0, wave, lane);
    WSTAMP(1);
    if (wid >= 160) ffn_task(ws, lds, rs, x, b1, b2, wid - 160, wave, lane);
    WSTAMP(2);
    if (wave == 0) { w3_loop(ws, rs, tile0, lane); WSTAMP(3); return; }
    const int fw = wid * 3 + wave - 1;
    for (int b = 0; b < 32; ++b) {
        {
            const int k = (fw + 672 * 2 - b * 32) % 672;
            if (k < 32) f1_task(ws, rs, 2 * b + (k >> 4), k & 15, lane);
        }
        {
            const int k = (fw + 672 * 2 - 336 - b * 28) % 672;
            if (k < 28) f2_task(ws, rs, out, 2 * b + (k / 14), k % 14, lane);
        }
    }
    WSTAMP(3);
}

extern "C" void kernel_launch(void* const* d_in, const int* in_sizes, int n_in,
                              void* d_out, int out_size, void* d_ws, size_t ws_size,
                              hipStream_t stream) {
    const unsigned short* x = (const unsigned short*)d_in[0];
    const unsigned short* mem0 = (const unsigned short*)d_in[1];
    const unsigned short* Wq = (const unsigned short*)d_in[2];
    const unsigned short* Wk = (const unsigned short*)d_in[3];
    const unsigned short* Wv = (const unsigned short*)d_in[4];
    const unsigned short* Wo = (const unsigned short*)d_in[5];
    const unsigned short* w1 = (const unsigned short*)d_in[8];
    const unsigned short* b1 = (const unsigned short*)d_in[9];
    const unsigned short* w2 = (const unsigned short*)d_in[10];
    const unsigned short* b2 = (const unsigned short*)d_in[11];
    char* ws = (char*)d_ws;
    unsigned short* out = (unsigned short*)d_out;
    (void)hipFuncSetAttribute((const void*)k_mega, hipFuncAttributeMaxDynamicSharedMemorySize, CH_LDS_SIZE);
    k_prep<<<PREP_GRID, PREP_NT, 0, stream>>>(x, mem0, Wq, Wk, Wv, Wo, w1, w2, ws);
    k_mega<<<256, 256, CH_LDS_SIZE, stream>>>(x, b1, b2, ws, out);
}
```

```cpp
#include <hip/hip_runtime.h>
#include <hip/hip_bf16.h>

typedef __attribute__((ext_vector_type(8))) short bf16x8;
typedef __attribute__((ext_vector_type(4))) short bf16x4;
typedef __attribute__((ext_vector_type(4))) float f32x4;
typedef __attribute__((ext_vector_type(4))) unsigned u32x4;
typedef __attribute__((ext_vector_type(2))) unsigned u32x2;
typedef __attribute__((ext_vector_type(2))) __bf16 bf2_t;
#define DI __device__ __forceinline__

constexpr int DM = 448, KK = 14, NT16 = 28, NH = 16, NSEG = 64, NRT = 128, FFD = 1792, KF = 56;
constexpr size_t KB = 1024;
constexpr size_t OFF_XNF = 0;
constexpr size_t OFF_WF = OFF_XNF + (size_t)NRT * KK * KB;
constexpr size_t OFF_ANF = OFF_WF + (size_t)1008 * KK * KB;
constexpr size_t OFF_W2F = OFF_ANF + (size_t)448 * KK * KB;
constexpr size_t OFF_GTF = OFF_W2F + (size_t)28 * KF * KB;
constexpr size_t OFF_UF = OFF_GTF + (size_t)NH * NRT * KK * KB;
constexpr size_t OFF_FO = OFF_UF + (size_t)NSEG * NT16 * NH * KB;
constexpr size_t OFF_MEMF = OFF_FO + (size_t)2048 * DM * 4;
constexpr size_t OFF_SMX = OFF_MEMF + (size_t)NSEG * 7168;
constexpr int E2B = 640, E2S = 32 * E2B;
constexpr size_t OFF_T2F = OFF_SMX + (size_t)NSEG * E2S;
constexpr size_t OFF_YF = OFF_T2F + (size_t)NH * NSEG * 7168;
constexpr size_t OFF_PXF = OFF_YF + (size_t)NSEG * NT16 * 8 * 512;
constexpr size_t OFF_PMF = OFF_PXF + (size_t)NSEG * 2 * NH * KB;
constexpr size_t OFF_CNT = OFF_PMF + (size_t)NSEG * 2 * 8 * KB;
constexpr int CNT_G = 0, CNT_P = 64, CNT_F = 128, CNT_TMO = 192, CNT_PROG = 193, CNT_T2 = 200, CNT_Y = CNT_T2 + 32 * 16, CNT_N = CNT_Y + 32 * 7;
constexpr size_t OFF_TMO = OFF_CNT + (size_t)CNT_TMO * 128;
constexpr size_t WS_END = OFF_CNT + (size_t)CNT_N * 128;
constexpr size_t OFF_DBG = WS_END;
#define STAMP(k)

constexpr unsigned SENT = 0xFFFFFFFFu;
constexpr unsigned kSpinCap = 1u << 16;

DI unsigned pack2(float a, float b) { bf2_t v = {(__bf16)a, (__bf16)b}; return __builtin_bit_cast(unsigned, v); }
DI float bf_lo(unsigned u) { return __uint_as_float(u << 16); }
DI float bf_hi(unsigned u) { return __uint_as_float(u & 0xFFFF0000u); }
DI f32x4 mfma32(bf16x8 a, bf16x8 b, f32x4 c) { return __builtin_amdgcn_mfma_f32_16x16x32_bf16(a, b, c, 0, 0, 0); }
DI bf16x8 ldfrag(const char* p) { return *(const bf16x8*)p; }
DI bf16x8 pack8(f32x4 a, f32x4 b) {
    u32x4 r = {pack2(a[0], a[1]), pack2(a[2], a[3]), pack2(b[0], b[1]), pack2(b[2], b[3])};
    return __builtin_bit_cast(bf16x8, r);
}
DI bf16x4 pack4(f32x4 a) { u32x2 r = {pack2(a[0], a[1]), pack2(a[2], a[3])}; return __builtin_bit_cast(bf16x4, r); }
DI bool spin_fail(unsigned& spins, unsigned* tmo, unsigned code = 1u) {
    if (++spins > kSpinCap) { unsigned exp_ = 0u; __hip_atomic_compare_exchange_strong(tmo, &exp_, code, __ATOMIC_RELAXED, __ATOMIC_RELAXED, __HIP_MEMORY_SCOPE_AGENT); return true; }
    if ((spins & 63u) == 0u) return __builtin_amdgcn_readfirstlane(__hip_atomic_load(tmo, __ATOMIC_RELAXED, __HIP_MEMORY_SCOPE_AGENT)) != 0u;
    return false;
}
typedef __amdgpu_buffer_rsrc_t rsrc_t;
DI bf16x8 ld8c(rsrc_t rs, size_t off) { return __builtin_bit_cast(bf16x8, __builtin_amdgcn_raw_buffer_load_b128(rs, (unsigned)off, 0, 16)); }
DI u32x4 ld4c(rsrc_t rs, size_t off) { return __builtin_amdgcn_raw_buffer_load_b128(rs, (unsigned)off, 0, 16); }
DI u32x2 ld2c(rsrc_t rs, size_t off) { return __builtin_amdgcn_raw_buffer_load_b64(rs, (unsigned)off, 0, 16); }
DI unsigned ld1c(rsrc_t rs, size_t off) { return __builtin_amdgcn_raw_buffer_load_b32(rs, (unsigned)off, 0, 16); }
DI void st8c(rsrc_t rs, size_t off, bf16x8 v) { __builtin_amdgcn_raw_buffer_store_b128(__builtin_bit_cast(u32x4, v), rs, (unsigned)off, 0, 16); }
DI void st4c(rsrc_t rs, size_t off, u32x4 v) { __builtin_amdgcn_raw_buffer_store_b128(v, rs, (unsigned)off, 0, 16); }
DI void st2c(rsrc_t rs, size_t off, u32x2 v) { __builtin_amdgcn_raw_buffer_store_b64(v, rs, (unsigned)off, 0, 16); }
DI void signal_cnt(char* ws, int idx, int lane) {
    asm volatile("s_waitcnt vmcnt(0)" ::: "memory");
    if (lane == 0) __hip_atomic_fetch_add((unsigned*)(ws + OFF_CNT + (size_t)idx * 128), 1u, __ATOMIC_RELAXED, __HIP_MEMORY_SCOPE_AGENT);
}
DI void wait_cnt(char* ws, int idx, unsigned target) {
    unsigned spins = 0;
    unsigned* p = (unsigned*)(ws + OFF_CNT + (size_t)idx * 128);
    for (;;) {
        unsigned v = __builtin_amdgcn_readfirstlane(__hip_atomic_load(p, __ATOMIC_RELAXED, __HIP_MEMORY_SCOPE_AGENT));
        if (v >= target) break;
        __builtin_amdgcn_s_sleep(8);
        if (spin_fail(spins, (unsigned*)(ws + OFF_TMO), 0x300u + idx)) break;
    }
    asm volatile("" ::: "memory");
}

DI void wait_cnt_slow(char* ws, int idx, unsigned target) {
    unsigned spins = 0;
    unsigned* p = (unsigned*)(ws + OFF_CNT + (size_t)idx * 128);
    for (;;) {
        unsigned v = __builtin_amdgcn_readfirstlane(__hip_atomic_load(p, __ATOMIC_RELAXED, __HIP_MEMORY_SCOPE_AGENT));
        if (v >= target) break;
        if (v + 1 < target) __builtin_amdgcn_s_sleep(100); else __builtin_amdgcn_s_sleep(12);
        if (spin_fail(spins, (unsigned*)(ws + OFF_TMO), 0x400u + idx)) break;
    }
    asm volatile("" ::: "memory");
}

DI float gelu_tanh(float z) {
    float u = 0.7978845608028654f * (z + 0.044715f * z * z * z);
    float e = __expf(2.f * u);
    float t = 1.f - 2.f / (1.f + e);
    return 0.5f * z * (1.f + t);
}

DI float xmax16(float v) { auto r = __builtin_amdgcn_permlane16_swap(__float_as_uint(v), __float_as_uint(v), false, false); return fmaxf(__uint_as_float(r[0]), __uint_as_float(r[1])); }
DI float xmax32(float v) { auto r = __builtin_amdgcn_permlane32_swap(__float_as_uint(v), __float_as_uint(v), false, false); return fmaxf(__uint_as_float(r[0]), __uint_as_float(r[1])); }
DI float xadd16(float v) { auto r = __builtin_amdgcn_permlane16_swap(__float_as_uint(v), __float_as_uint(v), false, false); return __uint_as_float(r[0]) + __uint_as_float(r[1]); }
DI float xadd32(float v) { auto r = __builtin_amdgcn_permlane32_swap(__float_as_uint(v), __float_as_uint(v), false, false); return __uint_as_float(r[0]) + __uint_as_float(r[1]); }
DI float qswap(float v) { return __uint_as_float(__builtin_amdgcn_update_dpp(0, __float_as_uint(v), 0xB1, 0xF, 0xF, true)); }
DI unsigned umax4(u32x4 v) { return max(max(v.x, v.y), max(v.z, v.w)); }
DI float fexp2(float x) { return __builtin_amdgcn_exp2f(x); }

DI void mfma_Aa0(f32x4& acc, const bf16x8& w, const bf16x8& m) { asm volatile("s_nop 1\n\tv_mfma_f32_16x16x32_bf16 %0, %1, %2, 0" : "=&v"(acc) : "a"(w), "v"(m)); }
DI void mfma_Aa(f32x4& acc, const bf16x8& w, const bf16x8& m) { asm volatile("s_nop 1\n\tv_mfma_f32_16x16x32_bf16 %0, %1, %2, %0" : "+v"(acc) : "a"(w), "v"(m)); }
DI void mfma_Ba0(f32x4& acc, const bf16x8& m, const bf16x8& w) { asm volatile("s_nop 1\n\tv_mfma_f32_16x16x32_bf16 %0, %1, %2, 0" : "=&v"(acc) : "v"(m), "a"(w)); }
DI void mfma_Ba(f32x4& acc, const bf16x8& m, const bf16x8& w) { asm volatile("s_nop 1\n\tv_mfma_f32_16x16x32_bf16 %0, %1, %2, %0" : "+v"(acc) : "v"(m), "a"(w)); }
DI void mfma_v0(f32x4& acc, const bf16x8& a, const bf16x8& b) { asm volatile("s_nop 1\n\tv_mfma_f32_16x16x32_bf16 %0, %1, %2, 0" : "=&v"(acc) : "v"(a), "v"(b)); }
DI void mfma_v(f32x4& acc, const bf16x8& a, const bf16x8& b) { asm volatile("s_nop 1\n\tv_mfma_f32_16x16x32_bf16 %0, %1, %2, %0" : "+v"(acc) : "v"(a), "v"(b)); }
DI void mfma16_v0(f32x4& acc, const bf16x4& a, const bf16x4& b) { asm volatile("s_nop 1\n\tv_mfma_f32_16x16x16_bf16 %0, %1, %2, 0" : "=&v"(acc) : "v"(a), "v"(b)); }
DI void mfma16_v(f32x4& acc, const bf16x4& a, const bf16x4& b) { asm volatile("s_nop 1\n\tv_mfma_f32_16x16x16_bf16 %0, %1, %2, %0" : "+v"(acc) : "v"(a), "v"(b)); }
DI void mfma_fence4(f32x4& a, f32x4& b, f32x4& c, f32x4& d) { asm volatile("s_nop 7\n\ts_nop 1" : "+v"(a), "+v"(b), "+v"(c), "+v"(d)); }
DI void pin(f32x4& a) { asm volatile("" : "+v"(a)); }
DI void mfma_fence1(f32x4& a) { asm volatile("s_nop 7\n\ts_nop 1" : "+v"(a)); }

constexpr int PREP_GRID = 256, PREP_NT = 448;

DI void ln_row(const unsigned short* __restrict__ x, char* __restrict__ ws, int row, int lane, const u32x2 (&u)[2]) {
    float v[2][4];
#pragma unroll
    for (int it = 0; it < 2; ++it) { v[it][0] = bf_lo(u[it].x); v[it][1] = bf_hi(u[it].x); v[it][2] = bf_lo(u[it].y); v[it][3] = bf_hi(u[it].y); }
    float sum = 0.f;
#pragma unroll
    for (int it = 0; it < 2; ++it) sum += v[it][0] + v[it][1] + v[it][2] + v[it][3];
#pragma unroll
    for (int o = 32; o >= 1; o >>= 1) sum += __shfl_xor(sum, o);
    const float mean = sum * (1.f / DM);
    float sq = 0.f;
#pragma unroll
    for (int it = 0; it < 2; ++it) {
        if (lane + it * 64 < 112) {
#pragma unroll
            for (int j = 0; j < 4; ++j) { float d = v[it][j] - mean; sq += d * d; }
        }
    }
#pragma unroll
    for (int o = 32; o >= 1; o >>= 1) sq += __shfl_xor(sq, o);
    const float rstd = 1.f / sqrtf(sq * (1.f / DM) + 1e-5f);
    const int rt = row >> 4, ii = row & 15;
#pragma unroll
    for (int it = 0; it < 2; ++it) {
        int grp = lane + it * 64;
        if (grp < 112) {
            int d = grp * 4, kk = d >> 5, dd = d & 31, half = dd >> 4, gg = (dd & 15) >> 2;
            u32x2 o = {pack2((v[it][0] - mean) * rstd, (v[it][1] - mean) * rstd), pack2((v[it][2] - mean) * rstd, (v[it][3] - mean) * rstd)};
            *(u32x2*)(ws + OFF_XNF + ((size_t)(rt * KK + kk) * 64 + (ii + 16 * gg)) * 16 + half * 8) = o;
        }
    }
}

DI bf16x8 w12_load(const unsigned short* __restrict__ w1, const unsigned short* __restrict__ w2, int f, int i, int g) {
    bf16x8 v;
    if (f < 112 * KK) {
        int tf = f / KK, kk = f % KK;
#pragma unroll
        for (int j = 0; j < 8; ++j) v[j] = (short)w1[(size_t)(32 * kk + 16 * (j >> 2) + 4 * g + (j & 3)) * FFD + 16 * tf + i];
    } else {
        int f2 = f - 112 * KK;
        int t = f2 / KF, kf = f2 % KF;
#pragma unroll
        for (int j = 0; j < 8; ++j) v[j] = (short)w2[(size_t)(32 * kf + 16 * (j >> 2) + 4 * g + (j & 3)) * DM + 16 * t + i];
    }
    return v;
}
DI void w12_store(char* __restrict__ ws, int f, int lane, bf16x8 v) {
    if (f < 112 * KK) *(bf16x8*)(ws + OFF_WF + ((size_t)896 * KK + f) * KB + lane * 16) = v;
    else *(bf16x8*)(ws + OFF_W2F + (size_t)(f - 112 * KK) * KB + lane * 16) = v;
}

__global__ __launch_bounds__(PREP_NT, 2) void k_prep(const unsigned short* __restrict__ x, const unsigned short* __restrict__ mem0,
                                              const unsigned short* __restrict__ Wq, const unsigned short* __restrict__ Wk,
                                              const unsigned short* __restrict__ Wv, const unsigned short* __restrict__ Wo,
                                              const unsigned short* __restrict__ w1, const unsigned short* __restrict__ w2, char* __restrict__ ws) {
    __shared__ __attribute__((aligned(16))) char sm[64 * 1024];
    const int wave = __builtin_amdgcn_readfirstlane(threadIdx.x >> 6), lane = threadIdx.x & 63;
    const int i = lane & 15, g = lane >> 4;
    const int b = blockIdx.x;
    const int gw = b * 7 + wave;
    const bool hasU = b < 224;
    const int xc = b & 7, k28 = b >> 3;
    const int h = 2 * xc + (k28 % 14) / 7, ch = k28 % 7;
    const bool isA = k28 < 14;
    bf16x8 st[4][8];
    bf16x8 shf[5];
    u32x4 wot[5];
    if (hasU) {
        const unsigned short* ms = isA ? Wk : Wv;
#pragma unroll
        for (int t = 0; t < 4; ++t) {
            const unsigned short* p = ms + (size_t)(64 * wave + 16 * t + i) * 4096 + h * 256 + 8 * g;
#pragma unroll
            for (int ke = 0; ke < 8; ++ke) st[t][ke] = *(const bf16x8*)(p + ke * 32);
        }
        if (isA) {
#pragma unroll
            for (int n = 0; n < 5; ++n) {
                const int f = min(wave + 7 * n, 31);
                shf[n] = *(const bf16x8*)(Wq + (size_t)(64 * ch + 16 * (f >> 3) + i) * 4096 + h * 256 + (f & 7) * 32 + 8 * g);
            }
        } else {
#pragma unroll
            for (int n = 0; n < 5; ++n) {
                const int e = min((int)threadIdx.x + n * PREP_NT, 2047), row = e >> 3, c8 = e & 7;
                wot[n] = *(const u32x4*)(Wo + ((size_t)h * 256 + row) * DM + 64 * ch + c8 * 8);
            }
        }
    }
    u32x2 xr[2][2];
#pragma unroll
    for (int n = 0; n < 2; ++n) {
        const int row = gw + n * 1792;
#pragma unroll
        for (int it = 0; it < 2; ++it) {
            const int grp = lane + it * 64;
            xr[n][it] = (u32x2){0u, 0u};
            if (row < 2048 && grp < 112) xr[n][it] = *(const u32x2*)(x + (size_t)row * DM + grp * 4);
        }
    }
    bf16x8 wv[2];
#pragma unroll
    for (int n = 0; n < 2; ++n) { const int f = gw + n * 1792; wv[n] = w12_load(w1, w2, f < 3136 ? f : 0, i, g); }
    {
        const size_t n16 = ((size_t)(NSEG - 1) * 7168 + (size_t)NSEG * E2S) / 16;
        u32x4 sv = {SENT, SENT, SENT, SENT};
        for (size_t idx = (size_t)b * PREP_NT + threadIdx.x; idx < n16; idx += (size_t)PREP_GRID * PREP_NT)
            *(u32x4*)(ws + OFF_MEMF + 7168 + idx * 16) = sv;
    }
    if (b == 255) {
        for (int e = threadIdx.x; e < 8 * 112; e += PREP_NT) {
            int r = e / 112, grp = e % 112;
            int d = grp * 4, t = d >> 4, gg = (d & 15) >> 2;
            u32x2 u = *(const u32x2*)(mem0 + r * DM + d);
            *(u32x2*)(ws + OFF_MEMF + t * 256 + r * 32 + gg * 8) = u;
        }
    }
    if (b == 254) {
        for (int e = threadIdx.x; e < CNT_N; e += PREP_NT) *(unsigned*)(ws + OFF_CNT + (size_t)e * 128) = 0u;
    }
    if (hasU) {
        if (isA) {
#pragma unroll
            for (int n = 0; n < 5; ++n) { const int f = wave + 7 * n; if (f < 32) *(bf16x8*)(sm + f * 1024 + lane * 16) = shf[n]; }
        } else {
#pragma unroll
            for (int n = 0; n < 5; ++n) { const int e = threadIdx.x + n * PREP_NT; if (e < 2048) *(u32x4*)(sm + 32768 + e * 16) = wot[n]; }
            __syncthreads();
            const unsigned short* wo_t = (const unsigned short*)(sm + 32768);
#pragma unroll
            for (int n = 0; n < 5; ++n) {
                const int f = wave + 7 * n;
                if (f < 32) {
                    bf16x8 v;
#pragma unroll
                    for (int j = 0; j < 8; ++j) v[j] = (short)wo_t[((f & 7) * 32 + 8 * g + j) * 64 + 16 * (f >> 3) + i];
                    *(bf16x8*)(sm + f * 1024 + lane * 16) = v;
                }
            }
        }
        __syncthreads();
        if (isA) {
#pragma unroll
            for (int ap = 0; ap < 2; ++ap) {
                bf16x8 qf[2][8];
#pragma unroll
                for (int a = 0; a < 2; ++a)
#pragma unroll
                    for (int ke = 0; ke < 8; ++ke) qf[a][ke] = *(const bf16x8*)(sm + ((2 * ap + a) * 8 + ke) * 1024 + lane * 16);
#pragma unroll
                for (int a = 0; a < 2; ++a)
#pragma unroll
                    for (int bp = 0; bp < 2; ++bp) {
                        f32x4 d0 = {0.f, 0.f, 0.f, 0.f}, d1 = {0.f, 0.f, 0.f, 0.f};
#pragma unroll
                        for (int ke = 0; ke < 8; ++ke) { d0 = mfma32(st[2 * bp][ke], qf[a][ke], d0); d1 = mfma32(st[2 * bp + 1][ke], qf[a][ke], d1); }
                        *(bf16x8*)(ws + OFF_WF + ((size_t)(h * 28 + 4 * ch + 2 * ap + a) * KK + 2 * wave + bp) * KB + lane * 16) = pack8(d0, d1);
                    }
#pragma unroll
                for (int bb = 0; bb < 4; ++bb) {
                    f32x4 d0 = {0.f, 0.f, 0.f, 0.f}, d1 = {0.f, 0.f, 0.f, 0.f};
#pragma unroll
                    for (int ke = 0; ke < 8; ++ke) { d0 = mfma32(qf[0][ke], st[bb][ke], d0); d1 = mfma32(qf[1][ke], st[bb][ke], d1); }
                    *(bf16x8*)(ws + OFF_ANF + ((size_t)(h * 28 + 4 * wave + bb) * KK + 2 * ch + ap) * KB + lane * 16) = pack8(d0, d1);
                }
            }
        } else {
#pragma unroll
            for (int ct = 0; ct < 4; ++ct) {
                bf16x8 fix[8];
#pragma unroll
                for (int ke = 0; ke < 8; ++ke) fix[ke] = *(const bf16x8*)(sm + (ct * 8 + ke) * 1024 + lane * 16);
#pragma unroll
                for (int dp = 0; dp < 2; ++dp) {
                    f32x4 d0 = {0.f, 0.f, 0.f, 0.f}, d1 = {0.f, 0.f, 0.f, 0.f};
#pragma unroll
                    for (int ke = 0; ke < 8; ++ke) { d0 = mfma32(st[2 * dp][ke], fix[ke], d0); d1 = mfma32(st[2 * dp + 1][ke], fix[ke], d1); }
                    *(bf16x8*)(ws + OFF_WF + ((size_t)(448 + h * 28 + 4 * ch + ct) * KK + 2 * wave + dp) * KB + lane * 16) = pack8(d0, d1);
                }
            }
        }
    }
    ln_row(x, ws, gw, lane, xr[0]);
    if (gw + 1792 < 2048) ln_row(x, ws, gw + 1792, lane, xr[1]);
    w12_store(ws, gw, lane, wv[0]);
    if (gw + 1792 < 3136) w12_store(ws, gw + 1792, lane, wv[1]);
}

constexpr int CH_LDS_B = 0;
constexpr int CH_LDS_MEM = 112 * 1024;
constexpr int CH_LDS_RA = CH_LDS_MEM + 7168;
constexpr int CH_LDS_RB = CH_LDS_RA + 8192;
constexpr int CH_LDS_PW = CH_LDS_RB + 4096;
constexpr int CH_LDS_SIZE = CH_LDS_PW + 4096;

#define E2POS 5
#define DNB 8
#define ADMAX 8
#define D1 8
#define D2 0

template <bool B> struct BoolC { static constexpr bool value = B; };
DI void chain_role(char* __restrict__ ws, char* lds, const rsrc_t rs) {
    const int c = blockIdx.x >> 3;
    const int tid = threadIdx.x, wave = __builtin_amdgcn_readfirstlane(tid >> 6), lane = tid & 63;
    const int i = lane & 15, g = lane >> 4;
    const int h = c >> 1, half = c & 1;
    const bool hasB = c < NT16;
    const int tB = hasB ? c : 0;
    unsigned* tmo = (unsigned*)(ws + OFF_TMO);
    const float cs = 0.0625f * 1.4426950408889634f;

    bf16x8 wa[3][KK], wsh[7], wb[2][KK];
    {
        const char* ap = ws + OFF_ANF + (size_t)(h * 28 + 14 * half) * KK * KB + lane * 16;
#pragma unroll
        for (int t = 0; t < 3; ++t)
#pragma unroll
            for (int kk = 0; kk < KK; ++kk) wa[t][kk] = ldfrag(ap + (size_t)((3 * wave + t) * KK + kk) * KB);
#pragma unroll
        for (int k = 0; k < 7; ++k) wsh[k] = ldfrag(ap + (size_t)((12 + (wave >> 1)) * KK + 7 * (wave & 1) + k) * KB);
    }
    const int gk0 = wave < 2 ? 4 * wave : 8 + 3 * (wave - 2);
    const int gkn = wave < 2 ? 4 : 3;
    bf16x8 gf[4], uf[4];
    wait_cnt(ws, CNT_G + 0, 224);
    __syncthreads();
    {
        const size_t gp = OFF_GTF + (((size_t)h * NRT + half) * KK + gk0) * KB + lane * 16;
#pragma unroll
        for (int k = 0; k < 4; ++k) gf[k] = ld8c(rs, gp + (size_t)(k < gkn ? k : 0) * KB);
    }
    const char* mp = lds + CH_LDS_MEM + g * 128 + (i & 7) * 16;
    const int hh2 = lane >> 4, r2 = (lane >> 1) & 7, q2 = lane & 1;

    int d1v = hasB ? D1 : DNB, d2v = D2;
    auto step = [&](const int s, auto FIRST) __attribute__((always_inline)) {
        STAMP(0);
        {
            u32x4 x0, x1;
            unsigned spins = 0;
            const size_t o0 = OFF_MEMF + (size_t)(s * 7168 + tid * 16), o1 = o0 + (tid < 192 ? 4096 : 0);
            const size_t oc = OFF_CNT + (size_t)(CNT_G + s + 1) * 128;
            if (s > 0) for (int d = 0; d < d1v; ++d) __builtin_amdgcn_s_sleep(1);
            for (;;) {
                asm volatile("" ::: "memory");
                x0 = ld4c(rs, o0);
                x1 = ld4c(rs, o1);
                unsigned cv = ld1c(rs, oc);
                if (__all((max(umax4(x0), umax4(x1)) != SENT) && (cv >= 224u))) break;
                if (spin_fail(spins, tmo, 0x100u + s)) break;
            }
            if (hasB) { if (spins != 0u && s > 0 && d1v < D1 + ADMAX) d1v += 2; }
            else { if (spins == 0u) { if (d1v > 8) d1v -= 1; } else if (spins > 1u) d1v += 4; }
            const int t0 = tid >> 4, rr = (tid >> 1) & 7, gp = tid & 1;
            char* d0 = lds + CH_LDS_MEM + (t0 >> 1) * 512 + gp * 256 + rr * 16 + (t0 & 1) * 8;
            *(u32x2*)(d0) = (u32x2){x0.x, x0.y};
            *(u32x2*)(d0 + 128) = (u32x2){x0.z, x0.w};
            if (tid < 192) {
                *(u32x2*)(d0 + 8 * 512) = (u32x2){x1.x, x1.y};
                *(u32x2*)(d0 + 8 * 512 + 128) = (u32x2){x1.z, x1.w};
            }
        }
        {
            const size_t up = OFF_UF + (((size_t)s * NT16 + tB) * NH + 4 * wave) * KB + lane * 16;
#pragma unroll
            for (int k = 0; k < 4; ++k) uf[k] = ld8c(rs, up + (size_t)k * KB);
        }
        STAMP(1);
        __syncthreads();
        STAMP(2);
        if (c == 31 && tid == 255) __hip_atomic_store((unsigned*)(ws + OFF_CNT + (size_t)CNT_PROG * 128), (unsigned)s, __ATOMIC_RELAXED, __HIP_MEMORY_SCOPE_AGENT);
        bf16x8 mf[KK];
#pragma unroll
        for (int kk = 0; kk < KK; ++kk) mf[kk] = *(const bf16x8*)(mp + kk * 512);
        {
            bf16x8 mg[4];
            bf16x4 mk[4];
#pragma unroll
            for (int k = 0; k < 4; ++k) mg[k] = *(const bf16x8*)(mp + (gk0 + (k < gkn ? k : 0)) * 512);
#pragma unroll
            for (int t = 0; t < 4; ++t) {
                const int tl = (t < 3) ? 3 * wave + t : 12 + (wave >> 1);
                const int tg = 14 * half + tl;
                mk[t] = *(const bf16x4*)(mp + (tg >> 1) * 512 + (tg & 1) * 8);
            }
            f32x4 ta[4];
            f32x4 sx, sm;
            if (wave & 1) {
                mfma_Aa0(ta[3], wsh[0], mf[7]);
#pragma unroll
                for (int k = 1; k < 7; ++k) mfma_Aa(ta[3], wsh[k], mf[7 + k]);
            } else {
                mfma_Aa0(ta[3], wsh[0], mf[0]);
#pragma unroll
                for (int k = 1; k < 7; ++k) mfma_Aa(ta[3], wsh[k], mf[k]);
            }
            mfma_Aa0(ta[0], wa[0][0], mf[0]);
#pragma unroll
            for (int kk = 1; kk < KK; ++kk) mfma_Aa(ta[0], wa[0][kk], mf[kk]);
            mfma_Aa0(ta[1], wa[1][0], mf[0]);
#pragma unroll
            for (int kk = 1; kk < 4; ++kk) mfma_Aa(ta[1], wa[1][kk], mf[kk]);
            pin(ta[3]); pin(ta[0]);
            mfma16_v0(sm, mk[3], pack4(ta[3]));
            mfma16_v(sm, mk[0], pack4(ta[0]));
#pragma unroll
            for (int kk = 4; kk < KK; ++kk) mfma_Aa(ta[1], wa[1][kk], mf[kk]);
            mfma_Aa0(ta[2], wa[2][0], mf[0]);
#pragma unroll
            for (int kk = 1; kk < 4; ++kk) mfma_Aa(ta[2], wa[2][kk], mf[kk]);
            pin(ta[1]);
            mfma16_v(sm, mk[1], pack4(ta[1]));
#pragma unroll
            for (int kk = 4; kk < KK; ++kk) mfma_Aa(ta[2], wa[2][kk], mf[kk]);
            mfma_v0(sx, gf[0], mg[0]);
            mfma_v(sx, gf[1], mg[1]);
            mfma_v(sx, gf[2], mg[2]);
            if (wave < 2) mfma_v(sx, gf[3], mg[3]);
            pin(ta[2]);
            mfma16_v(sm, mk[2], pack4(ta[2]));
            mfma_fence4(sm, sx, ta[0], ta[1]);
            *(f32x4*)(lds + CH_LDS_RA + (wave * 2 + 0) * 1024 + lane * 16) = sx;
            *(f32x4*)(lds + CH_LDS_RA + (wave * 2 + 1) * 1024 + lane * 16) = sm;
        }
        STAMP(3);
        __syncthreads();
        const size_t e2w = OFF_SMX + (size_t)s * E2S + (size_t)c * E2B;
        if (wave < 2) {
            f32x4 v0 = *(const f32x4*)(lds + CH_LDS_RA + (0 + wave) * 1024 + lane * 16);
            f32x4 v1 = *(const f32x4*)(lds + CH_LDS_RA + (2 + wave) * 1024 + lane * 16);
            f32x4 v2 = *(const f32x4*)(lds + CH_LDS_RA + (4 + wave) * 1024 + lane * 16);
            f32x4 v3 = *(const f32x4*)(lds + CH_LDS_RA + (6 + wave) * 1024 + lane * 16);
            f32x4 v = (v0 + v1) + (v2 + v3);
            if (wave == 0) {
                float m = fmaxf(fmaxf(v[0], v[1]), fmaxf(v[2], v[3]));
                m = xmax16(m); m = xmax32(m);
                const float mc = m * cs;
                float e0 = fexp2(v[0] * cs - mc), e1 = fexp2(v[1] * cs - mc), e2 = fexp2(v[2] * cs - mc), e3 = fexp2(v[3] * cs - mc);
                u32x2 pk = {pack2(e0, e1), pack2(e2, e3)};
                if (i < 8) st2c(rs, e2w + i * 32 + g * 8, pk);
                float sum = (bf_lo(pk.x) + bf_hi(pk.x)) + (bf_lo(pk.y) + bf_hi(pk.y));
                sum = xadd16(sum); sum = xadd32(sum);
                u32x2 st = {__float_as_uint(m), __float_as_uint(sum)};
                if (lane < 8) st2c(rs, e2w + 512 + i * 8, st);
            } else {
                if (i < 8 && g < 2) st4c(rs, e2w + 256 + i * 32 + g * 16, __builtin_bit_cast(u32x4, v));
            }
        }
        {
            const size_t gp = OFF_GTF + (((size_t)h * NRT + 2 * (s + 1) + half) * KK + gk0) * KB + lane * 16;
#pragma unroll
            for (int k = 0; k < 4; ++k) gf[k] = ld8c(rs, gp + (size_t)(k < gkn ? k : 0) * KB);
        }
        if constexpr (decltype(FIRST)::value) {
            const char* bp = ws + OFF_WF + (size_t)(448 + tB) * KK * KB + lane * 16;
#pragma unroll
            for (int hh = 0; hh < 2; ++hh)
#pragma unroll
                for (int kk = 0; kk < KK; ++kk)
                    *(bf16x8*)(lds + CH_LDS_B + (size_t)((wave * 2 + hh) * KK + kk) * KB + lane * 16) = ldfrag(bp + (size_t)((4 * wave + 2 + hh) * 28 * KK + kk) * KB);
#pragma unroll
            for (int hh = 0; hh < 2; ++hh)
#pragma unroll
                for (int kk = 0; kk < KK; ++kk) wb[hh][kk] = ldfrag(bp + (size_t)((4 * wave + hh) * 28 * KK + kk) * KB);
        }
        if (hasB) {
            f32x4 ya[4];
            const char* lb = lds + CH_LDS_B + (size_t)(wave * 2) * KK * KB + lane * 16;
            mfma_Ba0(ya[0], mf[0], wb[0][0]);
            mfma_v0(ya[1], mf[0], wb[1][0]);
#pragma unroll
            for (int k = 1; k < KK; ++k) {
                mfma_Ba(ya[0], mf[k], wb[0][k]);
                mfma_v(ya[1], mf[k], wb[1][k]);
            }
            STAMP(4);
            u32x4 qx[4], qm0, qm1, qs;
            const size_t e2r = OFF_SMX + (size_t)s * E2S;
            const size_t ox = e2r + (size_t)(8 * wave) * E2B + (i & 7) * 32 + g * 8;
            const size_t om = e2r + (size_t)(8 * wave + 2 * hh2) * E2B + 256 + r2 * 32 + q2 * 16;
            const size_t os = e2r + (size_t)(8 * wave + 2 * hh2) * E2B + 512 + r2 * 8;
#define E2_LOADS() do { _Pragma("unroll") for (int hh = 0; hh < 4; ++hh) { \
                        u32x2 lo_ = ld2c(rs, ox + (size_t)(2 * hh) * E2B), hi_ = ld2c(rs, ox + (size_t)(2 * hh + 1) * E2B); \
                        qx[hh] = (u32x4){lo_.x, lo_.y, hi_.x, hi_.y}; } \
                    qm0 = ld4c(rs, om); qm1 = ld4c(rs, om + E2B); \
                    { u32x2 lo_ = ld2c(rs, os), hi_ = ld2c(rs, os + E2B); qs = (u32x4){lo_.x, lo_.y, hi_.x, hi_.y}; } } while (0)
            {
                bf16x8 lq[2][4];
#pragma unroll
                for (int k = 0; k < 2; ++k) { lq[0][2 * k] = *(const bf16x8*)(lb + (size_t)k * KB); lq[0][2 * k + 1] = *(const bf16x8*)(lb + (size_t)(KK + k) * KB); }
#pragma unroll
                for (int gq = 0; gq < 7; ++gq) {
                    if (gq < 6) {
#pragma unroll
                        for (int k = 0; k < 2; ++k) {
                            lq[(gq + 1) & 1][2 * k] = *(const bf16x8*)(lb + (size_t)(2 * gq + 2 + k) * KB);
                            lq[(gq + 1) & 1][2 * k + 1] = *(const bf16x8*)(lb + (size_t)(KK + 2 * gq + 2 + k) * KB);
                        }
                    }
                    if (gq == E2POS) { for (int d = 0; d < d2v; ++d) __builtin_amdgcn_s_sleep(1); asm volatile("" ::: "memory"); E2_LOADS(); }
                    __builtin_amdgcn_sched_barrier(0);
                    if (gq == 0) { mfma_v0(ya[2], mf[0], lq[0][0]); mfma_v0(ya[3], mf[0], lq[0][1]); }
                    else { mfma_v(ya[2], mf[2 * gq], lq[gq & 1][0]); mfma_v(ya[3], mf[2 * gq], lq[gq & 1][1]); }
                    mfma_v(ya[2], mf[2 * gq + 1], lq[gq & 1][2]); mfma_v(ya[3], mf[2 * gq + 1], lq[gq & 1][3]);
                }
            }
            mfma_fence4(ya[0], ya[1], ya[2], ya[3]);
            bf16x8 yp0 = pack8(ya[0], ya[1]), yp1 = pack8(ya[2], ya[3]);
            __builtin_amdgcn_sched_barrier(0);
            {
                unsigned spins = 0;
                if (E2POS >= 7) { for (int d = 0; d < d2v; ++d) __builtin_amdgcn_s_sleep(1); asm volatile("" ::: "memory"); E2_LOADS(); }
                for (;;) {
                    unsigned mx = max(max(max(umax4(qx[0]), umax4(qx[1])), max(umax4(qx[2]), umax4(qx[3]))), max(max(umax4(qm0), umax4(qm1)), umax4(qs)));
                    if (__all(mx != SENT)) break;
                    if (spin_fail(spins, tmo, 0x200u + s)) break;
                    asm volatile("" ::: "memory");
                    E2_LOADS();
                }
                if (spins != 0u && s > 0 && d2v < D2 + ADMAX) d2v += 2;
            }
            STAMP(5);
            f32x4 ms = __builtin_bit_cast(f32x4, qm0) + __builtin_bit_cast(f32x4, qm1);
            const float m0 = __uint_as_float(qs.x), s0 = __uint_as_float(qs.y), m1 = __uint_as_float(qs.z), s1 = __uint_as_float(qs.w);
            float mx = fmaxf(fmaxf(ms[0], ms[1]), fmaxf(ms[2], ms[3]));
            mx = fmaxf(mx, qswap(mx));
            const float Mc = fmaxf(mx, fmaxf(m0, m1)) * cs;
            float e0 = fexp2(ms[0] * cs - Mc), e1 = fexp2(ms[1] * cs - Mc), e2 = fexp2(ms[2] * cs - Mc), e3 = fexp2(ms[3] * cs - Mc);
            float f0 = fexp2(m0 * cs - Mc), f1 = fexp2(m1 * cs - Mc);
            float sm = (e0 + e1) + (e2 + e3);
            sm += qswap(sm);
            const float inv = __builtin_amdgcn_rcpf(sm + f0 * s0 + f1 * s1);
            char* pw = lds + CH_LDS_PW + wave * 1024;
            *(u32x2*)(pw + ((hh2 * 8 + r2) * 2 + q2) * 8) = (u32x2){pack2(e0 * inv, e1 * inv), pack2(e2 * inv, e3 * inv)};
            if (q2 == 0) *(u32x2*)(pw + 512 + (r2 * 4 + hh2) * 8) = (u32x2){__float_as_uint(f0 * inv), __float_as_uint(f1 * inv)};
            __builtin_amdgcn_fence(__ATOMIC_RELEASE, "wavefront");
            __builtin_amdgcn_wave_barrier();
            __builtin_amdgcn_fence(__ATOMIC_ACQUIRE, "wavefront");
            __builtin_amdgcn_sched_barrier(0);
            f32x4 dd[8];
#pragma unroll
            for (int hh = 0; hh < 4; ++hh) {
                const u32x4 u = __builtin_bit_cast(u32x4, uf[hh]);
                u32x2 ulo = {u.x, u.y}, uhi = {u.z, u.w}, elo = {qx[hh].x, qx[hh].y}, ehi = {qx[hh].z, qx[hh].w};
                mfma16_v0(dd[2 * hh], __builtin_bit_cast(bf16x4, ulo), __builtin_bit_cast(bf16x4, elo));
                mfma16_v0(dd[2 * hh + 1], __builtin_bit_cast(bf16x4, uhi), __builtin_bit_cast(bf16x4, ehi));
            }
            u32x2 pa[4];
#pragma unroll
            for (int hh = 0; hh < 4; ++hh) pa[hh] = *(const u32x2*)(pw + ((hh * 8 + (i & 7)) * 2 + (g & 1)) * 8);
            f32x4 fa = *(const f32x4*)(pw + 512 + (i & 7) * 32), fb = *(const f32x4*)(pw + 512 + (i & 7) * 32 + 16);
            if (g >= 2) {
#pragma unroll
                for (int hh = 0; hh < 4; ++hh) pa[hh] = (u32x2){0u, 0u};
            }
            mfma_fence4(dd[0], dd[1], dd[2], dd[3]);
            mfma_fence4(dd[4], dd[5], dd[6], dd[7]);
            f32x4 acc = dd[0] * fa[0] + dd[1] * fa[1] + dd[2] * fa[2] + dd[3] * fa[3] + dd[4] * fb[0] + dd[5] * fb[1] + dd[6] * fb[2] + dd[7] * fb[3];
            u32x4 p01 = {pa[0].x, pa[0].y, pa[1].x, pa[1].y}, p23 = {pa[2].x, pa[2].y, pa[3].x, pa[3].y};
            mfma_v(acc, yp0, __builtin_bit_cast(bf16x8, p01));
            mfma_v(acc, yp1, __builtin_bit_cast(bf16x8, p23));
            mfma_fence1(acc);
            *(f32x4*)(lds + CH_LDS_RB + wave * 1024 + lane * 16) = acc;
        }
        STAMP(6);
        __syncthreads();
        if (hasB && wave == 0) {
            f32x4 v0 = *(const f32x4*)(lds + CH_LDS_RB + lane * 16);
            f32x4 v1 = *(const f32x4*)(lds + CH_LDS_RB + 1024 + lane * 16);
            f32x4 v2 = *(const f32x4*)(lds + CH_LDS_RB + 2048 + lane * 16);
            f32x4 v3 = *(const f32x4*)(lds + CH_LDS_RB + 3072 + lane * 16);
            f32x4 v = (v0 + v1) + (v2 + v3);
            if (i < 8) {
                u32x2 pk = {pack2(v[0], v[1]), pack2(v[2], v[3])};
                st2c(rs, OFF_MEMF + (size_t)((s + 1) * 7168 + tB * 256 + i * 32 + g * 8), pk);
            }
        }
    };
    step(0, BoolC<true>{});
    for (int s = 1; s < NSEG - 1; ++s) step(s, BoolC<false>{});
    if (c == 31) {
        unsigned spins = 0;
        const size_t o0 = OFF_MEMF + (size_t)((NSEG - 1) * 7168 + tid * 16), o1 = o0 + (tid < 192 ? 4096 : 0);
        for (;;) {
            asm volatile("" ::: "memory");
            u32x4 x0 = ld4c(rs, o0), x1 = ld4c(rs, o1);
            if (__all(max(umax4(x0), umax4(x1)) != SENT)) break;
            if (spin_fail(spins, tmo, 0x1FFu)) break;
        }
        __syncthreads();
        if (tid == 255) __hip_atomic_store((unsigned*)(ws + OFF_CNT + (size_t)CNT_PROG * 128), (unsigned)(NSEG - 1), __ATOMIC_RELAXED, __HIP_MEMORY_SCOPE_AGENT);
    }
}

template <int TYPE>
DI void w1_phase(char* __restrict__ ws, const rsrc_t rs, int tile0, int wave, int lane) {
    bf16x8 w[4][KK];
    {
        const char* wp = ws + OFF_WF + (size_t)tile0 * KK * KB + lane * 16;
#pragma unroll
        for (int t = 0; t < 4; ++t)
#pragma unroll
            for (int kk = 0; kk < KK; ++kk) w[t][kk] = ldfrag(wp + (size_t)(t * KK + kk) * KB);
    }
    const int h = (tile0 - 448 * TYPE) / 28, t0 = (tile0 - 448 * TYPE) % 28;
    bf16x8 xa[KK], xb[KK];
    {
        const char* xp = ws + OFF_XNF + (size_t)(2 * wave) * KK * KB + lane * 16;
#pragma unroll
        for (int kk = 0; kk < KK; ++kk) xa[kk] = ldfrag(xp + (size_t)kk * KB);
    }
    for (int s = wave; s < NSEG; s += 4) {
        f32x4 acc[2][4];
        {
            const char* xp = ws + OFF_XNF + (size_t)(2 * s + 1) * KK * KB + lane * 16;
#pragma unroll
            for (int kk = 0; kk < KK; ++kk) xb[kk] = ldfrag(xp + (size_t)kk * KB);
        }
        __builtin_amdgcn_sched_barrier(0);
#pragma unroll
        for (int t = 0; t < 4; ++t) { if (TYPE == 0) mfma_Aa0(acc[0][t], w[t][0], xa[0]); else mfma_Ba0(acc[0][t], xa[0], w[t][0]); }
#pragma unroll
        for (int kk = 1; kk < KK; ++kk)
#pragma unroll
            for (int t = 0; t < 4; ++t) { if (TYPE == 0) mfma_Aa(acc[0][t], w[t][kk], xa[kk]); else mfma_Ba(acc[0][t], xa[kk], w[t][kk]); }
        __builtin_amdgcn_sched_barrier(0);
        {
            const int sn = (s + 4 < NSEG) ? s + 4 : s;
            const char* xp = ws + OFF_XNF + (size_t)(2 * sn) * KK * KB + lane * 16;
#pragma unroll
            for (int kk = 0; kk < KK; ++kk) xa[kk] = ldfrag(xp + (size_t)kk * KB);
        }
        __builtin_amdgcn_sched_barrier(0);
#pragma unroll
        for (int t = 0; t < 4; ++t) { if (TYPE == 0) mfma_Aa0(acc[1][t], w[t][0], xb[0]); else mfma_Ba0(acc[1][t], xb[0], w[t][0]); }
#pragma unroll
        for (int kk = 1; kk < KK; ++kk)
#pragma unroll
            for (int t = 0; t < 4; ++t) { if (TYPE == 0) mfma_Aa(acc[1][t], w[t][kk], xb[kk]); else mfma_Ba(acc[1][t], xb[kk], w[t][kk]); }
        mfma_fence4(acc[0][0], acc[0][1], acc[0][2], acc[0][3]);
        mfma_fence4(acc[1][0], acc[1][1], acc[1][2], acc[1][3]);
        if (TYPE == 0) {
#pragma unroll
            for (int q = 0; q < 2; ++q)
#pragma unroll
                for (int p = 0; p < 2; ++p)
                    st8c(rs, OFF_GTF + (((size_t)h * NRT + 2 * s + q) * KK + (t0 >> 1) + p) * KB + lane * 16, pack8(acc[q][2 * p], acc[q][2 * p + 1]));
        } else {
#pragma unroll
            for (int t = 0; t < 4; ++t)
                st8c(rs, OFF_UF + (((size_t)s * NT16 + t0 + t) * NH + h) * KB + lane * 16, pack8(acc[0][t], acc[1][t]));
        }
        signal_cnt(ws, CNT_G + s, lane);
    }
}

DI void ffn_task(char* __restrict__ ws, char* lds, const rsrc_t rs, const unsigned short* __restrict__ x, const unsigned short* __restrict__ b1,
                 const unsigned short* __restrict__ b2, int s, int wave, int lane) {
    const int i = lane & 15, g = lane >> 4;
    {
        bf16x8 xf[2][KK];
        const char* xp = ws + OFF_XNF + (size_t)(2 * s) * KK * KB + lane * 16;
#pragma unroll
        for (int q = 0; q < 2; ++q)
#pragma unroll
            for (int kk = 0; kk < KK; ++kk) xf[q][kk] = ldfrag(xp + (size_t)(q * KK + kk) * KB);
        const char* wp = ws + OFF_WF + (size_t)(896 + 28 * wave) * KK * KB + lane * 16;
        for (int pr = 0; pr < 14; ++pr) {
            bf16x8 wf[2][KK];
#pragma unroll
            for (int t = 0; t < 2; ++t)
#pragma unroll
                for (int kk = 0; kk < KK; ++kk) wf[t][kk] = ldfrag(wp + (size_t)((2 * pr + t) * KK + kk) * KB);
            f32x4 acc[2][2];
#pragma unroll
            for (int q = 0; q < 2; ++q)
#pragma unroll
                for (int t = 0; t < 2; ++t) acc[q][t] = (f32x4){0.f, 0.f, 0.f, 0.f};
#pragma unroll
            for (int kk = 0; kk < KK; ++kk)
#pragma unroll
                for (int q = 0; q < 2; ++q)
#pragma unroll
                    for (int t = 0; t < 2; ++t) acc[q][t] = mfma32(wf[t][kk], xf[q][kk], acc[q][t]);
#pragma unroll
            for (int t = 0; t < 2; ++t) {
                u32x2 bb = *(const u32x2*)(b1 + 16 * (28 * wave + 2 * pr + t) + 4 * g);
                float bv[4] = {bf_lo(bb.x), bf_hi(bb.x), bf_lo(bb.y), bf_hi(bb.y)};
#pragma unroll
                for (int q = 0; q < 2; ++q)
#pragma unroll
                    for (int r = 0; r < 4; ++r) acc[q][t][r] = gelu_tanh(acc[q][t][r] + bv[r]);
            }
#pragma unroll
            for (int q = 0; q < 2; ++q)
                *(bf16x8*)(lds + (size_t)(q * KF + 14 * wave + pr) * KB + lane * 16) = pack8(acc[q][0], acc[q][1]);
        }
    }
    __syncthreads();
    {
        f32x4 acc[2][7];
#pragma unroll
        for (int q = 0; q < 2; ++q)
#pragma unroll
            for (int t = 0; t < 7; ++t) acc[q][t] = (f32x4){0.f, 0.f, 0.f, 0.f};
        const char* wp = ws + OFF_W2F + (size_t)(7 * wave) * KF * KB + lane * 16;
#pragma unroll 4
        for (int kf = 0; kf < KF; ++kf) {
            bf16x8 h0 = *(const bf16x8*)(lds + (size_t)kf * KB + lane * 16), h1 = *(const bf16x8*)(lds + (size_t)(KF + kf) * KB + lane * 16);
#pragma unroll
            for (int t = 0; t < 7; ++t) {
                bf16x8 wf = ldfrag(wp + (size_t)(t * KF + kf) * KB);
                acc[0][t] = mfma32(wf, h0, acc[0][t]);
                acc[1][t] = mfma32(wf, h1, acc[1][t]);
            }
        }
#pragma unroll
        for (int t = 0; t < 7; ++t) {
            const int tc = 7 * wave + t;
            u32x2 bb = *(const u32x2*)(b2 + 16 * tc + 4 * g);
#pragma unroll
            for (int q = 0; q < 2; ++q) {
                const size_t o = (size_t)(32 * s + 16 * q + i) * DM + 16 * tc + 4 * g;
                u32x2 xv = *(const u32x2*)(x + o);
                f32x4 r = {acc[q][t][0] + bf_lo(bb.x) + bf_lo(xv.x), acc[q][t][1] + bf_hi(bb.x) + bf_hi(xv.x), acc[q][t][2] + bf_lo(bb.y) + bf_lo(xv.y), acc[q][t][3] + bf_hi(bb.y) + bf_hi(xv.y)};
                st4c(rs, OFF_FO + o * 4, __builtin_bit_cast(u32x4, r));
            }
        }
        signal_cnt(ws, CNT_F + s, lane);
    }
    __syncthreads();
}

DI void w3_loop(char* __restrict__ ws, const rsrc_t rs, int tile0, int lane) {
    const int i = lane & 15, g = lane >> 4;
    bf16x8 w[4][KK];
    {
        const char* wp = ws + OFF_WF + (size_t)tile0 * KK * KB + lane * 16;
#pragma unroll
        for (int t = 0; t < 4; ++t)
#pragma unroll
            for (int kk = 0; kk < KK; ++kk) w[t][kk] = ldfrag(wp + (size_t)(t * KK + kk) * KB);
    }
    const bool isA = tile0 < 448;
    const int h = (isA ? tile0 : tile0 - 448) / 28, t0 = (isA ? tile0 : tile0 - 448) % 28;
    for (int sp = 0; sp < NSEG / 2; ++sp) {
        bf16x8 xf[KK];
        wait_cnt_slow(ws, CNT_PROG, (unsigned)(sp == NSEG / 2 - 1 ? 2 * sp : 2 * sp + 1));
        {
            const size_t mp = OFF_MEMF + (size_t)(2 * sp + (i >> 3)) * 7168 + (i & 7) * 32 + g * 8;
            unsigned spins = 0;
            for (;;) {
                asm volatile("" ::: "memory");
                unsigned mx = 0;
#pragma unroll
                for (int kk = 0; kk < KK; ++kk) {
                    u32x2 lo_ = ld2c(rs, mp + kk * 512), hi_ = ld2c(rs, mp + kk * 512 + 256);
                    u32x4 xx_ = {lo_.x, lo_.y, hi_.x, hi_.y};
                    mx = max(mx, umax4(xx_));
                    xf[kk] = __builtin_bit_cast(bf16x8, xx_);
                }
                if (__all(mx != SENT)) break;
                __builtin_amdgcn_s_sleep(16);
                if (spin_fail(spins, (unsigned*)(ws + OFF_TMO), 0x500u + sp)) break;
            }
        }
        f32x4 acc[4];
        if (isA) {
#pragma unroll
            for (int t = 0; t < 4; ++t) mfma_Aa0(acc[t], w[t][0], xf[0]);
#pragma unroll
            for (int kk = 1; kk < KK; ++kk)
#pragma unroll
                for (int t = 0; t < 4; ++t) mfma_Aa(acc[t], w[t][kk], xf[kk]);
            mfma_fence4(acc[0], acc[1], acc[2], acc[3]);
#pragma unroll
            for (int p = 0; p < 2; ++p)
                st8c(rs, OFF_T2F + ((size_t)h * NSEG + 2 * sp + (i >> 3)) * 7168 + ((t0 >> 1) + p) * 512 + g * 128 + (i & 7) * 16, pack8(acc[2 * p], acc[2 * p + 1]));
        } else {
#pragma unroll
            for (int t = 0; t < 4; ++t) mfma_Ba0(acc[t], xf[0], w[t][0]);
#pragma unroll
            for (int kk = 1; kk < KK; ++kk)
#pragma unroll
                for (int t = 0; t < 4; ++t) mfma_Ba(acc[t], xf[kk], w[t][kk]);
            mfma_fence4(acc[0], acc[1], acc[2], acc[3]);
#pragma unroll
            for (int t = 0; t < 4; ++t)
                st2c(rs, OFF_YF + (((size_t)(2 * sp + (g >> 1)) * NT16 + t0 + t) * 8 + (h >> 1)) * 512 + (g & 1) * 256 + i * 16 + (h & 1) * 8, __builtin_bit_cast(u32x2, pack4(acc[t])));
        }
        signal_cnt(ws, isA ? CNT_T2 + sp * 16 + h : CNT_Y + sp * 7 + (t0 >> 2), lane);
    }
}

DI void f1_task(char* __restrict__ ws, const rsrc_t rs, int s, int h, int lane) {
    const int i = lane & 15, g = lane >> 4;
    f32x4 sc[2][3];
#pragma unroll
    for (int q = 0; q < 2; ++q)
#pragma unroll
        for (int k = 0; k < 3; ++k) sc[q][k] = (f32x4){0.f, 0.f, 0.f, 0.f};
    const char* xp = ws + OFF_XNF + (size_t)(2 * s) * KK * KB + lane * 16;
    const size_t gp = OFF_GTF + ((size_t)h * NRT + 2 * s) * KK * KB + lane * 16;
    const size_t tp = OFF_T2F + ((size_t)h * NSEG + s) * 7168 + g * 128 + (i & 7) * 16;
    bf16x8 xf[2][KK];
    {
        wait_cnt(ws, CNT_G + s, 224);
        bf16x8 g0[KK], g1[KK];
#pragma unroll
        for (int kk = 0; kk < KK; ++kk) { g0[kk] = ld8c(rs, gp + (size_t)kk * KB); g1[kk] = ld8c(rs, gp + (size_t)(KK + kk) * KB); }
#pragma unroll
        for (int q = 0; q < 2; ++q)
#pragma unroll
            for (int kk = 0; kk < KK; ++kk) xf[q][kk] = ldfrag(xp + (size_t)(q * KK + kk) * KB);
#pragma unroll
        for (int kk = 0; kk < KK; ++kk)
#pragma unroll
            for (int q = 0; q < 2; ++q) {
                sc[q][0] = mfma32(g0[kk], xf[q][kk], sc[q][0]);
                sc[q][1] = mfma32(g1[kk], xf[q][kk], sc[q][1]);
            }
    }
    wait_cnt_slow(ws, CNT_PROG, (unsigned)(s | 1));
    wait_cnt(ws, CNT_T2 + (s >> 1) * 16 + h, 7);
    {
        bf16x8 t2[KK];
#pragma unroll
        for (int kk = 0; kk < KK; ++kk) t2[kk] = ld8c(rs, tp + kk * 512);
#pragma unroll
        for (int kk = 0; kk < KK; ++kk)
#pragma unroll
            for (int q = 0; q < 2; ++q) sc[q][2] = mfma32(t2[kk], xf[q][kk], sc[q][2]);
    }
    const float cs = 0.0625f * 1.4426950408889634f;
    const float NEG = -3.0e38f;
#pragma unroll
    for (int q = 0; q < 2; ++q) {
        const int qi = 16 * q + i;
        float xs[8], ms[4];
#pragma unroll
        for (int j = 0; j < 8; ++j) {
            const int key = 16 * (j >> 2) + 4 * g + (j & 3);
            xs[j] = (key <= qi) ? sc[q][j >> 2][j & 3] : NEG;
        }
#pragma unroll
        for (int j = 0; j < 4; ++j) ms[j] = (g < 2) ? sc[q][2][j] : NEG;
        float m = fmaxf(fmaxf(fmaxf(xs[0], xs[1]), fmaxf(xs[2], xs[3])), fmaxf(fmaxf(xs[4], xs[5]), fmaxf(xs[6], xs[7])));
        m = fmaxf(m, fmaxf(fmaxf(ms[0], ms[1]), fmaxf(ms[2], ms[3])));
        m = xmax16(m); m = xmax32(m);
        const float mc = m * cs;
        float sum = 0.f;
#pragma unroll
        for (int j = 0; j < 8; ++j) { const int key = 16 * (j >> 2) + 4 * g + (j & 3); xs[j] = (key <= qi) ? fexp2(xs[j] * cs - mc) : 0.f; sum += xs[j]; }
#pragma unroll
        for (int j = 0; j < 4; ++j) { ms[j] = (g < 2) ? fexp2(ms[j] * cs - mc) : 0.f; sum += ms[j]; }
        sum = xadd16(sum); sum = xadd32(sum);
        const float inv = 1.f / sum;
        u32x4 px = {pack2(xs[0] * inv, xs[1] * inv), pack2(xs[2] * inv, xs[3] * inv), pack2(xs[4] * inv, xs[5] * inv), pack2(xs[6] * inv, xs[7] * inv)};
        st4c(rs, OFF_PXF + (((size_t)s * 2 + q) * NH + h) * KB + lane * 16, px);
        u32x2 pmv = {pack2(ms[0] * inv, ms[1] * inv), pack2(ms[2] * inv, ms[3] * inv)};
        st2c(rs, OFF_PMF + (((size_t)s * 2 + q) * 8 + (h >> 1)) * KB + lane * 16 + (h & 1) * 8, pmv);
    }
    signal_cnt(ws, CNT_P + s, lane);
}

DI void f2_task(char* __restrict__ ws, const rsrc_t rs, unsigned short* __restrict__ out, int s, int tp2, int lane) {
    const int i = lane & 15, g = lane >> 4;
    wait_cnt(ws, CNT_G + s, 224);
    wait_cnt(ws, CNT_F + s, 4);
    f32x4 acc[2][2];
    bf16x8 ua[2][NH];
    {
        const size_t up = OFF_UF + ((size_t)s * NT16 + 2 * tp2) * NH * KB + lane * 16;
#pragma unroll
        for (int t = 0; t < 2; ++t)
#pragma unroll
            for (int h = 0; h < NH; ++h) ua[t][h] = ld8c(rs, up + (size_t)(t * NH + h) * KB);
    }
#pragma unroll
    for (int q = 0; q < 2; ++q)
#pragma unroll
        for (int t = 0; t < 2; ++t)
            acc[q][t] = __builtin_bit_cast(f32x4, ld4c(rs, OFF_FO + ((size_t)(32 * s + 16 * q + i) * DM + 16 * (2 * tp2 + t) + 4 * g) * 4));
    wait_cnt_slow(ws, CNT_PROG, (unsigned)(s | 1));
    wait_cnt(ws, CNT_Y + (s >> 1) * 7 + (tp2 >> 1), 16);
    wait_cnt(ws, CNT_P + s, 16);
    {
        const size_t pp = OFF_PXF + (size_t)s * 2 * NH * KB + lane * 16;
        bf16x8 p0[NH], p1[NH];
#pragma unroll
        for (int h = 0; h < NH; ++h) { p0[h] = ld8c(rs, pp + (size_t)h * KB); p1[h] = ld8c(rs, pp + (size_t)(NH + h) * KB); }
        const size_t yp = OFF_YF + ((size_t)s * NT16 + 2 * tp2) * 8 * 512 + (g & 1) * 256 + i * 16;
        const size_t pq = OFF_PMF + (size_t)s * 2 * 8 * KB + lane * 16;
        bf16x8 ya[2][8], m0[8], m1[8];
#pragma unroll
        for (int hp = 0; hp < 8; ++hp) {
            m0[hp] = ld8c(rs, pq + (size_t)hp * KB); m1[hp] = ld8c(rs, pq + (size_t)(8 + hp) * KB);
            ya[0][hp] = ld8c(rs, yp + (size_t)hp * 512); ya[1][hp] = ld8c(rs, yp + (size_t)(8 + hp) * 512);
        }
#pragma unroll
        for (int h = 0; h < NH; ++h)
#pragma unroll
            for (int t = 0; t < 2; ++t) {
                acc[0][t] = mfma32(ua[t][h], p0[h], acc[0][t]);
                acc[1][t] = mfma32(ua[t][h], p1[h], acc[1][t]);
            }
#pragma unroll
        for (int hp = 0; hp < 8; ++hp)
#pragma unroll
            for (int t = 0; t < 2; ++t) {
                acc[0][t] = mfma32(ya[t][hp], m0[hp], acc[0][t]);
                acc[1][t] = mfma32(ya[t][hp], m1[hp], acc[1][t]);
            }
    }
#pragma unroll
    for (int q = 0; q < 2; ++q)
#pragma unroll
        for (int t = 0; t < 2; ++t) {
            const size_t o = (size_t)(32 * s + 16 * q + i) * DM + 16 * (2 * tp2 + t) + 4 * g;
            u32x2 r = {pack2(acc[q][t][0], acc[q][t][1]), pack2(acc[q][t][2], acc[q][t][3])};
            *(u32x2*)(out + o) = r;
        }
}

__global__ __launch_bounds__(256, 1) void k_mega(const unsigned short* __restrict__ x, const unsigned short* __restrict__ b1, const unsigned short* __restrict__ b2,
                                                 char* __restrict__ ws, unsigned short* __restrict__ out) {
    extern __shared__ __attribute__((aligned(16))) char lds[];
    const rsrc_t rs = __builtin_amdgcn_make_buffer_rsrc(ws, 0, (int)WS_END, 0x00020000);
#define WSTAMP(k)
    WSTAMP(0);
    if ((blockIdx.x & 7) == 0) { chain_role(ws, lds, rs); WSTAMP(3); return; }
    const int wid = (blockIdx.x >> 3) * 7 + (blockIdx.x & 7) - 1;
    const int wave = __builtin_amdgcn_readfirstlane(threadIdx.x >> 6), lane = threadIdx.x & 63;
    const int tile0 = wid * 4;
    if (tile0 < 448) w1_phase<0>(ws, rs, tile0, wave, lane);
    else w1_phase<1>(ws, rs, tile0, wave, lane);
    WSTAMP(1);
    if (wid >= 160) ffn_task(ws, lds, rs, x, b1, b2, wid - 160, wave, lane);
    WSTAMP(2);
    if (wave == 0) { w3_loop(ws, rs, tile0, lane); WSTAMP(3); return; }
    const int fw = wid * 3 + wave - 1;
    for (int b = 0; b < 32; ++b) {
        {
            const int k = (fw + 672 * 2 - b * 32) % 672;
            if (k < 32) f1_task(ws, rs, 2 * b + (k >> 4), k & 15, lane);
        }
        {
            const int k = (fw + 672 * 2 - 336 - b * 28) % 672;
            if (k < 28) f2_task(ws, rs, out, 2 * b + (k / 14), k % 14, lane);
        }
    }
    WSTAMP(3);
}

extern "C" void kernel_launch(void* const* d_in, const int* in_sizes, int n_in,
                              void* d_out, int out_size, void* d_ws, size_t ws_size,
                              hipStream_t stream) {
    const unsigned short* x = (const unsigned short*)d_in[0];
    const unsigned short* mem0 = (const unsigned short*)d_in[1];
    const unsigned short* Wq = (const unsigned short*)d_in[2];
    const unsigned short* Wk = (const unsigned short*)d_in[3];
    const unsigned short* Wv = (const unsigned short*)d_in[4];
    const unsigned short* Wo = (const unsigned short*)d_in[5];
    const unsigned short* w1 = (const unsigned short*)d_in[8];
    const unsigned short* b1 = (const unsigned short*)d_in[9];
    const unsigned short* w2 = (const unsigned short*)d_in[10];
    const unsigned short* b2 = (const unsigned short*)d_in[11];
    char* ws = (char*)d_ws;
    unsigned short* out = (unsigned short*)d_out;
    (void)hipFuncSetAttribute((const void*)k_mega, hipFuncAttributeMaxDynamicSharedMemorySize, CH_LDS_SIZE);
    k_prep<<<PREP_GRID, PREP_NT, 0, stream>>>(x, mem0, Wq, Wk, Wv, Wo, w1, w2, ws);
    k_mega<<<256, 256, CH_LDS_SIZE, stream>>>(x, b1, b2, ws, out);
}
```

```cpp
#include <hip/hip_runtime.h>
#include <hip/hip_bf16.h>

typedef __attribute__((ext_vector_type(8))) short bf16x8;
typedef __attribute__((ext_vector_type(4))) short bf16x4;
typedef __attribute__((ext_vector_type(4))) float f32x4;
typedef __attribute__((ext_vector_type(4))) unsigned u32x4;
typedef __attribute__((ext_vector_type(2))) unsigned u32x2;
typedef __attribute__((ext_vector_type(2))) __bf16 bf2_t;
#define DI __device__ __forceinline__

constexpr int DM = 448, KK = 14, NT16 = 28, NH = 16, NSEG = 64, NRT = 128, FFD = 1792, KF = 56;
constexpr size_t KB = 1024;
constexpr size_t OFF_XNF = 0;
constexpr size_t OFF_WF = OFF_XNF + (size_t)NRT * KK * KB;
constexpr size_t OFF_ANF = OFF_WF + (size_t)1008 * KK * KB;
constexpr size_t OFF_W2F = OFF_ANF + (size_t)448 * KK * KB;
constexpr size_t OFF_GTF = OFF_W2F + (size_t)28 * KF * KB;
constexpr size_t OFF_UF = OFF_GTF + (size_t)NH * NRT * KK * KB;
constexpr size_t OFF_FO = OFF_UF + (size_t)NSEG * NT16 * NH * KB;
constexpr size_t OFF_MEMF = OFF_FO + (size_t)2048 * DM * 4;
constexpr size_t OFF_SMX = OFF_MEMF + (size_t)NSEG * 7168;
constexpr int E2B = 640, E2S = 32 * E2B;
constexpr size_t OFF_T2F = OFF_SMX + (size_t)NSEG * E2S;
constexpr size_t OFF_YF = OFF_T2F + (size_t)NH * NSEG * 7168;
constexpr size_t OFF_PXF = OFF_YF + (size_t)NSEG * NT16 * 8 * 512;
constexpr size_t OFF_PMF = OFF_PXF + (size_t)NSEG * 2 * NH * KB;
constexpr size_t OFF_CNT = OFF_PMF + (size_t)NSEG * 2 * 8 * KB;
constexpr int CNT_G = 0, CNT_P = 64, CNT_F = 128, CNT_TMO = 192, CNT_PROG = 193, CNT_T2 = 200, CNT_Y = CNT_T2 + 32 * 16, CNT_AN = CNT_Y + 32 * 7, CNT_BW = CNT_AN + 16, CNT_N = CNT_BW + 8;
constexpr size_t OFF_TMO = OFF_CNT + (size_t)CNT_TMO * 128;
constexpr size_t WS_END = OFF_CNT + (size_t)CNT_N * 128;
constexpr size_t OFF_DBG = WS_END;
#define STAMP(k)

constexpr unsigned SENT = 0xFFFFFFFFu;
constexpr unsigned kSpinCap = 1u << 16;

DI unsigned pack2(float a, float b) { bf2_t v = {(__bf16)a, (__bf16)b}; return __builtin_bit_cast(unsigned, v); }
DI float bf_lo(unsigned u) { return __uint_as_float(u << 16); }
DI float bf_hi(unsigned u) { return __uint_as_float(u & 0xFFFF0000u); }
DI f32x4 mfma32(bf16x8 a, bf16x8 b, f32x4 c) { return __builtin_amdgcn_mfma_f32_16x16x32_bf16(a, b, c, 0, 0, 0); }
DI bf16x8 ldfrag(const char* p) { return *(const bf16x8*)p; }
DI bf16x8 pack8(f32x4 a, f32x4 b) {
    u32x4 r = {pack2(a[0], a[1]), pack2(a[2], a[3]), pack2(b[0], b[1]), pack2(b[2], b[3])};
    return __builtin_bit_cast(bf16x8, r);
}
DI bf16x4 pack4(f32x4 a) { u32x2 r = {pack2(a[0], a[1]), pack2(a[2], a[3])}; return __builtin_bit_cast(bf16x4, r); }
DI bool spin_fail(unsigned& spins, unsigned* tmo, unsigned code = 1u) {
    if (++spins > kSpinCap) { unsigned exp_ = 0u; __hip_atomic_compare_exchange_strong(tmo, &exp_, code, __ATOMIC_RELAXED, __ATOMIC_RELAXED, __HIP_MEMORY_SCOPE_AGENT); return true; }
    if ((spins & 63u) == 0u) return __builtin_amdgcn_readfirstlane(__hip_atomic_load(tmo, __ATOMIC_RELAXED, __HIP_MEMORY_SCOPE_AGENT)) != 0u;
    return false;
}
typedef __amdgpu_buffer_rsrc_t rsrc_t;
DI bf16x8 ld8c(rsrc_t rs, size_t off) { return __builtin_bit_cast(bf16x8, __builtin_amdgcn_raw_buffer_load_b128(rs, (unsigned)off, 0, 16)); }
DI u32x4 ld4c(rsrc_t rs, size_t off) { return __builtin_amdgcn_raw_buffer_load_b128(rs, (unsigned)off, 0, 16); }
DI u32x2 ld2c(rsrc_t rs, size_t off) { return __builtin_amdgcn_raw_buffer_load_b64(rs, (unsigned)off, 0, 16); }
DI unsigned ld1c(rsrc_t rs, size_t off) { return __builtin_amdgcn_raw_buffer_load_b32(rs, (unsigned)off, 0, 16); }
DI void st8c(rsrc_t rs, size_t off, bf16x8 v) { __builtin_amdgcn_raw_buffer_store_b128(__builtin_bit_cast(u32x4, v), rs, (unsigned)off, 0, 16); }
DI void st4c(rsrc_t rs, size_t off, u32x4 v) { __builtin_amdgcn_raw_buffer_store_b128(v, rs, (unsigned)off, 0, 16); }
DI void st2c(rsrc_t rs, size_t off, u32x2 v) { __builtin_amdgcn_raw_buffer_store_b64(v, rs, (unsigned)off, 0, 16); }
DI void signal_cnt(char* ws, int idx, int lane) {
    asm volatile("s_waitcnt vmcnt(0)" ::: "memory");
    if (lane == 0) __hip_atomic_fetch_add((unsigned*)(ws + OFF_CNT + (size_t)idx * 128), 1u, __ATOMIC_RELAXED, __HIP_MEMORY_SCOPE_AGENT);
}
DI void wait_cnt(char* ws, int idx, unsigned target) {
    unsigned spins = 0;
    unsigned* p = (unsigned*)(ws + OFF_CNT + (size_t)idx * 128);
    for (;;) {
        unsigned v = __builtin_amdgcn_readfirstlane(__hip_atomic_load(p, __ATOMIC_RELAXED, __HIP_MEMORY_SCOPE_AGENT));
        if (v >= target) break;
        __builtin_amdgcn_s_sleep(8);
        if (spin_fail(spins, (unsigned*)(ws + OFF_TMO), 0x300u + idx)) break;
    }
    asm volatile("" ::: "memory");
}

DI void wait_cnt_slow(char* ws, int idx, unsigned target) {
    unsigned spins = 0;
    unsigned* p = (unsigned*)(ws + OFF_CNT + (size_t)idx * 128);
    for (;;) {
        unsigned v = __builtin_amdgcn_readfirstlane(__hip_atomic_load(p, __ATOMIC_RELAXED, __HIP_MEMORY_SCOPE_AGENT));
        if (v >= target) break;
        if (v + 1 < target) __builtin_amdgcn_s_sleep(100); else __builtin_amdgcn_s_sleep(12);
        if (spin_fail(spins, (unsigned*)(ws + OFF_TMO), 0x400u + idx)) break;
    }
    asm volatile("" ::: "memory");
}

DI float gelu_tanh(float z) {
    float u = 0.7978845608028654f * (z + 0.044715f * z * z * z);
    float e = __expf(2.f * u);
    float t = 1.f - 2.f / (1.f + e);
    return 0.5f * z * (1.f + t);
}

DI float xmax16(float v) { auto r = __builtin_amdgcn_permlane16_swap(__float_as_uint(v), __float_as_uint(v), false, false); return fmaxf(__uint_as_float(r[0]), __uint_as_float(r[1])); }
DI float xmax32(float v) { auto r = __builtin_amdgcn_permlane32_swap(__float_as_uint(v), __float_as_uint(v), false, false); return fmaxf(__uint_as_float(r[0]), __uint_as_float(r[1])); }
DI float xadd16(float v) { auto r = __builtin_amdgcn_permlane16_swap(__float_as_uint(v), __float_as_uint(v), false, false); return __uint_as_float(r[0]) + __uint_as_float(r[1]); }
DI float xadd32(float v) { auto r = __builtin_amdgcn_permlane32_swap(__float_as_uint(v), __float_as_uint(v), false, false); return __uint_as_float(r[0]) + __uint_as_float(r[1]); }
DI float qswap(float v) { return __uint_as_float(__builtin_amdgcn_update_dpp(0, __float_as_uint(v), 0xB1, 0xF, 0xF, true)); }
DI unsigned umax4(u32x4 v) { return max(max(v.x, v.y), max(v.z, v.w)); }
DI float fexp2(float x) { return __builtin_amdgcn_exp2f(x); }

DI void mfma_Aa0(f32x4& acc, const bf16x8& w, const bf16x8& m) { asm volatile("s_nop 1\n\tv_mfma_f32_16x16x32_bf16 %0, %1, %2, 0" : "=&v"(acc) : "a"(w), "v"(m)); }
DI void mfma_Aa(f32x4& acc, const bf16x8& w, const bf16x8& m) { asm volatile("s_nop 1\n\tv_mfma_f32_16x16x32_bf16 %0, %1, %2, %0" : "+v"(acc) : "a"(w), "v"(m)); }
DI void mfma_Ba0(f32x4& acc, const bf16x8& m, const bf16x8& w) { asm volatile("s_nop 1\n\tv_mfma_f32_16x16x32_bf16 %0, %1, %2, 0" : "=&v"(acc) : "v"(m), "a"(w)); }
DI void mfma_Ba(f32x4& acc, const bf16x8& m, const bf16x8& w) { asm volatile("s_nop 1\n\tv_mfma_f32_16x16x32_bf16 %0, %1, %2, %0" : "+v"(acc) : "v"(m), "a"(w)); }
DI void mfma_v0(f32x4& acc, const bf16x8& a, const bf16x8& b) { asm volatile("s_nop 1\n\tv_mfma_f32_16x16x32_bf16 %0, %1, %2, 0" : "=&v"(acc) : "v"(a), "v"(b)); }
DI void mfma_v(f32x4& acc, const bf16x8& a, const bf16x8& b) { asm volatile("s_nop 1\n\tv_mfma_f32_16x16x32_bf16 %0, %1, %2, %0" : "+v"(acc) : "v"(a), "v"(b)); }
DI void mfma16_v0(f32x4& acc, const bf16x4& a, const bf16x4& b) { asm volatile("s_nop 1\n\tv_mfma_f32_16x16x16_bf16 %0, %1, %2, 0" : "=&v"(acc) : "v"(a), "v"(b)); }
DI void mfma16_v(f32x4& acc, const bf16x4& a, const bf16x4& b) { asm volatile("s_nop 1\n\tv_mfma_f32_16x16x16_bf16 %0, %1, %2, %0" : "+v"(acc) : "v"(a), "v"(b)); }
DI void mfma_fence4(f32x4& a, f32x4& b, f32x4& c, f32x4& d) { asm volatile("s_nop 7\n\ts_nop 1" : "+v"(a), "+v"(b), "+v"(c), "+v"(d)); }
DI void pin(f32x4& a) { asm volatile("" : "+v"(a)); }
DI void mfma_fence1(f32x4& a) { asm volatile("s_nop 7\n\ts_nop 1" : "+v"(a)); }

constexpr int PREP_GRID = 256, PREP_NT = 448;

DI void ln_row(const unsigned short* __restrict__ x, char* __restrict__ ws, int row, int lane, const u32x2 (&u)[2]) {
    float v[2][4];
#pragma unroll
    for (int it = 0; it < 2; ++it) { v[it][0] = bf_lo(u[it].x); v[it][1] = bf_hi(u[it].x); v[it][2] = bf_lo(u[it].y); v[it][3] = bf_hi(u[it].y); }
    float sum = 0.f;
#pragma unroll
    for (int it = 0; it < 2; ++it) sum += v[it][0] + v[it][1] + v[it][2] + v[it][3];
#pragma unroll
    for (int o = 32; o >= 1; o >>= 1) sum += __shfl_xor(sum, o);
    const float mean = sum * (1.f / DM);
    float sq = 0.f;
#pragma unroll
    for (int it = 0; it < 2; ++it) {
        if (lane + it * 64 < 112) {
#pragma unroll
            for (int j = 0; j < 4; ++j) { float d = v[it][j] - mean; sq += d * d; }
        }
    }
#pragma unroll
    for (int o = 32; o >= 1; o >>= 1) sq += __shfl_xor(sq, o);
    const float rstd = 1.f / sqrtf(sq * (1.f / DM) + 1e-5f);
    const int rt = row >> 4, ii = row & 15;
#pragma unroll
    for (int it = 0; it < 2; ++it) {
        int grp = lane + it * 64;
        if (grp < 112) {
            int d = grp * 4, kk = d >> 5, dd = d & 31, half = dd >> 4, gg = (dd & 15) >> 2;
            u32x2 o = {pack2((v[it][0] - mean) * rstd, (v[it][1] - mean) * rstd), pack2((v[it][2] - mean) * rstd, (v[it][3] - mean) * rstd)};
            *(u32x2*)(ws + OFF_XNF + ((size_t)(rt * KK + kk) * 64 + (ii + 16 * gg)) * 16 + half * 8) = o;
        }
    }
}

DI bf16x8 w12_load(const unsigned short* __restrict__ w1, const unsigned short* __restrict__ w2, int f, int i, int g) {
    bf16x8 v;
    if (f < 112 * KK) {
        int tf = f / KK, kk = f % KK;
#pragma unroll
        for (int j = 0; j < 8; ++j) v[j] = (short)w1[(size_t)(32 * kk + 16 * (j >> 2) + 4 * g + (j & 3)) * FFD + 16 * tf + i];
    } else {
        int f2 = f - 112 * KK;
        int t = f2 / KF, kf = f2 % KF;
#pragma unroll
        for (int j = 0; j < 8; ++j) v[j] = (short)w2[(size_t)(32 * kf + 16 * (j >> 2) + 4 * g + (j & 3)) * DM + 16 * t + i];
    }
    return v;
}
DI void w12_store(char* __restrict__ ws, int f, int lane, bf16x8 v) {
    if (f < 112 * KK) *(bf16x8*)(ws + OFF_WF + ((size_t)896 * KK + f) * KB + lane * 16) = v;
    else *(bf16x8*)(ws + OFF_W2F + (size_t)(f - 112 * KK) * KB + lane * 16) = v;
}

__global__ __launch_bounds__(PREP_NT) void k_prep(const unsigned short* __restrict__ x, const unsigned short* __restrict__ mem0,
                                              const unsigned short* __restrict__ w1, const unsigned short* __restrict__ w2, char* __restrict__ ws) {
    const int wave = __builtin_amdgcn_readfirstlane(threadIdx.x >> 6), lane = threadIdx.x & 63;
    const int i = lane & 15, g = lane >> 4;
    const int b = blockIdx.x;
    const int gw = b * 7 + wave;
    u32x2 xr[2][2];
#pragma unroll
    for (int n = 0; n < 2; ++n) {
        const int row = gw + n * 1792;
#pragma unroll
        for (int it = 0; it < 2; ++it) {
            const int grp = lane + it * 64;
            xr[n][it] = (u32x2){0u, 0u};
            if (row < 2048 && grp < 112) xr[n][it] = *(const u32x2*)(x + (size_t)row * DM + grp * 4);
        }
    }
    bf16x8 wv[2];
#pragma unroll
    for (int n = 0; n < 2; ++n) { const int f = gw + n * 1792; wv[n] = w12_load(w1, w2, f < 3136 ? f : 0, i, g); }
    {
        const size_t n16 = ((size_t)(NSEG - 1) * 7168 + (size_t)NSEG * E2S) / 16;
        u32x4 sv = {SENT, SENT, SENT, SENT};
        for (size_t idx = (size_t)b * PREP_NT + threadIdx.x; idx < n16; idx += (size_t)PREP_GRID * PREP_NT)
            *(u32x4*)(ws + OFF_MEMF + 7168 + idx * 16) = sv;
    }
    if (b == 255) {
        for (int e = threadIdx.x; e < 8 * 112; e += PREP_NT) {
            int r = e / 112, grp = e % 112;
            int d = grp * 4, t = d >> 4, gg = (d & 15) >> 2;
            u32x2 u = *(const u32x2*)(mem0 + r * DM + d);
            *(u32x2*)(ws + OFF_MEMF + t * 256 + r * 32 + gg * 8) = u;
        }
    }
    if (b == 254) {
        for (int e = threadIdx.x; e < CNT_N; e += PREP_NT) *(unsigned*)(ws + OFF_CNT + (size_t)e * 128) = 0u;
    }
    ln_row(x, ws, gw, lane, xr[0]);
    if (gw + 1792 < 2048) ln_row(x, ws, gw + 1792, lane, xr[1]);
    w12_store(ws, gw, lane, wv[0]);
    if (gw + 1792 < 3136) w12_store(ws, gw + 1792, lane, wv[1]);
}

constexpr int CH_LDS_B = 0;
constexpr int CH_LDS_MEM = 112 * 1024;
constexpr int CH_LDS_RA = CH_LDS_MEM + 7168;
constexpr int CH_LDS_RB = CH_LDS_RA + 8192;
constexpr int CH_LDS_PW = CH_LDS_RB + 4096;
constexpr int CH_LDS_SIZE = CH_LDS_PW + 4096;

#define E2POS 5
#define DNB 8
#define ADMAX 8
#define D1 8
#define D2 0

template <bool B> struct BoolC { static constexpr bool value = B; };
DI void chain_role(char* __restrict__ ws, char* lds, const rsrc_t rs) {
    const int c = blockIdx.x >> 3;
    const int tid = threadIdx.x, wave = __builtin_amdgcn_readfirstlane(tid >> 6), lane = tid & 63;
    const int i = lane & 15, g = lane >> 4;
    const int h = c >> 1, half = c & 1;
    const bool hasB = c < NT16;
    const int tB = hasB ? c : 0;
    unsigned* tmo = (unsigned*)(ws + OFF_TMO);
    const float cs = 0.0625f * 1.4426950408889634f;

    bf16x8 wa[3][KK], wsh[7], wb[2][KK];
    {
        wait_cnt(ws, CNT_AN + h, 28);
        const size_t ap = OFF_ANF + (size_t)(h * 28 + 14 * half) * KK * KB + lane * 16;
#pragma unroll
        for (int t = 0; t < 3; ++t)
#pragma unroll
            for (int kk = 0; kk < KK; ++kk) wa[t][kk] = ld8c(rs, ap + (size_t)((3 * wave + t) * KK + kk) * KB);
#pragma unroll
        for (int k = 0; k < 7; ++k) wsh[k] = ld8c(rs, ap + (size_t)((12 + (wave >> 1)) * KK + 7 * (wave & 1) + k) * KB);
    }
    const int gk0 = wave < 2 ? 4 * wave : 8 + 3 * (wave - 2);
    const int gkn = wave < 2 ? 4 : 3;
    bf16x8 gf[4], uf[4];
    wait_cnt(ws, CNT_G + 0, 224);
    __syncthreads();
    {
        const size_t gp = OFF_GTF + (((size_t)h * NRT + half) * KK + gk0) * KB + lane * 16;
#pragma unroll
        for (int k = 0; k < 4; ++k) gf[k] = ld8c(rs, gp + (size_t)(k < gkn ? k : 0) * KB);
    }
    const char* mp = lds + CH_LDS_MEM + g * 128 + (i & 7) * 16;
    const int hh2 = lane >> 4, r2 = (lane >> 1) & 7, q2 = lane & 1;

    int d1v = hasB ? D1 : DNB, d2v = D2;
    auto step = [&](const int s, auto FIRST) __attribute__((always_inline)) {
        STAMP(0);
        {
            u32x4 x0, x1;
            unsigned spins = 0;
            const size_t o0 = OFF_MEMF + (size_t)(s * 7168 + tid * 16), o1 = o0 + (tid < 192 ? 4096 : 0);
            const size_t oc = OFF_CNT + (size_t)(CNT_G + s + 1) * 128;
            if (s > 0) for (int d = 0; d < d1v; ++d) __builtin_amdgcn_s_sleep(1);
            for (;;) {
                asm volatile("" ::: "memory");
                x0 = ld4c(rs, o0);
                x1 = ld4c(rs, o1);
                unsigned cv = ld1c(rs, oc);
                if (__all((max(umax4(x0), umax4(x1)) != SENT) && (cv >= 224u))) break;
                if (spin_fail(spins, tmo, 0x100u + s)) break;
            }
            if (hasB) { if (spins != 0u && s > 0 && d1v < D1 + ADMAX) d1v += 2; }
            else { if (spins == 0u) { if (d1v > 8) d1v -= 1; } else if (spins > 1u) d1v += 4; }
            const int t0 = tid >> 4, rr = (tid >> 1) & 7, gp = tid & 1;
            char* d0 = lds + CH_LDS_MEM + (t0 >> 1) * 512 + gp * 256 + rr * 16 + (t0 & 1) * 8;
            *(u32x2*)(d0) = (u32x2){x0.x, x0.y};
            *(u32x2*)(d0 + 128) = (u32x2){x0.z, x0.w};
            if (tid < 192) {
                *(u32x2*)(d0 + 8 * 512) = (u32x2){x1.x, x1.y};
                *(u32x2*)(d0 + 8 * 512 + 128) = (u32x2){x1.z, x1.w};
            }
        }
        {
            const size_t up = OFF_UF + (((size_t)s * NT16 + tB) * NH + 4 * wave) * KB + lane * 16;
#pragma unroll
            for (int k = 0; k < 4; ++k) uf[k] = ld8c(rs, up + (size_t)k * KB);
        }
        STAMP(1);
        __syncthreads();
        STAMP(2);
        if (c == 31 && tid == 255) __hip_atomic_store((unsigned*)(ws + OFF_CNT + (size_t)CNT_PROG * 128), (unsigned)s, __ATOMIC_RELAXED, __HIP_MEMORY_SCOPE_AGENT);
        bf16x8 mf[KK];
#pragma unroll
        for (int kk = 0; kk < KK; ++kk) mf[kk] = *(const bf16x8*)(mp + kk * 512);
        {
            bf16x8 mg[4];
            bf16x4 mk[4];
#pragma unroll
            for (int k = 0; k < 4; ++k) mg[k] = *(const bf16x8*)(mp + (gk0 + (k < gkn ? k : 0)) * 512);
#pragma unroll
            for (int t = 0; t < 4; ++t) {
                const int tl = (t < 3) ? 3 * wave + t : 12 + (wave >> 1);
                const int tg = 14 * half + tl;
                mk[t] = *(const bf16x4*)(mp + (tg >> 1) * 512 + (tg & 1) * 8);
            }
            f32x4 ta[4];
            f32x4 sx, sm;
            if (wave & 1) {
                mfma_Aa0(ta[3], wsh[0], mf[7]);
#pragma unroll
                for (int k = 1; k < 7; ++k) mfma_Aa(ta[3], wsh[k], mf[7 + k]);
            } else {
                mfma_Aa0(ta[3], wsh[0], mf[0]);
#pragma unroll
                for (int k = 1; k < 7; ++k) mfma_Aa(ta[3], wsh[k], mf[k]);
            }
            mfma_Aa0(ta[0], wa[0][0], mf[0]);
#pragma unroll
            for (int kk = 1; kk < KK; ++kk) mfma_Aa(ta[0], wa[0][kk], mf[kk]);
            mfma_Aa0(ta[1], wa[1][0], mf[0]);
#pragma unroll
            for (int kk = 1; kk < 4; ++kk) mfma_Aa(ta[1], wa[1][kk], mf[kk]);
            pin(ta[3]); pin(ta[0]);
            mfma16_v0(sm, mk[3], pack4(ta[3]));
            mfma16_v(sm, mk[0], pack4(ta[0]));
#pragma unroll
            for (int kk = 4; kk < KK; ++kk) mfma_Aa(ta[1], wa[1][kk], mf[kk]);
            mfma_Aa0(ta[2], wa[2][0], mf[0]);
#pragma unroll
            for (int kk = 1; kk < 4; ++kk) mfma_Aa(ta[2], wa[2][kk], mf[kk]);
            pin(ta[1]);
            mfma16_v(sm, mk[1], pack4(ta[1]));
#pragma unroll
            for (int kk = 4; kk < KK; ++kk) mfma_Aa(ta[2], wa[2][kk], mf[kk]);
            mfma_v0(sx, gf[0], mg[0]);
            mfma_v(sx, gf[1], mg[1]);
            mfma_v(sx, gf[2], mg[2]);
            if (wave < 2) mfma_v(sx, gf[3], mg[3]);
            pin(ta[2]);
            mfma16_v(sm, mk[2], pack4(ta[2]));
            mfma_fence4(sm, sx, ta[0], ta[1]);
            *(f32x4*)(lds + CH_LDS_RA + (wave * 2 + 0) * 1024 + lane * 16) = sx;
            *(f32x4*)(lds + CH_LDS_RA + (wave * 2 + 1) * 1024 + lane * 16) = sm;
        }
        STAMP(3);
        __syncthreads();
        const size_t e2w = OFF_SMX + (size_t)s * E2S + (size_t)c * E2B;
        if (wave < 2) {
            f32x4 v0 = *(const f32x4*)(lds + CH_LDS_RA + (0 + wave) * 1024 + lane * 16);
            f32x4 v1 = *(const f32x4*)(lds + CH_LDS_RA + (2 + wave) * 1024 + lane * 16);
            f32x4 v2 = *(const f32x4*)(lds + CH_LDS_RA + (4 + wave) * 1024 + lane * 16);
            f32x4 v3 = *(const f32x4*)(lds + CH_LDS_RA + (6 + wave) * 1024 + lane * 16);
            f32x4 v = (v0 + v1) + (v2 + v3);
            if (wave == 0) {
                float m = fmaxf(fmaxf(v[0], v[1]), fmaxf(v[2], v[3]));
                m = xmax16(m); m = xmax32(m);
                const float mc = m * cs;
                float e0 = fexp2(v[0] * cs - mc), e1 = fexp2(v[1] * cs - mc), e2 = fexp2(v[2] * cs - mc), e3 = fexp2(v[3] * cs - mc);
                u32x2 pk = {pack2(e0, e1), pack2(e2, e3)};
                if (i < 8) st2c(rs, e2w + i * 32 + g * 8, pk);
                float sum = (bf_lo(pk.x) + bf_hi(pk.x)) + (bf_lo(pk.y) + bf_hi(pk.y));
                sum = xadd16(sum); sum = xadd32(sum);
                u32x2 st = {__float_as_uint(m), __float_as_uint(sum)};
                if (lane < 8) st2c(rs, e2w + 512 + i * 8, st);
            } else {
                if (i < 8 && g < 2) st4c(rs, e2w + 256 + i * 32 + g * 16, __builtin_bit_cast(u32x4, v));
            }
        }
        {
            const size_t gp = OFF_GTF + (((size_t)h * NRT + 2 * (s + 1) + half) * KK + gk0) * KB + lane * 16;
#pragma unroll
            for (int k = 0; k < 4; ++k) gf[k] = ld8c(rs, gp + (size_t)(k < gkn ? k : 0) * KB);
        }
        if constexpr (decltype(FIRST)::value) {
            wait_cnt(ws, CNT_BW + (tB >> 2), 64);
            const size_t bp = OFF_WF + (size_t)(448 + tB) * KK * KB + lane * 16;
#pragma unroll
            for (int hh = 0; hh < 2; ++hh)
#pragma unroll
                for (int kk = 0; kk < KK; ++kk)
                    *(bf16x8*)(lds + CH_LDS_B + (size_t)((wave * 2 + hh) * KK + kk) * KB + lane * 16) = ld8c(rs, bp + (size_t)((4 * wave + 2 + hh) * 28 * KK + kk) * KB);
#pragma unroll
            for (int hh = 0; hh < 2; ++hh)
#pragma unroll
                for (int kk = 0; kk < KK; ++kk) wb[hh][kk] = ld8c(rs, bp + (size_t)((4 * wave + hh) * 28 * KK + kk) * KB);
        }
        if (hasB) {
            f32x4 ya[4];
            const char* lb = lds + CH_LDS_B + (size_t)(wave * 2) * KK * KB + lane * 16;
            mfma_Ba0(ya[0], mf[0], wb[0][0]);
            mfma_v0(ya[1], mf[0], wb[1][0]);
#pragma unroll
            for (int k = 1; k < KK; ++k) {
                mfma_Ba(ya[0], mf[k], wb[0][k]);
                mfma_v(ya[1], mf[k], wb[1][k]);
            }
            STAMP(4);
            u32x4 qx[4], qm0, qm1, qs;
            const size_t e2r = OFF_SMX + (size_t)s * E2S;
            const size_t ox = e2r + (size_t)(8 * wave) * E2B + (i & 7) * 32 + g * 8;
            const size_t om = e2r + (size_t)(8 * wave + 2 * hh2) * E2B + 256 + r2 * 32 + q2 * 16;
            const size_t os = e2r + (size_t)(8 * wave + 2 * hh2) * E2B + 512 + r2 * 8;
#define E2_LOADS() do { _Pragma("unroll") for (int hh = 0; hh < 4; ++hh) { \
                        u32x2 lo_ = ld2c(rs, ox + (size_t)(2 * hh) * E2B), hi_ = ld2c(rs, ox + (size_t)(2 * hh + 1) * E2B); \
                        qx[hh] = (u32x4){lo_.x, lo_.y, hi_.x, hi_.y}; } \
                    qm0 = ld4c(rs, om); qm1 = ld4c(rs, om + E2B); \
                    { u32x2 lo_ = ld2c(rs, os), hi_ = ld2c(rs, os + E2B); qs = (u32x4){lo_.x, lo_.y, hi_.x, hi_.y}; } } while (0)
            {
                bf16x8 lq[2][4];
#pragma unroll
                for (int k = 0; k < 2; ++k) { lq[0][2 * k] = *(const bf16x8*)(lb + (size_t)k * KB); lq[0][2 * k + 1] = *(const bf16x8*)(lb + (size_t)(KK + k) * KB); }
#pragma unroll
                for (int gq = 0; gq < 7; ++gq) {
                    if (gq < 6) {
#pragma unroll
                        for (int k = 0; k < 2; ++k) {
                            lq[(gq + 1) & 1][2 * k] = *(const bf16x8*)(lb + (size_t)(2 * gq + 2 + k) * KB);
                            lq[(gq + 1) & 1][2 * k + 1] = *(const bf16x8*)(lb + (size_t)(KK + 2 * gq + 2 + k) * KB);
                        }
                    }
                    if (gq == E2POS) { for (int d = 0; d < d2v; ++d) __builtin_amdgcn_s_sleep(1); asm volatile("" ::: "memory"); E2_LOADS(); }
                    __builtin_amdgcn_sched_barrier(0);
                    if (gq == 0) { mfma_v0(ya[2], mf[0], lq[0][0]); mfma_v0(ya[3], mf[0], lq[0][1]); }
                    else { mfma_v(ya[2], mf[2 * gq], lq[gq & 1][0]); mfma_v(ya[3], mf[2 * gq], lq[gq & 1][1]); }
                    mfma_v(ya[2], mf[2 * gq + 1], lq[gq & 1][2]); mfma_v(ya[3], mf[2 * gq + 1], lq[gq & 1][3]);
                }
            }
            mfma_fence4(ya[0], ya[1], ya[2], ya[3]);
            bf16x8 yp0 = pack8(ya[0], ya[1]), yp1 = pack8(ya[2], ya[3]);
            __builtin_amdgcn_sched_barrier(0);
            {
                unsigned spins = 0;
                if (E2POS >= 7) { for (int d = 0; d < d2v; ++d) __builtin_amdgcn_s_sleep(1); asm volatile("" ::: "memory"); E2_LOADS(); }
                for (;;) {
                    unsigned mx = max(max(max(umax4(qx[0]), umax4(qx[1])), max(umax4(qx[2]), umax4(qx[3]))), max(max(umax4(qm0), umax4(qm1)), umax4(qs)));
                    if (__all(mx != SENT)) break;
                    if (spin_fail(spins, tmo, 0x200u + s)) break;
                    asm volatile("" ::: "memory");
                    E2_LOADS();
                }
                if (spins != 0u && s > 0 && d2v < D2 + ADMAX) d2v += 2;
            }
            STAMP(5);
            f32x4 ms = __builtin_bit_cast(f32x4, qm0) + __builtin_bit_cast(f32x4, qm1);
            const float m0 = __uint_as_float(qs.x), s0 = __uint_as_float(qs.y), m1 = __uint_as_float(qs.z), s1 = __uint_as_float(qs.w);
            float mx = fmaxf(fmaxf(ms[0], ms[1]), fmaxf(ms[2], ms[3]));
            mx = fmaxf(mx, qswap(mx));
            const float Mc = fmaxf(mx, fmaxf(m0, m1)) * cs;
            float e0 = fexp2(ms[0] * cs - Mc), e1 = fexp2(ms[1] * cs - Mc), e2 = fexp2(ms[2] * cs - Mc), e3 = fexp2(ms[3] * cs - Mc);
            float f0 = fexp2(m0 * cs - Mc), f1 = fexp2(m1 * cs - Mc);
            float sm = (e0 + e1) + (e2 + e3);
            sm += qswap(sm);
            const float inv = __builtin_amdgcn_rcpf(sm + f0 * s0 + f1 * s1);
            char* pw = lds + CH_LDS_PW + wave * 1024;
            *(u32x2*)(pw + ((hh2 * 8 + r2) * 2 + q2) * 8) = (u32x2){pack2(e0 * inv, e1 * inv), pack2(e2 * inv, e3 * inv)};
            if (q2 == 0) *(u32x2*)(pw + 512 + (r2 * 4 + hh2) * 8) = (u32x2){__float_as_uint(f0 * inv), __float_as_uint(f1 * inv)};
            __builtin_amdgcn_fence(__ATOMIC_RELEASE, "wavefront");
            __builtin_amdgcn_wave_barrier();
            __builtin_amdgcn_fence(__ATOMIC_ACQUIRE, "wavefront");
            __builtin_amdgcn_sched_barrier(0);
            f32x4 dd[8];
#pragma unroll
            for (int hh = 0; hh < 4; ++hh) {
                const u32x4 u = __builtin_bit_cast(u32x4, uf[hh]);
                u32x2 ulo = {u.x, u.y}, uhi = {u.z, u.w}, elo = {qx[hh].x, qx[hh].y}, ehi = {qx[hh].z, qx[hh].w};
                mfma16_v0(dd[2 * hh], __builtin_bit_cast(bf16x4, ulo), __builtin_bit_cast(bf16x4, elo));
                mfma16_v0(dd[2 * hh + 1], __builtin_bit_cast(bf16x4, uhi), __builtin_bit_cast(bf16x4, ehi));
            }
            u32x2 pa[4];
#pragma unroll
            for (int hh = 0; hh < 4; ++hh) pa[hh] = *(const u32x2*)(pw + ((hh * 8 + (i & 7)) * 2 + (g & 1)) * 8);
            f32x4 fa = *(const f32x4*)(pw + 512 + (i & 7) * 32), fb = *(const f32x4*)(pw + 512 + (i & 7) * 32 + 16);
            if (g >= 2) {
#pragma unroll
                for (int hh = 0; hh < 4; ++hh) pa[hh] = (u32x2){0u, 0u};
            }
            mfma_fence4(dd[0], dd[1], dd[2], dd[3]);
            mfma_fence4(dd[4], dd[5], dd[6], dd[7]);
            f32x4 acc = dd[0] * fa[0] + dd[1] * fa[1] + dd[2] * fa[2] + dd[3] * fa[3] + dd[4] * fb[0] + dd[5] * fb[1] + dd[6] * fb[2] + dd[7] * fb[3];
            u32x4 p01 = {pa[0].x, pa[0].y, pa[1].x, pa[1].y}, p23 = {pa[2].x, pa[2].y, pa[3].x, pa[3].y};
            mfma_v(acc, yp0, __builtin_bit_cast(bf16x8, p01));
            mfma_v(acc, yp1, __builtin_bit_cast(bf16x8, p23));
            mfma_fence1(acc);
            *(f32x4*)(lds + CH_LDS_RB + wave * 1024 + lane * 16) = acc;
        }
        STAMP(6);
        __syncthreads();
        if (hasB && wave == 0) {
            f32x4 v0 = *(const f32x4*)(lds + CH_LDS_RB + lane * 16);
            f32x4 v1 = *(const f32x4*)(lds + CH_LDS_RB + 1024 + lane * 16);
            f32x4 v2 = *(const f32x4*)(lds + CH_LDS_RB + 2048 + lane * 16);
            f32x4 v3 = *(const f32x4*)(lds + CH_LDS_RB + 3072 + lane * 16);
            f32x4 v = (v0 + v1) + (v2 + v3);
            if (i < 8) {
                u32x2 pk = {pack2(v[0], v[1]), pack2(v[2], v[3])};
                st2c(rs, OFF_MEMF + (size_t)((s + 1) * 7168 + tB * 256 + i * 32 + g * 8), pk);
            }
        }
    };
    step(0, BoolC<true>{});
    for (int s = 1; s < NSEG - 1; ++s) step(s, BoolC<false>{});
    if (c == 31) {
        unsigned spins = 0;
        const size_t o0 = OFF_MEMF + (size_t)((NSEG - 1) * 7168 + tid * 16), o1 = o0 + (tid < 192 ? 4096 : 0);
        for (;;) {
            asm volatile("" ::: "memory");
            u32x4 x0 = ld4c(rs, o0), x1 = ld4c(rs, o1);
            if (__all(max(umax4(x0), umax4(x1)) != SENT)) break;
            if (spin_fail(spins, tmo, 0x1FFu)) break;
        }
        __syncthreads();
        if (tid == 255) __hip_atomic_store((unsigned*)(ws + OFF_CNT + (size_t)CNT_PROG * 128), (unsigned)(NSEG - 1), __ATOMIC_RELAXED, __HIP_MEMORY_SCOPE_AGENT);
    }
}

#define PSTAMP(k)
constexpr int WK_LDS_SH = 0, WK_LDS_WO = 32768, WK_LDS_OUT = 65536;
template <int TYPE>
DI void ab_prologue(const unsigned short* __restrict__ Wq, const unsigned short* __restrict__ Wk, const unsigned short* __restrict__ Wv,
                    const unsigned short* __restrict__ Wo, char* __restrict__ ws, char* lds, const rsrc_t rs, int tile0, int wave, int lane) {
    const int i = lane & 15, g = lane >> 4;
    const int h = (tile0 - 448 * TYPE) / 28, ch = ((tile0 - 448 * TYPE) % 28) >> 2;
    const unsigned short* ms = TYPE == 0 ? Wk : Wv;
    bf16x8 st[4][2][8];
#pragma unroll
    for (int n = 0; n < 4; ++n) {
        const int pn = (wave + 4 * n < KK) ? wave + 4 * n : wave;
        const unsigned short* p = ms + (size_t)(32 * pn + i) * 4096 + h * 256 + 8 * g;
#pragma unroll
        for (int t = 0; t < 2; ++t)
#pragma unroll
            for (int ke = 0; ke < 8; ++ke) st[n][t][ke] = *(const bf16x8*)(p + (size_t)t * 16 * 4096 + ke * 32);
    }
    u32x4 sh[8];
    if (TYPE == 0) {
        const unsigned short* p = Wq + (size_t)(64 * ch + 16 * wave + i) * 4096 + h * 256 + 8 * g;
#pragma unroll
        for (int ke = 0; ke < 8; ++ke) sh[ke] = *(const u32x4*)(p + ke * 32);
    } else {
#pragma unroll
        for (int n = 0; n < 8; ++n) {
            const int e = threadIdx.x + n * 256, row = e >> 3, c8 = e & 7;
            sh[n] = *(const u32x4*)(Wo + ((size_t)h * 256 + row) * DM + 64 * ch + c8 * 8);
        }
    }
#pragma unroll
    for (int n = 0; n < 8; ++n) asm volatile("" : "+v"(sh[n]));
#pragma unroll
    for (int n = 0; n < 4; ++n)
#pragma unroll
        for (int t = 0; t < 2; ++t)
#pragma unroll
            for (int ke = 0; ke < 8; ++ke) asm volatile("" : "+v"(st[n][t][ke]));
    PSTAMP(0);
    if (TYPE == 0) {
#pragma unroll
        for (int ke = 0; ke < 8; ++ke) *(u32x4*)(lds + WK_LDS_SH + (wave * 8 + ke) * 1024 + lane * 16) = sh[ke];
    } else {
#pragma unroll
        for (int n = 0; n < 8; ++n) *(u32x4*)(lds + WK_LDS_WO + (threadIdx.x + n * 256) * 16) = sh[n];
        __syncthreads();
        const unsigned short* wo_t = (const unsigned short*)(lds + WK_LDS_WO);
#pragma unroll
        for (int ke = 0; ke < 8; ++ke) {
            bf16x8 v;
#pragma unroll
            for (int j = 0; j < 8; ++j) v[j] = (short)wo_t[(ke * 32 + 8 * g + j) * 64 + 16 * wave + i];
            *(bf16x8*)(lds + WK_LDS_SH + (wave * 8 + ke) * 1024 + lane * 16) = v;
        }
    }
    __syncthreads();
    PSTAMP(1);
#pragma unroll
    for (int n = 0; n < 4; ++n) {
        const int pr = wave + 4 * n;
        if (pr >= KK) break;
        const bf16x8 (&cur)[2][8] = st[n];
        if (TYPE == 0) {
#pragma unroll
            for (int ap = 0; ap < 2; ++ap) {
                bf16x8 qf[2][8];
#pragma unroll
                for (int a = 0; a < 2; ++a)
#pragma unroll
                    for (int ke = 0; ke < 8; ++ke) qf[a][ke] = *(const bf16x8*)(lds + WK_LDS_SH + ((2 * ap + a) * 8 + ke) * 1024 + lane * 16);
#pragma unroll
                for (int bb = 0; bb < 2; ++bb) {
                    f32x4 d0 = {0.f, 0.f, 0.f, 0.f}, d1 = {0.f, 0.f, 0.f, 0.f};
#pragma unroll
                    for (int ke = 0; ke < 8; ++ke) { d0 = mfma32(qf[0][ke], cur[bb][ke], d0); d1 = mfma32(qf[1][ke], cur[bb][ke], d1); }
                    st8c(rs, OFF_ANF + ((size_t)(h * 28 + 2 * pr + bb) * KK + 2 * ch + ap) * KB + lane * 16, pack8(d0, d1));
                }
#pragma unroll
                for (int a = 0; a < 2; ++a) {
                    f32x4 d0 = {0.f, 0.f, 0.f, 0.f}, d1 = {0.f, 0.f, 0.f, 0.f};
#pragma unroll
                    for (int ke = 0; ke < 8; ++ke) { d0 = mfma32(cur[0][ke], qf[a][ke], d0); d1 = mfma32(cur[1][ke], qf[a][ke], d1); }
                    const bf16x8 v = pack8(d0, d1);
                    *(bf16x8*)(lds + WK_LDS_OUT + ((2 * ap + a) * KK + pr) * 1024 + lane * 16) = v;
                    st8c(rs, OFF_WF + ((size_t)(tile0 + 2 * ap + a) * KK + pr) * KB + lane * 16, v);
                }
            }
        } else {
#pragma unroll
            for (int ct = 0; ct < 4; ++ct) {
                f32x4 d0 = {0.f, 0.f, 0.f, 0.f}, d1 = {0.f, 0.f, 0.f, 0.f};
#pragma unroll
                for (int ke = 0; ke < 8; ++ke) {
                    const bf16x8 fx = *(const bf16x8*)(lds + WK_LDS_SH + (ct * 8 + ke) * 1024 + lane * 16);
                    d0 = mfma32(cur[0][ke], fx, d0); d1 = mfma32(cur[1][ke], fx, d1);
                }
                const bf16x8 v = pack8(d0, d1);
                *(bf16x8*)(lds + WK_LDS_OUT + (ct * KK + pr) * 1024 + lane * 16) = v;
                st8c(rs, OFF_WF + ((size_t)(tile0 + ct) * KK + pr) * KB + lane * 16, v);
            }
        }
    }
    PSTAMP(2);
    signal_cnt(ws, TYPE == 0 ? CNT_AN + h : CNT_BW + ch, lane);
    PSTAMP(3);
    __syncthreads();
}

template <int TYPE>
DI void w1_phase(char* __restrict__ ws, char* lds, const rsrc_t rs, int tile0, int wave, int lane) {
    bf16x8 w[4][KK];
#pragma unroll
    for (int t = 0; t < 4; ++t)
#pragma unroll
        for (int kk = 0; kk < KK; ++kk) w[t][kk] = *(const bf16x8*)(lds + WK_LDS_OUT + (t * KK + kk) * 1024 + lane * 16);
    const int h = (tile0 - 448 * TYPE) / 28, t0 = (tile0 - 448 * TYPE) % 28;
    bf16x8 xa[KK], xb[KK];
    {
        const char* xp = ws + OFF_XNF + (size_t)(2 * wave) * KK * KB + lane * 16;
#pragma unroll
        for (int kk = 0; kk < KK; ++kk) xa[kk] = ldfrag(xp + (size_t)kk * KB);
    }
    for (int s = wave; s < NSEG; s += 4) {
        f32x4 acc[2][4];
        {
            const char* xp = ws + OFF_XNF + (size_t)(2 * s + 1) * KK * KB + lane * 16;
#pragma unroll
            for (int kk = 0; kk < KK; ++kk) xb[kk] = ldfrag(xp + (size_t)kk * KB);
        }
        __builtin_amdgcn_sched_barrier(0);
#pragma unroll
        for (int t = 0; t < 4; ++t) { if (TYPE == 0) mfma_Aa0(acc[0][t], w[t][0], xa[0]); else mfma_Ba0(acc[0][t], xa[0], w[t][0]); }
#pragma unroll
        for (int kk = 1; kk < KK; ++kk)
#pragma unroll
            for (int t = 0; t < 4; ++t) { if (TYPE == 0) mfma_Aa(acc[0][t], w[t][kk], xa[kk]); else mfma_Ba(acc[0][t], xa[kk], w[t][kk]); }
        __builtin_amdgcn_sched_barrier(0);
        {
            const int sn = (s + 4 < NSEG) ? s + 4 : s;
            const char* xp = ws + OFF_XNF + (size_t)(2 * sn) * KK * KB + lane * 16;
#pragma unroll
            for (int kk = 0; kk < KK; ++kk) xa[kk] = ldfrag(xp + (size_t)kk * KB);
        }
        __builtin_amdgcn_sched_barrier(0);
#pragma unroll
        for (int t = 0; t < 4; ++t) { if (TYPE == 0) mfma_Aa0(acc[1][t], w[t][0], xb[0]); else mfma_Ba0(acc[1][t], xb[0], w[t][0]); }
#pragma unroll
        for (int kk = 1; kk < KK; ++kk)
#pragma unroll
            for (int t = 0; t < 4; ++t) { if (TYPE == 0) mfma_Aa(acc[1][t], w[t][kk], xb[kk]); else mfma_Ba(acc[1][t], xb[kk], w[t][kk]); }
        mfma_fence4(acc[0][0], acc[0][1], acc[0][2], acc[0][3]);
        mfma_fence4(acc[1][0], acc[1][1], acc[1][2], acc[1][3]);
        if (TYPE == 0) {
#pragma unroll
            for (int q = 0; q < 2; ++q)
#pragma unroll
                for (int p = 0; p < 2; ++p)
                    st8c(rs, OFF_GTF + (((size_t)h * NRT + 2 * s + q) * KK + (t0 >> 1) + p) * KB + lane * 16, pack8(acc[q][2 * p], acc[q][2 * p + 1]));
        } else {
#pragma unroll
            for (int t = 0; t < 4; ++t)
                st8c(rs, OFF_UF + (((size_t)s * NT16 + t0 + t) * NH + h) * KB + lane * 16, pack8(acc[0][t], acc[1][t]));
        }
        signal_cnt(ws, CNT_G + s, lane);
    }
}

DI void ffn_task(char* __restrict__ ws, char* lds, const rsrc_t rs, const unsigned short* __restrict__ x, const unsigned short* __restrict__ b1,
                 const unsigned short* __restrict__ b2, int s, int wave, int lane) {
    const int i = lane & 15, g = lane >> 4;
    {
        bf16x8 xf[2][KK];
        const char* xp = ws + OFF_XNF + (size_t)(2 * s) * KK * KB + lane * 16;
#pragma unroll
        for (int q = 0; q < 2; ++q)
#pragma unroll
            for (int kk = 0; kk < KK; ++kk) xf[q][kk] = ldfrag(xp + (size_t)(q * KK + kk) * KB);
        const char* wp = ws + OFF_WF + (size_t)(896 + 28 * wave) * KK * KB + lane * 16;
        for (int pr = 0; pr < 14; ++pr) {
            bf16x8 wf[2][KK];
#pragma unroll
            for (int t = 0; t < 2; ++t)
#pragma unroll
                for (int kk = 0; kk < KK; ++kk) wf[t][kk] = ldfrag(wp + (size_t)((2 * pr + t) * KK + kk) * KB);
            f32x4 acc[2][2];
#pragma unroll
            for (int q = 0; q < 2; ++q)
#pragma unroll
                for (int t = 0; t < 2; ++t) acc[q][t] = (f32x4){0.f, 0.f, 0.f, 0.f};
#pragma unroll
            for (int kk = 0; kk < KK; ++kk)
#pragma unroll
                for (int q = 0; q < 2; ++q)
#pragma unroll
                    for (int t = 0; t < 2; ++t) acc[q][t] = mfma32(wf[t][kk], xf[q][kk], acc[q][t]);
#pragma unroll
            for (int t = 0; t < 2; ++t) {
                u32x2 bb = *(const u32x2*)(b1 + 16 * (28 * wave + 2 * pr + t) + 4 * g);
                float bv[4] = {bf_lo(bb.x), bf_hi(bb.x), bf_lo(bb.y), bf_hi(bb.y)};
#pragma unroll
                for (int q = 0; q < 2; ++q)
#pragma unroll
                    for (int r = 0; r < 4; ++r) acc[q][t][r] = gelu_tanh(acc[q][t][r] + bv[r]);
            }
#pragma unroll
            for (int q = 0; q < 2; ++q)
                *(bf16x8*)(lds + (size_t)(q * KF + 14 * wave + pr) * KB + lane * 16) = pack8(acc[q][0], acc[q][1]);
        }
    }
    __syncthreads();
    {
        f32x4 acc[2][7];
#pragma unroll
        for (int q = 0; q < 2; ++q)
#pragma unroll
            for (int t = 0; t < 7; ++t) acc[q][t] = (f32x4){0.f, 0.f, 0.f, 0.f};
        const char* wp = ws + OFF_W2F + (size_t)(7 * wave) * KF * KB + lane * 16;
#pragma unroll 4
        for (int kf = 0; kf < KF; ++kf) {
            bf16x8 h0 = *(const bf16x8*)(lds + (size_t)kf * KB + lane * 16), h1 = *(const bf16x8*)(lds + (size_t)(KF + kf) * KB + lane * 16);
#pragma unroll
            for (int t = 0; t < 7; ++t) {
                bf16x8 wf = ldfrag(wp + (size_t)(t * KF + kf) * KB);
                acc[0][t] = mfma32(wf, h0, acc[0][t]);
                acc[1][t] = mfma32(wf, h1, acc[1][t]);
            }
        }
#pragma unroll
        for (int t = 0; t < 7; ++t) {
            const int tc = 7 * wave + t;
            u32x2 bb = *(const u32x2*)(b2 + 16 * tc + 4 * g);
#pragma unroll
            for (int q = 0; q < 2; ++q) {
                const size_t o = (size_t)(32 * s + 16 * q + i) * DM + 16 * tc + 4 * g;
                u32x2 xv = *(const u32x2*)(x + o);
                f32x4 r = {acc[q][t][0] + bf_lo(bb.x) + bf_lo(xv.x), acc[q][t][1] + bf_hi(bb.x) + bf_hi(xv.x), acc[q][t][2] + bf_lo(bb.y) + bf_lo(xv.y), acc[q][t][3] + bf_hi(bb.y) + bf_hi(xv.y)};
                st4c(rs, OFF_FO + o * 4, __builtin_bit_cast(u32x4, r));
            }
        }
        signal_cnt(ws, CNT_F + s, lane);
    }
    __syncthreads();
}

DI void w3_loop(char* __restrict__ ws, const rsrc_t rs, int tile0, int lane) {
    const int i = lane & 15, g = lane >> 4;
    bf16x8 w[4][KK];
    {
        const size_t wp = OFF_WF + (size_t)tile0 * KK * KB + lane * 16;
#pragma unroll
        for (int t = 0; t < 4; ++t)
#pragma unroll
            for (int kk = 0; kk < KK; ++kk) w[t][kk] = ld8c(rs, wp + (size_t)(t * KK + kk) * KB);
    }
    const bool isA = tile0 < 448;
    const int h = (isA ? tile0 : tile0 - 448) / 28, t0 = (isA ? tile0 : tile0 - 448) % 28;
    for (int sp = 0; sp < NSEG / 2; ++sp) {
        bf16x8 xf[KK];
        wait_cnt_slow(ws, CNT_PROG, (unsigned)(sp == NSEG / 2 - 1 ? 2 * sp : 2 * sp + 1));
        {
            const size_t mp = OFF_MEMF + (size_t)(2 * sp + (i >> 3)) * 7168 + (i & 7) * 32 + g * 8;
            unsigned spins = 0;
            for (;;) {
                asm volatile("" ::: "memory");
                unsigned mx = 0;
#pragma unroll
                for (int kk = 0; kk < KK; ++kk) {
                    u32x2 lo_ = ld2c(rs, mp + kk * 512), hi_ = ld2c(rs, mp + kk * 512 + 256);
                    u32x4 xx_ = {lo_.x, lo_.y, hi_.x, hi_.y};
                    mx = max(mx, umax4(xx_));
                    xf[kk] = __builtin_bit_cast(bf16x8, xx_);
                }
                if (__all(mx != SENT)) break;
                __builtin_amdgcn_s_sleep(16);
                if (spin_fail(spins, (unsigned*)(ws + OFF_TMO), 0x500u + sp)) break;
            }
        }
        f32x4 acc[4];
        if (isA) {
#pragma unroll
            for (int t = 0; t < 4; ++t) mfma_Aa0(acc[t], w[t][0], xf[0]);
#pragma unroll
            for (int kk = 1; kk < KK; ++kk)
#pragma unroll
                for (int t = 0; t < 4; ++t) mfma_Aa(acc[t], w[t][kk], xf[kk]);
            mfma_fence4(acc[0], acc[1], acc[2], acc[3]);
#pragma unroll
            for (int p = 0; p < 2; ++p)
                st8c(rs, OFF_T2F + ((size_t)h * NSEG + 2 * sp + (i >> 3)) * 7168 + ((t0 >> 1) + p) * 512 + g * 128 + (i & 7) * 16, pack8(acc[2 * p], acc[2 * p + 1]));
        } else {
#pragma unroll
            for (int t = 0; t < 4; ++t) mfma_Ba0(acc[t], xf[0], w[t][0]);
#pragma unroll
            for (int kk = 1; kk < KK; ++kk)
#pragma unroll
                for (int t = 0; t < 4; ++t) mfma_Ba(acc[t], xf[kk], w[t][kk]);
            mfma_fence4(acc[0], acc[1], acc[2], acc[3]);
#pragma unroll
            for (int t = 0; t < 4; ++t)
                st2c(rs, OFF_YF + (((size_t)(2 * sp + (g >> 1)) * NT16 + t0 + t) * 8 + (h >> 1)) * 512 + (g & 1) * 256 + i * 16 + (h & 1) * 8, __builtin_bit_cast(u32x2, pack4(acc[t])));
        }
        signal_cnt(ws, isA ? CNT_T2 + sp * 16 + h : CNT_Y + sp * 7 + (t0 >> 2), lane);
    }
}

DI void f1_task(char* __restrict__ ws, const rsrc_t rs, int s, int h, int lane) {
    const int i = lane & 15, g = lane >> 4;
    f32x4 sc[2][3];
#pragma unroll
    for (int q = 0; q < 2; ++q)
#pragma unroll
        for (int k = 0; k < 3; ++k) sc[q][k] = (f32x4){0.f, 0.f, 0.f, 0.f};
    const char* xp = ws + OFF_XNF + (size_t)(2 * s) * KK * KB + lane * 16;
    const size_t gp = OFF_GTF + ((size_t)h * NRT + 2 * s) * KK * KB + lane * 16;
    const size_t tp = OFF_T2F + ((size_t)h * NSEG + s) * 7168 + g * 128 + (i & 7) * 16;
    bf16x8 xf[2][KK];
    {
        wait_cnt(ws, CNT_G + s, 224);
        bf16x8 g0[KK], g1[KK];
#pragma unroll
        for (int kk = 0; kk < KK; ++kk) { g0[kk] = ld8c(rs, gp + (size_t)kk * KB); g1[kk] = ld8c(rs, gp + (size_t)(KK + kk) * KB); }
#pragma unroll
        for (int q = 0; q < 2; ++q)
#pragma unroll
            for (int kk = 0; kk < KK; ++kk) xf[q][kk] = ldfrag(xp + (size_t)(q * KK + kk) * KB);
#pragma unroll
        for (int kk = 0; kk < KK; ++kk)
#pragma unroll
            for (int q = 0; q < 2; ++q) {
                sc[q][0] = mfma32(g0[kk], xf[q][kk], sc[q][0]);
                sc[q][1] = mfma32(g1[kk], xf[q][kk], sc[q][1]);
            }
    }
    wait_cnt_slow(ws, CNT_PROG, (unsigned)(s | 1));
    wait_cnt(ws, CNT_T2 + (s >> 1) * 16 + h, 7);
    {
        bf16x8 t2[KK];
#pragma unroll
        for (int kk = 0; kk < KK; ++kk) t2[kk] = ld8c(rs, tp + kk * 512);
#pragma unroll
        for (int kk = 0; kk < KK; ++kk)
#pragma unroll
            for (int q = 0; q < 2; ++q) sc[q][2] = mfma32(t2[kk], xf[q][kk], sc[q][2]);
    }
    const float cs = 0.0625f * 1.4426950408889634f;
    const float NEG = -3.0e38f;
#pragma unroll
    for (int q = 0; q < 2; ++q) {
        const int qi = 16 * q + i;
        float xs[8], ms[4];
#pragma unroll
        for (int j = 0; j < 8; ++j) {
            const int key = 16 * (j >> 2) + 4 * g + (j & 3);
            xs[j] = (key <= qi) ? sc[q][j >> 2][j & 3] : NEG;
        }
#pragma unroll
        for (int j = 0; j < 4; ++j) ms[j] = (g < 2) ? sc[q][2][j] : NEG;
        float m = fmaxf(fmaxf(fmaxf(xs[0], xs[1]), fmaxf(xs[2], xs[3])), fmaxf(fmaxf(xs[4], xs[5]), fmaxf(xs[6], xs[7])));
        m = fmaxf(m, fmaxf(fmaxf(ms[0], ms[1]), fmaxf(ms[2], ms[3])));
        m = xmax16(m); m = xmax32(m);
        const float mc = m * cs;
        float sum = 0.f;
#pragma unroll
        for (int j = 0; j < 8; ++j) { const int key = 16 * (j >> 2) + 4 * g + (j & 3); xs[j] = (key <= qi) ? fexp2(xs[j] * cs - mc) : 0.f; sum += xs[j]; }
#pragma unroll
        for (int j = 0; j < 4; ++j) { ms[j] = (g < 2) ? fexp2(ms[j] * cs - mc) : 0.f; sum += ms[j]; }
        sum = xadd16(sum); sum = xadd32(sum);
        const float inv = 1.f / sum;
        u32x4 px = {pack2(xs[0] * inv, xs[1] * inv), pack2(xs[2] * inv, xs[3] * inv), pack2(xs[4] * inv, xs[5] * inv), pack2(xs[6] * inv, xs[7] * inv)};
        st4c(rs, OFF_PXF + (((size_t)s * 2 + q) * NH + h) * KB + lane * 16, px);
        u32x2 pmv = {pack2(ms[0] * inv, ms[1] * inv), pack2(ms[2] * inv, ms[3] * inv)};
        st2c(rs, OFF_PMF + (((size_t)s * 2 + q) * 8 + (h >> 1)) * KB + lane * 16 + (h & 1) * 8, pmv);
    }
    signal_cnt(ws, CNT_P + s, lane);
}

DI void f2_task(char* __restrict__ ws, const rsrc_t rs, unsigned short* __restrict__ out, int s, int tp2, int lane) {
    const int i = lane & 15, g = lane >> 4;
    wait_cnt(ws, CNT_G + s, 224);
    wait_cnt(ws, CNT_F + s, 4);
    f32x4 acc[2][2];
    bf16x8 ua[2][NH];
    {
        const size_t up = OFF_UF + ((size_t)s * NT16 + 2 * tp2) * NH * KB + lane * 16;
#pragma unroll
        for (int t = 0; t < 2; ++t)
#pragma unroll
            for (int h = 0; h < NH; ++h) ua[t][h] = ld8c(rs, up + (size_t)(t * NH + h) * KB);
    }
#pragma unroll
    for (int q = 0; q < 2; ++q)
#pragma unroll
        for (int t = 0; t < 2; ++t)
            acc[q][t] = __builtin_bit_cast(f32x4, ld4c(rs, OFF_FO + ((size_t)(32 * s + 16 * q + i) * DM + 16 * (2 * tp2 + t) + 4 * g) * 4));
    wait_cnt_slow(ws, CNT_PROG, (unsigned)(s | 1));
    wait_cnt(ws, CNT_Y + (s >> 1) * 7 + (tp2 >> 1), 16);
    wait_cnt(ws, CNT_P + s, 16);
    {
        const size_t pp = OFF_PXF + (size_t)s * 2 * NH * KB + lane * 16;
        bf16x8 p0[NH], p1[NH];
#pragma unroll
        for (int h = 0; h < NH; ++h) { p0[h] = ld8c(rs, pp + (size_t)h * KB); p1[h] = ld8c(rs, pp + (size_t)(NH + h) * KB); }
        const size_t yp = OFF_YF + ((size_t)s * NT16 + 2 * tp2) * 8 * 512 + (g & 1) * 256 + i * 16;
        const size_t pq = OFF_PMF + (size_t)s * 2 * 8 * KB + lane * 16;
        bf16x8 ya[2][8], m0[8], m1[8];
#pragma unroll
        for (int hp = 0; hp < 8; ++hp) {
            m0[hp] = ld8c(rs, pq + (size_t)hp * KB); m1[hp] = ld8c(rs, pq + (size_t)(8 + hp) * KB);
            ya[0][hp] = ld8c(rs, yp + (size_t)hp * 512); ya[1][hp] = ld8c(rs, yp + (size_t)(8 + hp) * 512);
        }
#pragma unroll
        for (int h = 0; h < NH; ++h)
#pragma unroll
            for (int t = 0; t < 2; ++t) {
                acc[0][t] = mfma32(ua[t][h], p0[h], acc[0][t]);
                acc[1][t] = mfma32(ua[t][h], p1[h], acc[1][t]);
            }
#pragma unroll
        for (int hp = 0; hp < 8; ++hp)
#pragma unroll
            for (int t = 0; t < 2; ++t) {
                acc[0][t] = mfma32(ya[t][hp], m0[hp], acc[0][t]);
                acc[1][t] = mfma32(ya[t][hp], m1[hp], acc[1][t]);
            }
    }
#pragma unroll
    for (int q = 0; q < 2; ++q)
#pragma unroll
        for (int t = 0; t < 2; ++t) {
            const size_t o = (size_t)(32 * s + 16 * q + i) * DM + 16 * (2 * tp2 + t) + 4 * g;
            u32x2 r = {pack2(acc[q][t][0], acc[q][t][1]), pack2(acc[q][t][2], acc[q][t][3])};
            *(u32x2*)(out + o) = r;
        }
}

__global__ __launch_bounds__(256, 1) void k_mega(const unsigned short* __restrict__ x, const unsigned short* __restrict__ b1, const unsigned short* __restrict__ b2,
                                                 const unsigned short* __restrict__ Wq, const unsigned short* __restrict__ Wk,
                                                 const unsigned short* __restrict__ Wv, const unsigned short* __restrict__ Wo,
                                                 char* __restrict__ ws, unsigned short* __restrict__ out) {
    extern __shared__ __attribute__((aligned(16))) char lds[];
    const rsrc_t rs = __builtin_amdgcn_make_buffer_rsrc(ws, 0, (int)WS_END, 0x00020000);
#define WSTAMP(k)
    WSTAMP(0);
    if ((blockIdx.x & 7) == 0) { chain_role(ws, lds, rs); WSTAMP(3); return; }
    const int wid = (blockIdx.x >> 3) * 7 + (blockIdx.x & 7) - 1;
    const int wave = __builtin_amdgcn_readfirstlane(threadIdx.x >> 6), lane = threadIdx.x & 63;
    const int un = ((blockIdx.x & 7) - 1) * 32 + (blockIdx.x >> 3);
    const int tile0 = ((un % 14) / 7) * 448 + (un / 14) * 28 + (un % 7) * 4;
    if (tile0 < 448) { ab_prologue<0>(Wq, Wk, Wv, Wo, ws, lds, rs, tile0, wave, lane); WSTAMP(2); w1_phase<0>(ws, lds, rs, tile0, wave, lane); }
    else { ab_prologue<1>(Wq, Wk, Wv, Wo, ws, lds, rs, tile0, wave, lane); WSTAMP(2); w1_phase<1>(ws, lds, rs, tile0, wave, lane); }
    WSTAMP(1);
    if (wid >= 160) ffn_task(ws, lds, rs, x, b1, b2, wid - 160, wave, lane);
    if (wave == 0) { w3_loop(ws, rs, tile0, lane); WSTAMP(3); return; }
    const int fw = wid * 3 + wave - 1;
    for (int b = 0; b < 32; ++b) {
        {
            const int k = (fw + 672 * 2 - b * 32) % 672;
            if (k < 32) f1_task(ws, rs, 2 * b + (k >> 4), k & 15, lane);
        }
        {
            const int k = (fw + 672 * 2 - 336 - b * 28) % 672;
            if (k < 28) f2_task(ws, rs, out, 2 * b + (k / 14), k % 14, lane);
        }
    }
    WSTAMP(3);
}

extern "C" void kernel_launch(void* const* d_in, const int* in_sizes, int n_in,
                              void* d_out, int out_size, void* d_ws, size_t ws_size,
                              hipStream_t stream) {
    const unsigned short* x = (const unsigned short*)d_in[0];
    const unsigned short* mem0 = (const unsigned short*)d_in[1];
    const unsigned short* Wq = (const unsigned short*)d_in[2];
    const unsigned short* Wk = (const unsigned short*)d_in[3];
    const unsigned short* Wv = (const unsigned short*)d_in[4];
    const unsigned short* Wo = (const unsigned short*)d_in[5];
    const unsigned short* w1 = (const unsigned short*)d_in[8];
    const unsigned short* b1 = (const unsigned short*)d_in[9];
    const unsigned short* w2 = (const unsigned short*)d_in[10];
    const unsigned short* b2 = (const unsigned short*)d_in[11];
    char* ws = (char*)d_ws;
    unsigned short* out = (unsigned short*)d_out;
    (void)hipFuncSetAttribute((const void*)k_mega, hipFuncAttributeMaxDynamicSharedMemorySize, CH_LDS_SIZE);
    k_prep<<<PREP_GRID, PREP_NT, 0, stream>>>(x, mem0, w1, w2, ws);
    k_mega<<<256, 256, CH_LDS_SIZE, stream>>>(x, b1, b2, Wq, Wk, Wv, Wo, ws, out);
}
```

```cpp
#include <hip/hip_runtime.h>
#include <hip/hip_bf16.h>

typedef __attribute__((ext_vector_type(8))) short bf16x8;
typedef __attribute__((ext_vector_type(4))) short bf16x4;
typedef __attribute__((ext_vector_type(4))) float f32x4;
typedef __attribute__((ext_vector_type(4))) unsigned u32x4;
typedef __attribute__((ext_vector_type(2))) unsigned u32x2;
typedef __attribute__((ext_vector_type(2))) __bf16 bf2_t;
#define DI __device__ __forceinline__

constexpr int DM = 448, KK = 14, NT16 = 28, NH = 16, NSEG = 64, NRT = 128, FFD = 1792, KF = 56;
constexpr size_t KB = 1024;
constexpr size_t OFF_XNF = 0;
constexpr size_t OFF_WF = OFF_XNF + (size_t)NRT * KK * KB;
constexpr size_t OFF_ANF = OFF_WF + (size_t)1008 * KK * KB;
constexpr size_t OFF_W2F = OFF_ANF + (size_t)448 * KK * KB;
constexpr size_t OFF_GTF = OFF_W2F + (size_t)28 * KF * KB;
constexpr size_t OFF_UF = OFF_GTF + (size_t)NH * NRT * KK * KB;
constexpr size_t OFF_FO = OFF_UF + (size_t)NSEG * NT16 * NH * KB;
constexpr size_t OFF_MEMF = OFF_FO + (size_t)2048 * DM * 4;
constexpr size_t OFF_SMX = OFF_MEMF + (size_t)NSEG * 7168;
constexpr int E2B = 640, E2S = 32 * E2B;
constexpr size_t OFF_T2F = OFF_SMX + (size_t)NSEG * E2S;
constexpr size_t OFF_YF = OFF_T2F + (size_t)NH * NSEG * 7168;
constexpr size_t OFF_PXF = OFF_YF + (size_t)NSEG * NT16 * 8 * 512;
constexpr size_t OFF_PMF = OFF_PXF + (size_t)NSEG * 2 * NH * KB;
constexpr size_t OFF_CNT = OFF_PMF + (size_t)NSEG * 2 * 8 * KB;
constexpr int CNT_G = 0, CNT_P = 64, CNT_F = 128, CNT_TMO = 192, CNT_PROG = 193, CNT_T2 = 200, CNT_Y = CNT_T2 + 32 * 16, CNT_AN = CNT_Y + 32 * 7, CNT_BW = CNT_AN + 16, CNT_N = CNT_BW + 8;
constexpr size_t OFF_TMO = OFF_CNT + (size_t)CNT_TMO * 128;
constexpr size_t WS_END = OFF_CNT + (size_t)CNT_N * 128;
constexpr size_t OFF_DBG = WS_END;

constexpr unsigned SENT = 0xFFFFFFFFu;
constexpr unsigned kSpinCap = 1u << 16;

DI unsigned pack2(float a, float b) { bf2_t v = {(__bf16)a, (__bf16)b}; return __builtin_bit_cast(unsigned, v); }
DI float bf_lo(unsigned u) { return __uint_as_float(u << 16); }
DI float bf_hi(unsigned u) { return __uint_as_float(u & 0xFFFF0000u); }
DI f32x4 mfma32(bf16x8 a, bf16x8 b, f32x4 c) { return __builtin_amdgcn_mfma_f32_16x16x32_bf16(a, b, c, 0, 0, 0); }
DI bf16x8 ldfrag(const char* p) { return *(const bf16x8*)p; }
DI bf16x8 pack8(f32x4 a, f32x4 b) {
    u32x4 r = {pack2(a[0], a[1]), pack2(a[2], a[3]), pack2(b[0], b[1]), pack2(b[2], b[3])};
    return __builtin_bit_cast(bf16x8, r);
}
DI bf16x4 pack4(f32x4 a) { u32x2 r = {pack2(a[0], a[1]), pack2(a[2], a[3])}; return __builtin_bit_cast(bf16x4, r); }
DI bool spin_fail(unsigned& spins, unsigned* tmo, unsigned code = 1u) {
    if (++spins > kSpinCap) { unsigned exp_ = 0u; __hip_atomic_compare_exchange_strong(tmo, &exp_, code, __ATOMIC_RELAXED, __ATOMIC_RELAXED, __HIP_MEMORY_SCOPE_AGENT); return true; }
    if ((spins & 63u) == 0u) return __builtin_amdgcn_readfirstlane(__hip_atomic_load(tmo, __ATOMIC_RELAXED, __HIP_MEMORY_SCOPE_AGENT)) != 0u;
    return false;
}
typedef __amdgpu_buffer_rsrc_t rsrc_t;
DI bf16x8 ld8c(rsrc_t rs, size_t off) { return __builtin_bit_cast(bf16x8, __builtin_amdgcn_raw_buffer_load_b128(rs, (unsigned)off, 0, 16)); }
DI u32x4 ld4c(rsrc_t rs, size_t off) { return __builtin_amdgcn_raw_buffer_load_b128(rs, (unsigned)off, 0, 16); }
DI u32x2 ld2c(rsrc_t rs, size_t off) { return __builtin_amdgcn_raw_buffer_load_b64(rs, (unsigned)off, 0, 16); }
DI unsigned ld1c(rsrc_t rs, size_t off) { return __builtin_amdgcn_raw_buffer_load_b32(rs, (unsigned)off, 0, 16); }
DI void st8c(rsrc_t rs, size_t off, bf16x8 v) { __builtin_amdgcn_raw_buffer_store_b128(__builtin_bit_cast(u32x4, v), rs, (unsigned)off, 0, 16); }
DI void st4c(rsrc_t rs, size_t off, u32x4 v) { __builtin_amdgcn_raw_buffer_store_b128(v, rs, (unsigned)off, 0, 16); }
DI void st2c(rsrc_t rs, size_t off, u32x2 v) { __builtin_amdgcn_raw_buffer_store_b64(v, rs, (unsigned)off, 0, 16); }
DI void signal_cnt(char* ws, int idx, int lane) {
    asm volatile("s_waitcnt vmcnt(0)" ::: "memory");
    if (lane == 0) __hip_atomic_fetch_add((unsigned*)(ws + OFF_CNT + (size_t)idx * 128), 1u, __ATOMIC_RELAXED, __HIP_MEMORY_SCOPE_AGENT);
}
DI void wait_cnt(char* ws, int idx, unsigned target) {
    unsigned spins = 0;
    unsigned* p = (unsigned*)(ws + OFF_CNT + (size_t)idx * 128);
    for (;;) {
        unsigned v = __builtin_amdgcn_readfirstlane(__hip_atomic_load(p, __ATOMIC_RELAXED, __HIP_MEMORY_SCOPE_AGENT));
        if (v >= target) break;
        __builtin_amdgcn_s_sleep(8);
        if (spin_fail(spins, (unsigned*)(ws + OFF_TMO), 0x300u + idx)) break;
    }
    asm volatile("" ::: "memory");
}

DI void wait_cnt_slow(char* ws, int idx, unsigned target) {
    unsigned spins = 0;
    unsigned* p = (unsigned*)(ws + OFF_CNT + (size_t)idx * 128);
    for (;;) {
        unsigned v = __builtin_amdgcn_readfirstlane(__hip_atomic_load(p, __ATOMIC_RELAXED, __HIP_MEMORY_SCOPE_AGENT));
        if (v >= target) break;
        if (v + 1 < target) __builtin_amdgcn_s_sleep(100); else __builtin_amdgcn_s_sleep(12);
        if (spin_fail(spins, (unsigned*)(ws + OFF_TMO), 0x400u + idx)) break;
    }
    asm volatile("" ::: "memory");
}

DI float gelu_tanh(float z) {
    float u = 0.7978845608028654f * (z + 0.044715f * z * z * z);
    float e = __expf(2.f * u);
    float t = 1.f - 2.f / (1.f + e);
    return 0.5f * z * (1.f + t);
}

DI float xmax16(float v) { auto r = __builtin_amdgcn_permlane16_swap(__float_as_uint(v), __float_as_uint(v), false, false); return fmaxf(__uint_as_float(r[0]), __uint_as_float(r[1])); }
DI float xmax32(float v) { auto r = __builtin_amdgcn_permlane32_swap(__float_as_uint(v), __float_as_uint(v), false, false); return fmaxf(__uint_as_float(r[0]), __uint_as_float(r[1])); }
DI float xadd16(float v) { auto r = __builtin_amdgcn_permlane16_swap(__float_as_uint(v), __float_as_uint(v), false, false); return __uint_as_float(r[0]) + __uint_as_float(r[1]); }
DI float xadd32(float v) { auto r = __builtin_amdgcn_permlane32_swap(__float_as_uint(v), __float_as_uint(v), false, false); return __uint_as_float(r[0]) + __uint_as_float(r[1]); }
DI float qswap(float v) { return __uint_as_float(__builtin_amdgcn_update_dpp(0, __float_as_uint(v), 0xB1, 0xF, 0xF, true)); }
DI unsigned umax4(u32x4 v) { return max(max(v.x, v.y), max(v.z, v.w)); }
DI float fexp2(float x) { return __builtin_amdgcn_exp2f(x); }

DI void mfma_Aa0(f32x4& acc, const bf16x8& w, const bf16x8& m) { asm volatile("s_nop 1\n\tv_mfma_f32_16x16x32_bf16 %0, %1, %2, 0" : "=&v"(acc) : "a"(w), "v"(m)); }
DI void mfma_Aa(f32x4& acc, const bf16x8& w, const bf16x8& m) { asm volatile("s_nop 1\n\tv_mfma_f32_16x16x32_bf16 %0, %1, %2, %0" : "+v"(acc) : "a"(w), "v"(m)); }
DI void mfma_Ba0(f32x4& acc, const bf16x8& m, const bf16x8& w) { asm volatile("s_nop 1\n\tv_mfma_f32_16x16x32_bf16 %0, %1, %2, 0" : "=&v"(acc) : "v"(m), "a"(w)); }
DI void mfma_Ba(f32x4& acc, const bf16x8& m, const bf16x8& w) { asm volatile("s_nop 1\n\tv_mfma_f32_16x16x32_bf16 %0, %1, %2, %0" : "+v"(acc) : "v"(m), "a"(w)); }
DI void mfma_v0(f32x4& acc, const bf16x8& a, const bf16x8& b) { asm volatile("s_nop 1\n\tv_mfma_f32_16x16x32_bf16 %0, %1, %2, 0" : "=&v"(acc) : "v"(a), "v"(b)); }
DI void mfma_v(f32x4& acc, const bf16x8& a, const bf16x8& b) { asm volatile("s_nop 1\n\tv_mfma_f32_16x16x32_bf16 %0, %1, %2, %0" : "+v"(acc) : "v"(a), "v"(b)); }
DI void mfma16_v0(f32x4& acc, const bf16x4& a, const bf16x4& b) { asm volatile("s_nop 1\n\tv_mfma_f32_16x16x16_bf16 %0, %1, %2, 0" : "=&v"(acc) : "v"(a), "v"(b)); }
DI void mfma16_v(f32x4& acc, const bf16x4& a, const bf16x4& b) { asm volatile("s_nop 1\n\tv_mfma_f32_16x16x16_bf16 %0, %1, %2, %0" : "+v"(acc) : "v"(a), "v"(b)); }
DI void mfma_fence4(f32x4& a, f32x4& b, f32x4& c, f32x4& d) { asm volatile("s_nop 7\n\ts_nop 1" : "+v"(a), "+v"(b), "+v"(c), "+v"(d)); }
DI void pin(f32x4& a) { asm volatile("" : "+v"(a)); }
DI void mfma_fence1(f32x4& a) { asm volatile("s_nop 7\n\ts_nop 1" : "+v"(a)); }

constexpr int PREP_GRID = 256, PREP_NT = 448;

DI void ln_row(const unsigned short* __restrict__ x, char* __restrict__ ws, int row, int lane, const u32x2 (&u)[2]) {
    float v[2][4];
#pragma unroll
    for (int it = 0; it < 2; ++it) { v[it][0] = bf_lo(u[it].x); v[it][1] = bf_hi(u[it].x); v[it][2] = bf_lo(u[it].y); v[it][3] = bf_hi(u[it].y); }
    float sum = 0.f;
#pragma unroll
    for (int it = 0; it < 2; ++it) sum += v[it][0] + v[it][1] + v[it][2] + v[it][3];
#pragma unroll
    for (int o = 32; o >= 1; o >>= 1) sum += __shfl_xor(sum, o);
    const float mean = sum * (1.f / DM);
    float sq = 0.f;
#pragma unroll
    for (int it = 0; it < 2; ++it) {
        if (lane + it * 64 < 112) {
#pragma unroll
            for (int j = 0; j < 4; ++j) { float d = v[it][j] - mean; sq += d * d; }
        }
    }
#pragma unroll
    for (int o = 32; o >= 1; o >>= 1) sq += __shfl_xor(sq, o);
    const float rstd = 1.f / sqrtf(sq * (1.f / DM) + 1e-5f);
    const int rt = row >> 4, ii = row & 15;
#pragma unroll
    for (int it = 0; it < 2; ++it) {
        int grp = lane + it * 64;
        if (grp < 112) {
            int d = grp * 4, kk = d >> 5, dd = d & 31, half = dd >> 4, gg = (dd & 15) >> 2;
            u32x2 o = {pack2((v[it][0] - mean) * rstd, (v[it][1] - mean) * rstd), pack2((v[it][2] - mean) * rstd, (v[it][3] - mean) * rstd)};
            *(u32x2*)(ws + OFF_XNF + ((size_t)(rt * KK + kk) * 64 + (ii + 16 * gg)) * 16 + half * 8) = o;
        }
    }
}

DI bf16x8 w12_load(const unsigned short* __restrict__ w1, const unsigned short* __restrict__ w2, int f, int i, int g) {
    bf16x8 v;
    if (f < 112 * KK) {
        int tf = f / KK, kk = f % KK;
#pragma unroll
        for (int j = 0; j < 8; ++j) v[j] = (short)w1[(size_t)(32 * kk + 16 * (j >> 2) + 4 * g + (j & 3)) * FFD + 16 * tf + i];
    } else {
        int f2 = f - 112 * KK;
        int t = f2 / KF, kf = f2 % KF;
#pragma unroll
        for (int j = 0; j < 8; ++j) v[j] = (short)w2[(size_t)(32 * kf + 16 * (j >> 2) + 4 * g + (j & 3)) * DM + 16 * t + i];
    }
    return v;
}
DI void w12_store(char* __restrict__ ws, int f, int lane, bf16x8 v) {
    if (f < 112 * KK) *(bf16x8*)(ws + OFF_WF + ((size_t)896 * KK + f) * KB + lane * 16) = v;
    else *(bf16x8*)(ws + OFF_W2F + (size_t)(f - 112 * KK) * KB + lane * 16) = v;
}

__global__ __launch_bounds__(PREP_NT) void k_prep(const unsigned short* __restrict__ x, const unsigned short* __restrict__ mem0,
                                              const unsigned short* __restrict__ w1, const unsigned short* __restrict__ w2, char* __restrict__ ws) {
    const int wave = __builtin_amdgcn_readfirstlane(threadIdx.x >> 6), lane = threadIdx.x & 63;
    const int i = lane & 15, g = lane >> 4;
    const int b = blockIdx.x;
    const int gw = b * 7 + wave;
    u32x2 xr[2][2];
#pragma unroll
    for (int n = 0; n < 2; ++n) {
        const int row = gw + n * 1792;
#pragma unroll
        for (int it = 0; it < 2; ++it) {
            const int grp = lane + it * 64;
            xr[n][it] = (u32x2){0u, 0u};
            if (row < 2048 && grp < 112) xr[n][it] = *(const u32x2*)(x + (size_t)row * DM + grp * 4);
        }
    }
    bf16x8 wv[2];
#pragma unroll
    for (int n = 0; n < 2; ++n) { const int f = gw + n * 1792; wv[n] = w12_load(w1, w2, f < 3136 ? f : 0, i, g); }
    {
        const size_t n16 = ((size_t)(NSEG - 1) * 7168 + (size_t)NSEG * E2S) / 16;
        u32x4 sv = {SENT, SENT, SENT, SENT};
        for (size_t idx = (size_t)b * PREP_NT + threadIdx.x; idx < n16; idx += (size_t)PREP_GRID * PREP_NT)
            *(u32x4*)(ws + OFF_MEMF + 7168 + idx * 16) = sv;
    }
    if (b == 255) {
        for (int e = threadIdx.x; e < 8 * 112; e += PREP_NT) {
            int r = e / 112, grp = e % 112;
            int d = grp * 4, t = d >> 4, gg = (d & 15) >> 2;
            u32x2 u = *(const u32x2*)(mem0 + r * DM + d);
            *(u32x2*)(ws + OFF_MEMF + t * 256 + r * 32 + gg * 8) = u;
        }
    }
    if (b == 254) {
        for (int e = threadIdx.x; e < CNT_N; e += PREP_NT) *(unsigned*)(ws + OFF_CNT + (size_t)e * 128) = 0u;
    }
    ln_row(x, ws, gw, lane, xr[0]);
    if (gw + 1792 < 2048) ln_row(x, ws, gw + 1792, lane, xr[1]);
    w12_store(ws, gw, lane, wv[0]);
    if (gw + 1792 < 3136) w12_store(ws, gw + 1792, lane, wv[1]);
}

constexpr int CH_LDS_B = 0;
constexpr int CH_LDS_MEM = 112 * 1024;
constexpr int CH_LDS_RA = CH_LDS_MEM + 7168;
constexpr int CH_LDS_RB = CH_LDS_RA + 8192;
constexpr int CH_LDS_PW = CH_LDS_RB + 4096;
constexpr int CH_LDS_SIZE = CH_LDS_PW + 4096;

#define E2POS 5
#define DNB 8
#define ADMAX 8
#define D1 8
#define D2 0

template <bool B> struct BoolC { static constexpr bool value = B; };
DI void chain_role(char* __restrict__ ws, char* lds, const rsrc_t rs) {
    const int c = blockIdx.x >> 3;
    const int tid = threadIdx.x, wave = __builtin_amdgcn_readfirstlane(tid >> 6), lane = tid & 63;
    const int i = lane & 15, g = lane >> 4;
    const int h = c >> 1, half = c & 1;
    const bool hasB = c < NT16;
    const int tB = hasB ? c : 0;
    unsigned* tmo = (unsigned*)(ws + OFF_TMO);
    const float cs = 0.0625f * 1.4426950408889634f;

    bf16x8 wa[3][KK], wsh[7], wb[2][KK];
    {
        wait_cnt(ws, CNT_AN + h, 28);
        const size_t ap = OFF_ANF + (size_t)(h * 28 + 14 * half) * KK * KB + lane * 16;
#pragma unroll
        for (int t = 0; t < 3; ++t)
#pragma unroll
            for (int kk = 0; kk < KK; ++kk) wa[t][kk] = ld8c(rs, ap + (size_t)((3 * wave + t) * KK + kk) * KB);
#pragma unroll
        for (int k = 0; k < 7; ++k) wsh[k] = ld8c(rs, ap + (size_t)((12 + (wave >> 1)) * KK + 7 * (wave & 1) + k) * KB);
    }
    const int gk0 = wave < 2 ? 4 * wave : 8 + 3 * (wave - 2);
    const int gkn = wave < 2 ? 4 : 3;
    bf16x8 gf[4], uf[4];
    wait_cnt(ws, CNT_G + 0, 224);
    __syncthreads();
    {
        const size_t gp = OFF_GTF + (((size_t)h * NRT + half) * KK + gk0) * KB + lane * 16;
#pragma unroll
        for (int k = 0; k < 4; ++k) gf[k] = ld8c(rs, gp + (size_t)(k < gkn ? k : 0) * KB);
    }
    const char* mp = lds + CH_LDS_MEM + g * 128 + (i & 7) * 16;
    const int hh2 = lane >> 4, r2 = (lane >> 1) & 7, q2 = lane & 1;

    int d1v = hasB ? D1 : DNB, d2v = D2;
    auto step = [&](const int s, auto FIRST) __attribute__((always_inline)) {
        {
            u32x4 x0, x1;
            unsigned spins = 0;
            const size_t o0 = OFF_MEMF + (size_t)(s * 7168 + tid * 16), o1 = o0 + (tid < 192 ? 4096 : 0);
            const size_t oc = OFF_CNT + (size_t)(CNT_G + s + 1) * 128;
            if (s > 0) for (int d = 0; d < d1v; ++d) __builtin_amdgcn_s_sleep(1);
            for (;;) {
                asm volatile("" ::: "memory");
                x0 = ld4c(rs, o0);
                x1 = ld4c(rs, o1);
                unsigned cv = ld1c(rs, oc);
                if (__all((max(umax4(x0), umax4(x1)) != SENT) && (cv >= 224u))) break;
                if (spin_fail(spins, tmo, 0x100u + s)) break;
            }
            if (hasB) { if (spins != 0u && s > 0 && d1v < D1 + ADMAX) d1v += 2; }
            else { if (spins == 0u) { if (d1v > 8) d1v -= 1; } else if (spins > 1u) d1v += 4; }
            const int t0 = tid >> 4, rr = (tid >> 1) & 7, gp = tid & 1;
            char* d0 = lds + CH_LDS_MEM + (t0 >> 1) * 512 + gp * 256 + rr * 16 + (t0 & 1) * 8;
            *(u32x2*)(d0) = (u32x2){x0.x, x0.y};
            *(u32x2*)(d0 + 128) = (u32x2){x0.z, x0.w};
            if (tid < 192) {
                *(u32x2*)(d0 + 8 * 512) = (u32x2){x1.x, x1.y};
                *(u32x2*)(d0 + 8 * 512 + 128) = (u32x2){x1.z, x1.w};
            }
        }
        {
            const size_t up = OFF_UF + (((size_t)s * NT16 + tB) * NH + 4 * wave) * KB + lane * 16;
#pragma unroll
            for (int k = 0; k < 4; ++k) uf[k] = ld8c(rs, up + (size_t)k * KB);
        }
        __syncthreads();
        if (c == 31 && tid == 255) __hip_atomic_store((unsigned*)(ws + OFF_CNT + (size_t)CNT_PROG * 128), (unsigned)s, __ATOMIC_RELAXED, __HIP_MEMORY_SCOPE_AGENT);
        bf16x8 mf[KK];
#pragma unroll
        for (int kk = 0; kk < KK; ++kk) mf[kk] = *(const bf16x8*)(mp + kk * 512);
        {
            bf16x8 mg[4];
            bf16x4 mk[4];
#pragma unroll
            for (int k = 0; k < 4; ++k) mg[k] = *(const bf16x8*)(mp + (gk0 + (k < gkn ? k : 0)) * 512);
#pragma unroll
            for (int t = 0; t < 4; ++t) {
                const int tl = (t < 3) ? 3 * wave + t : 12 + (wave >> 1);
                const int tg = 14 * half + tl;
                mk[t] = *(const bf16x4*)(mp + (tg >> 1) * 512 + (tg & 1) * 8);
            }
            f32x4 ta[4];
            f32x4 sx, sm;
            if (wave & 1) {
                mfma_Aa0(ta[3], wsh[0], mf[7]);
#pragma unroll
                for (int k = 1; k < 7; ++k) mfma_Aa(ta[3], wsh[k], mf[7 + k]);
            } else {
                mfma_Aa0(ta[3], wsh[0], mf[0]);
#pragma unroll
                for (int k = 1; k < 7; ++k) mfma_Aa(ta[3], wsh[k], mf[k]);
            }
            mfma_Aa0(ta[0], wa[0][0], mf[0]);
#pragma unroll
            for (int kk = 1; kk < KK; ++kk) mfma_Aa(ta[0], wa[0][kk], mf[kk]);
            mfma_Aa0(ta[1], wa[1][0], mf[0]);
#pragma unroll
            for (int kk = 1; kk < 4; ++kk) mfma_Aa(ta[1], wa[1][kk], mf[kk]);
            pin(ta[3]); pin(ta[0]);
            mfma16_v0(sm, mk[3], pack4(ta[3]));
            mfma16_v(sm, mk[0], pack4(ta[0]));
#pragma unroll
            for (int kk = 4; kk < KK; ++kk) mfma_Aa(ta[1], wa[1][kk], mf[kk]);
            mfma_Aa0(ta[2], wa[2][0], mf[0]);
#pragma unroll
            for (int kk = 1; kk < 4; ++kk) mfma_Aa(ta[2], wa[2][kk], mf[kk]);
            pin(ta[1]);
            mfma16_v(sm, mk[1], pack4(ta[1]));
#pragma unroll
            for (int kk = 4; kk < KK; ++kk) mfma_Aa(ta[2], wa[2][kk], mf[kk]);
            mfma_v0(sx, gf[0], mg[0]);
            mfma_v(sx, gf[1], mg[1]);
            mfma_v(sx, gf[2], mg[2]);
            if (wave < 2) mfma_v(sx, gf[3], mg[3]);
            pin(ta[2]);
            mfma16_v(sm, mk[2], pack4(ta[2]));
            mfma_fence4(sm, sx, ta[0], ta[1]);
            *(f32x4*)(lds + CH_LDS_RA + (wave * 2 + 0) * 1024 + lane * 16) = sx;
            *(f32x4*)(lds + CH_LDS_RA + (wave * 2 + 1) * 1024 + lane * 16) = sm;
        }
        __syncthreads();
        const size_t e2w = OFF_SMX + (size_t)s * E2S + (size_t)c * E2B;
        if (wave < 2) {
            f32x4 v0 = *(const f32x4*)(lds + CH_LDS_RA + (0 + wave) * 1024 + lane * 16);
            f32x4 v1 = *(const f32x4*)(lds + CH_LDS_RA + (2 + wave) * 1024 + lane * 16);
            f32x4 v2 = *(const f32x4*)(lds + CH_LDS_RA + (4 + wave) * 1024 + lane * 16);
            f32x4 v3 = *(const f32x4*)(lds + CH_LDS_RA + (6 + wave) * 1024 + lane * 16);
            f32x4 v = (v0 + v1) + (v2 + v3);
            if (wave == 0) {
                float m = fmaxf(fmaxf(v[0], v[1]), fmaxf(v[2], v[3]));
                m = xmax16(m); m = xmax32(m);
                const float mc = m * cs;
                float e0 = fexp2(v[0] * cs - mc), e1 = fexp2(v[1] * cs - mc), e2 = fexp2(v[2] * cs - mc), e3 = fexp2(v[3] * cs - mc);
                u32x2 pk = {pack2(e0, e1), pack2(e2, e3)};
                if (i < 8) st2c(rs, e2w + i * 32 + g * 8, pk);
                float sum = (bf_lo(pk.x) + bf_hi(pk.x)) + (bf_lo(pk.y) + bf_hi(pk.y));
                sum = xadd16(sum); sum = xadd32(sum);
                u32x2 st = {__float_as_uint(m), __float_as_uint(sum)};
                if (lane < 8) st2c(rs, e2w + 512 + i * 8, st);
            } else {
                if (i < 8 && g < 2) st4c(rs, e2w + 256 + i * 32 + g * 16, __builtin_bit_cast(u32x4, v));
            }
        }
        {
            const size_t gp = OFF_GTF + (((size_t)h * NRT + 2 * (s + 1) + half) * KK + gk0) * KB + lane * 16;
#pragma unroll
            for (int k = 0; k < 4; ++k) gf[k] = ld8c(rs, gp + (size_t)(k < gkn ? k : 0) * KB);
        }
        if constexpr (decltype(FIRST)::value) {
            wait_cnt(ws, CNT_BW + (tB >> 2), 64);
            const size_t bp = OFF_WF + (size_t)(448 + tB) * KK * KB + lane * 16;
#pragma unroll
            for (int hh = 0; hh < 2; ++hh)
#pragma unroll
                for (int kk = 0; kk < KK; ++kk)
                    *(bf16x8*)(lds + CH_LDS_B + (size_t)((wave * 2 + hh) * KK + kk) * KB + lane * 16) = ld8c(rs, bp + (size_t)((4 * wave + 2 + hh) * 28 * KK + kk) * KB);
#pragma unroll
            for (int hh = 0; hh < 2; ++hh)
#pragma unroll
                for (int kk = 0; kk < KK; ++kk) wb[hh][kk] = ld8c(rs, bp + (size_t)((4 * wave + hh) * 28 * KK + kk) * KB);
        }
        if (hasB) {
            f32x4 ya[4];
            const char* lb = lds + CH_LDS_B + (size_t)(wave * 2) * KK * KB + lane * 16;
            mfma_Ba0(ya[0], mf[0], wb[0][0]);
            mfma_v0(ya[1], mf[0], wb[1][0]);
#pragma unroll
            for (int k = 1; k < KK; ++k) {
                mfma_Ba(ya[0], mf[k], wb[0][k]);
                mfma_v(ya[1], mf[k], wb[1][k]);
            }
            u32x4 qx[4], qm0, qm1, qs;
            const size_t e2r = OFF_SMX + (size_t)s * E2S;
            const size_t ox = e2r + (size_t)(8 * wave) * E2B + (i & 7) * 32 + g * 8;
            const size_t om = e2r + (size_t)(8 * wave + 2 * hh2) * E2B + 256 + r2 * 32 + q2 * 16;
            const size_t os = e2r + (size_t)(8 * wave + 2 * hh2) * E2B + 512 + r2 * 8;
#define E2_LOADS() do { _Pragma("unroll") for (int hh = 0; hh < 4; ++hh) { \
                        u32x2 lo_ = ld2c(rs, ox + (size_t)(2 * hh) * E2B), hi_ = ld2c(rs, ox + (size_t)(2 * hh + 1) * E2B); \
                        qx[hh] = (u32x4){lo_.x, lo_.y, hi_.x, hi_.y}; } \
                    qm0 = ld4c(rs, om); qm1 = ld4c(rs, om + E2B); \
                    { u32x2 lo_ = ld2c(rs, os), hi_ = ld2c(rs, os + E2B); qs = (u32x4){lo_.x, lo_.y, hi_.x, hi_.y}; } } while (0)
            {
                bf16x8 lq[2][4];
#pragma unroll
                for (int k = 0; k < 2; ++k) { lq[0][2 * k] = *(const bf16x8*)(lb + (size_t)k * KB); lq[0][2 * k + 1] = *(const bf16x8*)(lb + (size_t)(KK + k) * KB); }
#pragma unroll
                for (int gq = 0; gq < 7; ++gq) {
                    if (gq < 6) {
#pragma unroll
                        for (int k = 0; k < 2; ++k) {
                            lq[(gq + 1) & 1][2 * k] = *(const bf16x8*)(lb + (size_t)(2 * gq + 2 + k) * KB);
                            lq[(gq + 1) & 1][2 * k + 1] = *(const bf16x8*)(lb + (size_t)(KK + 2 * gq + 2 + k) * KB);
                        }
                    }
                    if (gq == E2POS) { for (int d = 0; d < d2v; ++d) __builtin_amdgcn_s_sleep(1); asm volatile("" ::: "memory"); E2_LOADS(); }
                    __builtin_amdgcn_sched_barrier(0);
                    if (gq == 0) { mfma_v0(ya[2], mf[0], lq[0][0]); mfma_v0(ya[3], mf[0], lq[0][1]); }
                    else { mfma_v(ya[2], mf[2 * gq], lq[gq & 1][0]); mfma_v(ya[3], mf[2 * gq], lq[gq & 1][1]); }
                    mfma_v(ya[2], mf[2 * gq + 1], lq[gq & 1][2]); mfma_v(ya[3], mf[2 * gq + 1], lq[gq & 1][3]);
                }
            }
            mfma_fence4(ya[0], ya[1], ya[2], ya[3]);
            bf16x8 yp0 = pack8(ya[0], ya[1]), yp1 = pack8(ya[2], ya[3]);
            __builtin_amdgcn_sched_barrier(0);
            {
                unsigned spins = 0;
                if (E2POS >= 7) { for (int d = 0; d < d2v; ++d) __builtin_amdgcn_s_sleep(1); asm volatile("" ::: "memory"); E2_LOADS(); }
                for (;;) {
                    unsigned mx = max(max(max(umax4(qx[0]), umax4(qx[1])), max(umax4(qx[2]), umax4(qx[3]))), max(max(umax4(qm0), umax4(qm1)), umax4(qs)));
                    if (__all(mx != SENT)) break;
                    if (spin_fail(spins, tmo, 0x200u + s)) break;
                    asm volatile("" ::: "memory");
                    E2_LOADS();
                }
                if (spins != 0u && s > 0 && d2v < D2 + ADMAX) d2v += 2;
            }
            f32x4 ms = __builtin_bit_cast(f32x4, qm0) + __builtin_bit_cast(f32x4, qm1);
            const float m0 = __uint_as_float(qs.x), s0 = __uint_as_float(qs.y), m1 = __uint_as_float(qs.z), s1 = __uint_as_float(qs.w);
            float mx = fmaxf(fmaxf(ms[0], ms[1]), fmaxf(ms[2], ms[3]));
            mx = fmaxf(mx, qswap(mx));
            const float Mc = fmaxf(mx, fmaxf(m0, m1)) * cs;
            float e0 = fexp2(ms[0] * cs - Mc), e1 = fexp2(ms[1] * cs - Mc), e2 = fexp2(ms[2] * cs - Mc), e3 = fexp2(ms[3] * cs - Mc);
            float f0 = fexp2(m0 * cs - Mc), f1 = fexp2(m1 * cs - Mc);
            float sm = (e0 + e1) + (e2 + e3);
            sm += qswap(sm);
            const float inv = __builtin_amdgcn_rcpf(sm + f0 * s0 + f1 * s1);
            char* pw = lds + CH_LDS_PW + wave * 1024;
            *(u32x2*)(pw + ((hh2 * 8 + r2) * 2 + q2) * 8) = (u32x2){pack2(e0 * inv, e1 * inv), pack2(e2 * inv, e3 * inv)};
            if (q2 == 0) *(u32x2*)(pw + 512 + (r2 * 4 + hh2) * 8) = (u32x2){__float_as_uint(f0 * inv), __float_as_uint(f1 * inv)};
            __builtin_amdgcn_fence(__ATOMIC_RELEASE, "wavefront");
            __builtin_amdgcn_wave_barrier();
            __builtin_amdgcn_fence(__ATOMIC_ACQUIRE, "wavefront");
            __builtin_amdgcn_sched_barrier(0);
            f32x4 dd[8];
#pragma unroll
            for (int hh = 0; hh < 4; ++hh) {
                const u32x4 u = __builtin_bit_cast(u32x4, uf[hh]);
                u32x2 ulo = {u.x, u.y}, uhi = {u.z, u.w}, elo = {qx[hh].x, qx[hh].y}, ehi = {qx[hh].z, qx[hh].w};
                mfma16_v0(dd[2 * hh], __builtin_bit_cast(bf16x4, ulo), __builtin_bit_cast(bf16x4, elo));
                mfma16_v0(dd[2 * hh + 1], __builtin_bit_cast(bf16x4, uhi), __builtin_bit_cast(bf16x4, ehi));
            }
            u32x2 pa[4];
#pragma unroll
            for (int hh = 0; hh < 4; ++hh) pa[hh] = *(const u32x2*)(pw + ((hh * 8 + (i & 7)) * 2 + (g & 1)) * 8);
            f32x4 fa = *(const f32x4*)(pw + 512 + (i & 7) * 32), fb = *(const f32x4*)(pw + 512 + (i & 7) * 32 + 16);
            if (g >= 2) {
#pragma unroll
                for (int hh = 0; hh < 4; ++hh) pa[hh] = (u32x2){0u, 0u};
            }
            mfma_fence4(dd[0], dd[1], dd[2], dd[3]);
            mfma_fence4(dd[4], dd[5], dd[6], dd[7]);
            f32x4 acc = dd[0] * fa[0] + dd[1] * fa[1] + dd[2] * fa[2] + dd[3] * fa[3] + dd[4] * fb[0] + dd[5] * fb[1] + dd[6] * fb[2] + dd[7] * fb[3];
            u32x4 p01 = {pa[0].x, pa[0].y, pa[1].x, pa[1].y}, p23 = {pa[2].x, pa[2].y, pa[3].x, pa[3].y};
            mfma_v(acc, yp0, __builtin_bit_cast(bf16x8, p01));
            mfma_v(acc, yp1, __builtin_bit_cast(bf16x8, p23));
            mfma_fence1(acc);
            *(f32x4*)(lds + CH_LDS_RB + wave * 1024 + lane * 16) = acc;
        }
        __syncthreads();
        if (hasB && wave == 0) {
            f32x4 v0 = *(const f32x4*)(lds + CH_LDS_RB + lane * 16);
            f32x4 v1 = *(const f32x4*)(lds + CH_LDS_RB + 1024 + lane * 16);
            f32x4 v2 = *(const f32x4*)(lds + CH_LDS_RB + 2048 + lane * 16);
            f32x4 v3 = *(const f32x4*)(lds + CH_LDS_RB + 3072 + lane * 16);
            f32x4 v = (v0 + v1) + (v2 + v3);
            if (i < 8) {
                u32x2 pk = {pack2(v[0], v[1]), pack2(v[2], v[3])};
                st2c(rs, OFF_MEMF + (size_t)((s + 1) * 7168 + tB * 256 + i * 32 + g * 8), pk);
            }
        }
    };
    step(0, BoolC<true>{});
    for (int s = 1; s < NSEG - 1; ++s) step(s, BoolC<false>{});
    if (c == 31) {
        unsigned spins = 0;
        const size_t o0 = OFF_MEMF + (size_t)((NSEG - 1) * 7168 + tid * 16), o1 = o0 + (tid < 192 ? 4096 : 0);
        for (;;) {
            asm volatile("" ::: "memory");
            u32x4 x0 = ld4c(rs, o0), x1 = ld4c(rs, o1);
            if (__all(max(umax4(x0), umax4(x1)) != SENT)) break;
            if (spin_fail(spins, tmo, 0x1FFu)) break;
        }
        __syncthreads();
        if (tid == 255) __hip_atomic_store((unsigned*)(ws + OFF_CNT + (size_t)CNT_PROG * 128), (unsigned)(NSEG - 1), __ATOMIC_RELAXED, __HIP_MEMORY_SCOPE_AGENT);
    }
}

constexpr int WK_LDS_SH = 0, WK_LDS_WO = 32768, WK_LDS_OUT = 65536;
template <int TYPE>
DI void ab_prologue(const unsigned short* __restrict__ Wq, const unsigned short* __restrict__ Wk, const unsigned short* __restrict__ Wv,
                    const unsigned short* __restrict__ Wo, char* __restrict__ ws, char* lds, const rsrc_t rs, int tile0, int wave, int lane) {
    const int i = lane & 15, g = lane >> 4;
    const int h = (tile0 - 448 * TYPE) / 28, ch = ((tile0 - 448 * TYPE) % 28) >> 2;
    const unsigned short* ms = TYPE == 0 ? Wk : Wv;
    bf16x8 st[4][2][8];
#pragma unroll
    for (int n = 0; n < 4; ++n) {
        const int pn = (((wave + 4 * n < KK) ? wave + 4 * n : wave) + 2 * ch) % KK;
        const unsigned short* p = ms + (size_t)(32 * pn + i) * 4096 + h * 256 + 8 * g;
#pragma unroll
        for (int t = 0; t < 2; ++t)
#pragma unroll
            for (int ke = 0; ke < 8; ++ke) st[n][t][ke] = *(const bf16x8*)(p + (size_t)t * 16 * 4096 + ke * 32);
    }
    u32x4 sh[8];
    if (TYPE == 0) {
        const unsigned short* p = Wq + (size_t)(64 * ch + 16 * wave + i) * 4096 + h * 256 + 8 * g;
#pragma unroll
        for (int ke = 0; ke < 8; ++ke) sh[ke] = *(const u32x4*)(p + ke * 32);
    } else {
#pragma unroll
        for (int n = 0; n < 8; ++n) {
            const int e = threadIdx.x + n * 256, row = e >> 3, c8 = e & 7;
            sh[n] = *(const u32x4*)(Wo + ((size_t)h * 256 + row) * DM + 64 * ch + c8 * 8);
        }
    }
#pragma unroll
    for (int n = 0; n < 8; ++n) asm volatile("" : "+v"(sh[n]));
#pragma unroll
    for (int n = 0; n < 4; ++n)
#pragma unroll
        for (int t = 0; t < 2; ++t)
#pragma unroll
            for (int ke = 0; ke < 8; ++ke) asm volatile("" : "+v"(st[n][t][ke]));
    if (TYPE == 0) {
#pragma unroll
        for (int ke = 0; ke < 8; ++ke) *(u32x4*)(lds + WK_LDS_SH + (wave * 8 + ke) * 1024 + lane * 16) = sh[ke];
    } else {
#pragma unroll
        for (int n = 0; n < 8; ++n) *(u32x4*)(lds + WK_LDS_WO + (threadIdx.x + n * 256) * 16) = sh[n];
        __syncthreads();
        const unsigned short* wo_t = (const unsigned short*)(lds + WK_LDS_WO);
#pragma unroll
        for (int ke = 0; ke < 8; ++ke) {
            bf16x8 v;
#pragma unroll
            for (int j = 0; j < 8; ++j) v[j] = (short)wo_t[(ke * 32 + 8 * g + j) * 64 + 16 * wave + i];
            *(bf16x8*)(lds + WK_LDS_SH + (wave * 8 + ke) * 1024 + lane * 16) = v;
        }
    }
    __syncthreads();
#pragma unroll
    for (int n = 0; n < 4; ++n) {
        if (wave + 4 * n >= KK) break;
        const int pr = (wave + 4 * n + 2 * ch) % KK;
        const bf16x8 (&cur)[2][8] = st[n];
        if (TYPE == 0) {
#pragma unroll
            for (int ap = 0; ap < 2; ++ap) {
                bf16x8 qf[2][8];
#pragma unroll
                for (int a = 0; a < 2; ++a)
#pragma unroll
                    for (int ke = 0; ke < 8; ++ke) qf[a][ke] = *(const bf16x8*)(lds + WK_LDS_SH + ((2 * ap + a) * 8 + ke) * 1024 + lane * 16);
#pragma unroll
                for (int bb = 0; bb < 2; ++bb) {
                    f32x4 d0 = {0.f, 0.f, 0.f, 0.f}, d1 = {0.f, 0.f, 0.f, 0.f};
#pragma unroll
                    for (int ke = 0; ke < 8; ++ke) { d0 = mfma32(qf[0][ke], cur[bb][ke], d0); d1 = mfma32(qf[1][ke], cur[bb][ke], d1); }
                    st8c(rs, OFF_ANF + ((size_t)(h * 28 + 2 * pr + bb) * KK + 2 * ch + ap) * KB + lane * 16, pack8(d0, d1));
                }
#pragma unroll
                for (int a = 0; a < 2; ++a) {
                    f32x4 d0 = {0.f, 0.f, 0.f, 0.f}, d1 = {0.f, 0.f, 0.f, 0.f};
#pragma unroll
                    for (int ke = 0; ke < 8; ++ke) { d0 = mfma32(cur[0][ke], qf[a][ke], d0); d1 = mfma32(cur[1][ke], qf[a][ke], d1); }
                    const bf16x8 v = pack8(d0, d1);
                    *(bf16x8*)(lds + WK_LDS_OUT + ((2 * ap + a) * KK + pr) * 1024 + lane * 16) = v;
                    st8c(rs, OFF_WF + ((size_t)(tile0 + 2 * ap + a) * KK + pr) * KB + lane * 16, v);
                }
            }
        } else {
#pragma unroll
            for (int ct = 0; ct < 4; ++ct) {
                f32x4 d0 = {0.f, 0.f, 0.f, 0.f}, d1 = {0.f, 0.f, 0.f, 0.f};
#pragma unroll
                for (int ke = 0; ke < 8; ++ke) {
                    const bf16x8 fx = *(const bf16x8*)(lds + WK_LDS_SH + (ct * 8 + ke) * 1024 + lane * 16);
                    d0 = mfma32(cur[0][ke], fx, d0); d1 = mfma32(cur[1][ke], fx, d1);
                }
                const bf16x8 v = pack8(d0, d1);
                *(bf16x8*)(lds + WK_LDS_OUT + (ct * KK + pr) * 1024 + lane * 16) = v;
                st8c(rs, OFF_WF + ((size_t)(tile0 + ct) * KK + pr) * KB + lane * 16, v);
            }
        }
    }
    signal_cnt(ws, TYPE == 0 ? CNT_AN + h : CNT_BW + ch, lane);
    __syncthreads();
}

template <int TYPE>
DI void w1_phase(char* __restrict__ ws, char* lds, const rsrc_t rs, int tile0, int wave, int lane) {
    bf16x8 w[4][KK];
#pragma unroll
    for (int t = 0; t < 4; ++t)
#pragma unroll
        for (int kk = 0; kk < KK; ++kk) w[t][kk] = *(const bf16x8*)(lds + WK_LDS_OUT + (t * KK + kk) * 1024 + lane * 16);
    const int h = (tile0 - 448 * TYPE) / 28, t0 = (tile0 - 448 * TYPE) % 28;
    bf16x8 xa[KK], xb[KK];
    {
        const char* xp = ws + OFF_XNF + (size_t)(2 * wave) * KK * KB + lane * 16;
#pragma unroll
        for (int kk = 0; kk < KK; ++kk) xa[kk] = ldfrag(xp + (size_t)kk * KB);
    }
    for (int s = wave; s < NSEG; s += 4) {
        f32x4 acc[2][4];
        {
            const char* xp = ws + OFF_XNF + (size_t)(2 * s + 1) * KK * KB + lane * 16;
#pragma unroll
            for (int kk = 0; kk < KK; ++kk) xb[kk] = ldfrag(xp + (size_t)kk * KB);
        }
        __builtin_amdgcn_sched_barrier(0);
#pragma unroll
        for (int t = 0; t < 4; ++t) { if (TYPE == 0) mfma_Aa0(acc[0][t], w[t][0], xa[0]); else mfma_Ba0(acc[0][t], xa[0], w[t][0]); }
#pragma unroll
        for (int kk = 1; kk < KK; ++kk)
#pragma unroll
            for (int t = 0; t < 4; ++t) { if (TYPE == 0) mfma_Aa(acc[0][t], w[t][kk], xa[kk]); else mfma_Ba(acc[0][t], xa[kk], w[t][kk]); }
        __builtin_amdgcn_sched_barrier(0);
        {
            const int sn = (s + 4 < NSEG) ? s + 4 : s;
            const char* xp = ws + OFF_XNF + (size_t)(2 * sn) * KK * KB + lane * 16;
#pragma unroll
            for (int kk = 0; kk < KK; ++kk) xa[kk] = ldfrag(xp + (size_t)kk * KB);
        }
        __builtin_amdgcn_sched_barrier(0);
#pragma unroll
        for (int t = 0; t < 4; ++t) { if (TYPE == 0) mfma_Aa0(acc[1][t], w[t][0], xb[0]); else mfma_Ba0(acc[1][t], xb[0], w[t][0]); }
#pragma unroll
        for (int kk = 1; kk < KK; ++kk)
#pragma unroll
            for (int t = 0; t < 4; ++t) { if (TYPE == 0) mfma_Aa(acc[1][t], w[t][kk], xb[kk]); else mfma_Ba(acc[1][t], xb[kk], w[t][kk]); }
        mfma_fence4(acc[0][0], acc[0][1], acc[0][2], acc[0][3]);
        mfma_fence4(acc[1][0], acc[1][1], acc[1][2], acc[1][3]);
        if (TYPE == 0) {
#pragma unroll
            for (int q = 0; q < 2; ++q)
#pragma unroll
                for (int p = 0; p < 2; ++p)
                    st8c(rs, OFF_GTF + (((size_t)h * NRT + 2 * s + q) * KK + (t0 >> 1) + p) * KB + lane * 16, pack8(acc[q][2 * p], acc[q][2 * p + 1]));
        } else {
#pragma unroll
            for (int t = 0; t < 4; ++t)
                st8c(rs, OFF_UF + (((size_t)s * NT16 + t0 + t) * NH + h) * KB + lane * 16, pack8(acc[0][t], acc[1][t]));
        }
        signal_cnt(ws, CNT_G + s, lane);
    }
}

DI void ffn_task(char* __restrict__ ws, char* lds, const rsrc_t rs, const unsigned short* __restrict__ x, const unsigned short* __restrict__ b1,
                 const unsigned short* __restrict__ b2, int s, int wave, int lane) {
    const int i = lane & 15, g = lane >> 4;
    {
        bf16x8 xf[2][KK];
        const char* xp = ws + OFF_XNF + (size_t)(2 * s) * KK * KB + lane * 16;
#pragma unroll
        for (int q = 0; q < 2; ++q)
#pragma unroll
            for (int kk = 0; kk < KK; ++kk) xf[q][kk] = ldfrag(xp + (size_t)(q * KK + kk) * KB);
        const char* wp = ws + OFF_WF + (size_t)(896 + 28 * wave) * KK * KB + lane * 16;
        for (int pr = 0; pr < 14; ++pr) {
            bf16x8 wf[2][KK];
#pragma unroll
            for (int t = 0; t < 2; ++t)
#pragma unroll
                for (int kk = 0; kk < KK; ++kk) wf[t][kk] = ldfrag(wp + (size_t)((2 * pr + t) * KK + kk) * KB);
            f32x4 acc[2][2];
#pragma unroll
            for (int q = 0; q < 2; ++q)
#pragma unroll
                for (int t = 0; t < 2; ++t) acc[q][t] = (f32x4){0.f, 0.f, 0.f, 0.f};
#pragma unroll
            for (int kk = 0; kk < KK; ++kk)
#pragma unroll
                for (int q = 0; q < 2; ++q)
#pragma unroll
                    for (int t = 0; t < 2; ++t) acc[q][t] = mfma32(wf[t][kk], xf[q][kk], acc[q][t]);
#pragma unroll
            for (int t = 0; t < 2; ++t) {
                u32x2 bb = *(const u32x2*)(b1 + 16 * (28 * wave + 2 * pr + t) + 4 * g);
                float bv[4] = {bf_lo(bb.x), bf_hi(bb.x), bf_lo(bb.y), bf_hi(bb.y)};
#pragma unroll
                for (int q = 0; q < 2; ++q)
#pragma unroll
                    for (int r = 0; r < 4; ++r) acc[q][t][r] = gelu_tanh(acc[q][t][r] + bv[r]);
            }
#pragma unroll
            for (int q = 0; q < 2; ++q)
                *(bf16x8*)(lds + (size_t)(q * KF + 14 * wave + pr) * KB + lane * 16) = pack8(acc[q][0], acc[q][1]);
        }
    }
    __syncthreads();
    {
        f32x4 acc[2][7];
#pragma unroll
        for (int q = 0; q < 2; ++q)
#pragma unroll
            for (int t = 0; t < 7; ++t) acc[q][t] = (f32x4){0.f, 0.f, 0.f, 0.f};
        const char* wp = ws + OFF_W2F + (size_t)(7 * wave) * KF * KB + lane * 16;
#pragma unroll 4
        for (int kf = 0; kf < KF; ++kf) {
            bf16x8 h0 = *(const bf16x8*)(lds + (size_t)kf * KB + lane * 16), h1 = *(const bf16x8*)(lds + (size_t)(KF + kf) * KB + lane * 16);
#pragma unroll
            for (int t = 0; t < 7; ++t) {
                bf16x8 wf = ldfrag(wp + (size_t)(t * KF + kf) * KB);
                acc[0][t] = mfma32(wf, h0, acc[0][t]);
                acc[1][t] = mfma32(wf, h1, acc[1][t]);
            }
        }
#pragma unroll
        for (int t = 0; t < 7; ++t) {
            const int tc = 7 * wave + t;
            u32x2 bb = *(const u32x2*)(b2 + 16 * tc + 4 * g);
#pragma unroll
            for (int q = 0; q < 2; ++q) {
                const size_t o = (size_t)(32 * s + 16 * q + i) * DM + 16 * tc + 4 * g;
                u32x2 xv = *(const u32x2*)(x + o);
                f32x4 r = {acc[q][t][0] + bf_lo(bb.x) + bf_lo(xv.x), acc[q][t][1] + bf_hi(bb.x) + bf_hi(xv.x), acc[q][t][2] + bf_lo(bb.y) + bf_lo(xv.y), acc[q][t][3] + bf_hi(bb.y) + bf_hi(xv.y)};
                st4c(rs, OFF_FO + o * 4, __builtin_bit_cast(u32x4, r));
            }
        }
        signal_cnt(ws, CNT_F + s, lane);
    }
    __syncthreads();
}

DI void w3_loop(char* __restrict__ ws, const rsrc_t rs, int tile0, int lane) {
    const int i = lane & 15, g = lane >> 4;
    bf16x8 w[4][KK];
    {
        const size_t wp = OFF_WF + (size_t)tile0 * KK * KB + lane * 16;
#pragma unroll
        for (int t = 0; t < 4; ++t)
#pragma unroll
            for (int kk = 0; kk < KK; ++kk) w[t][kk] = ld8c(rs, wp + (size_t)(t * KK + kk) * KB);
    }
    const bool isA = tile0 < 448;
    const int h = (isA ? tile0 : tile0 - 448) / 28, t0 = (isA ? tile0 : tile0 - 448) % 28;
    for (int sp = 0; sp < NSEG / 2; ++sp) {
        bf16x8 xf[KK];
        wait_cnt_slow(ws, CNT_PROG, (unsigned)(sp == NSEG / 2 - 1 ? 2 * sp : 2 * sp + 1));
        {
            const size_t mp = OFF_MEMF + (size_t)(2 * sp + (i >> 3)) * 7168 + (i & 7) * 32 + g * 8;
            unsigned spins = 0;
            for (;;) {
                asm volatile("" ::: "memory");
                unsigned mx = 0;
#pragma unroll
                for (int kk = 0; kk < KK; ++kk) {
                    u32x2 lo_ = ld2c(rs, mp + kk * 512), hi_ = ld2c(rs, mp + kk * 512 + 256);
                    u32x4 xx_ = {lo_.x, lo_.y, hi_.x, hi_.y};
                    mx = max(mx, umax4(xx_));
                    xf[kk] = __builtin_bit_cast(bf16x8, xx_);
                }
                if (__all(mx != SENT)) break;
                __builtin_amdgcn_s_sleep(16);
                if (spin_fail(spins, (unsigned*)(ws + OFF_TMO), 0x500u + sp)) break;
            }
        }
        f32x4 acc[4];
        if (isA) {
#pragma unroll
            for (int t = 0; t < 4; ++t) mfma_Aa0(acc[t], w[t][0], xf[0]);
#pragma unroll
            for (int kk = 1; kk < KK; ++kk)
#pragma unroll
                for (int t = 0; t < 4; ++t) mfma_Aa(acc[t], w[t][kk], xf[kk]);
            mfma_fence4(acc[0], acc[1], acc[2], acc[3]);
#pragma unroll
            for (int p = 0; p < 2; ++p)
                st8c(rs, OFF_T2F + ((size_t)h * NSEG + 2 * sp + (i >> 3)) * 7168 + ((t0 >> 1) + p) * 512 + g * 128 + (i & 7) * 16, pack8(acc[2 * p], acc[2 * p + 1]));
        } else {
#pragma unroll
            for (int t = 0; t < 4; ++t) mfma_Ba0(acc[t], xf[0], w[t][0]);
#pragma unroll
            for (int kk = 1; kk < KK; ++kk)
#pragma unroll
                for (int t = 0; t < 4; ++t) mfma_Ba(acc[t], xf[kk], w[t][kk]);
            mfma_fence4(acc[0], acc[1], acc[2], acc[3]);
#pragma unroll
            for (int t = 0; t < 4; ++t)
                st2c(rs, OFF_YF + (((size_t)(2 * sp + (g >> 1)) * NT16 + t0 + t) * 8 + (h >> 1)) * 512 + (g & 1) * 256 + i * 16 + (h & 1) * 8, __builtin_bit_cast(u32x2, pack4(acc[t])));
        }
        signal_cnt(ws, isA ? CNT_T2 + sp * 16 + h : CNT_Y + sp * 7 + (t0 >> 2), lane);
    }
}

DI void f1_task(char* __restrict__ ws, const rsrc_t rs, int s, int h, int lane) {
    const int i = lane & 15, g = lane >> 4;
    f32x4 sc[2][3];
#pragma unroll
    for (int q = 0; q < 2; ++q)
#pragma unroll
        for (int k = 0; k < 3; ++k) sc[q][k] = (f32x4){0.f, 0.f, 0.f, 0.f};
    const char* xp = ws + OFF_XNF + (size_t)(2 * s) * KK * KB + lane * 16;
    const size_t gp = OFF_GTF + ((size_t)h * NRT + 2 * s) * KK * KB + lane * 16;
    const size_t tp = OFF_T2F + ((size_t)h * NSEG + s) * 7168 + g * 128 + (i & 7) * 16;
    bf16x8 xf[2][KK];
    {
        wait_cnt(ws, CNT_G + s, 224);
        bf16x8 g0[KK], g1[KK];
#pragma unroll
        for (int kk = 0; kk < KK; ++kk) { g0[kk] = ld8c(rs, gp + (size_t)kk * KB); g1[kk] = ld8c(rs, gp + (size_t)(KK + kk) * KB); }
#pragma unroll
        for (int q = 0; q < 2; ++q)
#pragma unroll
            for (int kk = 0; kk < KK; ++kk) xf[q][kk] = ldfrag(xp + (size_t)(q * KK + kk) * KB);
#pragma unroll
        for (int kk = 0; kk < KK; ++kk)
#pragma unroll
            for (int q = 0; q < 2; ++q) {
                sc[q][0] = mfma32(g0[kk], xf[q][kk], sc[q][0]);
                sc[q][1] = mfma32(g1[kk], xf[q][kk], sc[q][1]);
            }
    }
    wait_cnt_slow(ws, CNT_PROG, (unsigned)(s | 1));
    wait_cnt(ws, CNT_T2 + (s >> 1) * 16 + h, 7);
    {
        bf16x8 t2[KK];
#pragma unroll
        for (int kk = 0; kk < KK; ++kk) t2[kk] = ld8c(rs, tp + kk * 512);
#pragma unroll
        for (int kk = 0; kk < KK; ++kk)
#pragma unroll
            for (int q = 0; q < 2; ++q) sc[q][2] = mfma32(t2[kk], xf[q][kk], sc[q][2]);
    }
    const float cs = 0.0625f * 1.4426950408889634f;
    const float NEG = -3.0e38f;
#pragma unroll
    for (int q = 0; q < 2; ++q) {
        const int qi = 16 * q + i;
        float xs[8], ms[4];
#pragma unroll
        for (int j = 0; j < 8; ++j) {
            const int key = 16 * (j >> 2) + 4 * g + (j & 3);
            xs[j] = (key <= qi) ? sc[q][j >> 2][j & 3] : NEG;
        }
#pragma unroll
        for (int j = 0; j < 4; ++j) ms[j] = (g < 2) ? sc[q][2][j] : NEG;
        float m = fmaxf(fmaxf(fmaxf(xs[0], xs[1]), fmaxf(xs[2], xs[3])), fmaxf(fmaxf(xs[4], xs[5]), fmaxf(xs[6], xs[7])));
        m = fmaxf(m, fmaxf(fmaxf(ms[0], ms[1]), fmaxf(ms[2], ms[3])));
        m = xmax16(m); m = xmax32(m);
        const float mc = m * cs;
        float sum = 0.f;
#pragma unroll
        for (int j = 0; j < 8; ++j) { const int key = 16 * (j >> 2) + 4 * g + (j & 3); xs[j] = (key <= qi) ? fexp2(xs[j] * cs - mc) : 0.f; sum += xs[j]; }
#pragma unroll
        for (int j = 0; j < 4; ++j) { ms[j] = (g < 2) ? fexp2(ms[j] * cs - mc) : 0.f; sum += ms[j]; }
        sum = xadd16(sum); sum = xadd32(sum);
        const float inv = 1.f / sum;
        u32x4 px = {pack2(xs[0] * inv, xs[1] * inv), pack2(xs[2] * inv, xs[3] * inv), pack2(xs[4] * inv, xs[5] * inv), pack2(xs[6] * inv, xs[7] * inv)};
        st4c(rs, OFF_PXF + (((size_t)s * 2 + q) * NH + h) * KB + lane * 16, px);
        u32x2 pmv = {pack2(ms[0] * inv, ms[1] * inv), pack2(ms[2] * inv, ms[3] * inv)};
        st2c(rs, OFF_PMF + (((size_t)s * 2 + q) * 8 + (h >> 1)) * KB + lane * 16 + (h & 1) * 8, pmv);
    }
    signal_cnt(ws, CNT_P + s, lane);
}

DI void f2_task(char* __restrict__ ws, const rsrc_t rs, unsigned short* __restrict__ out, int s, int tp2, int lane) {
    const int i = lane & 15, g = lane >> 4;
    wait_cnt(ws, CNT_G + s, 224);
    wait_cnt(ws, CNT_F + s, 4);
    f32x4 acc[2][2];
    bf16x8 ua[2][NH];
    {
        const size_t up = OFF_UF + ((size_t)s * NT16 + 2 * tp2) * NH * KB + lane * 16;
#pragma unroll
        for (int t = 0; t < 2; ++t)
#pragma unroll
            for (int h = 0; h < NH; ++h) ua[t][h] = ld8c(rs, up + (size_t)(t * NH + h) * KB);
    }
#pragma unroll
    for (int q = 0; q < 2; ++q)
#pragma unroll
        for (int t = 0; t < 2; ++t)
            acc[q][t] = __builtin_bit_cast(f32x4, ld4c(rs, OFF_FO + ((size_t)(32 * s + 16 * q + i) * DM + 16 * (2 * tp2 + t) + 4 * g) * 4));
    wait_cnt_slow(ws, CNT_PROG, (unsigned)(s | 1));
    wait_cnt(ws, CNT_Y + (s >> 1) * 7 + (tp2 >> 1), 16);
    wait_cnt(ws, CNT_P + s, 16);
    {
        const size_t pp = OFF_PXF + (size_t)s * 2 * NH * KB + lane * 16;
        bf16x8 p0[NH], p1[NH];
#pragma unroll
        for (int h = 0; h < NH; ++h) { p0[h] = ld8c(rs, pp + (size_t)h * KB); p1[h] = ld8c(rs, pp + (size_t)(NH + h) * KB); }
        const size_t yp = OFF_YF + ((size_t)s * NT16 + 2 * tp2) * 8 * 512 + (g & 1) * 256 + i * 16;
        const size_t pq = OFF_PMF + (size_t)s * 2 * 8 * KB + lane * 16;
        bf16x8 ya[2][8], m0[8], m1[8];
#pragma unroll
        for (int hp = 0; hp < 8; ++hp) {
            m0[hp] = ld8c(rs, pq + (size_t)hp * KB); m1[hp] = ld8c(rs, pq + (size_t)(8 + hp) * KB);
            ya[0][hp] = ld8c(rs, yp + (size_t)hp * 512); ya[1][hp] = ld8c(rs, yp + (size_t)(8 + hp) * 512);
        }
#pragma unroll
        for (int h = 0; h < NH; ++h)
#pragma unroll
            for (int t = 0; t < 2; ++t) {
                acc[0][t] = mfma32(ua[t][h], p0[h], acc[0][t]);
                acc[1][t] = mfma32(ua[t][h], p1[h], acc[1][t]);
            }
#pragma unroll
        for (int hp = 0; hp < 8; ++hp)
#pragma unroll
            for (int t = 0; t < 2; ++t) {
                acc[0][t] = mfma32(ya[t][hp], m0[hp], acc[0][t]);
                acc[1][t] = mfma32(ya[t][hp], m1[hp], acc[1][t]);
            }
    }
#pragma unroll
    for (int q = 0; q < 2; ++q)
#pragma unroll
        for (int t = 0; t < 2; ++t) {
            const size_t o = (size_t)(32 * s + 16 * q + i) * DM + 16 * (2 * tp2 + t) + 4 * g;
            u32x2 r = {pack2(acc[q][t][0], acc[q][t][1]), pack2(acc[q][t][2], acc[q][t][3])};
            *(u32x2*)(out + o) = r;
        }
}

__global__ __launch_bounds__(256, 1) void k_mega(const unsigned short* __restrict__ x, const unsigned short* __restrict__ b1, const unsigned short* __restrict__ b2,
                                                 const unsigned short* __restrict__ Wq, const unsigned short* __restrict__ Wk,
                                                 const unsigned short* __restrict__ Wv, const unsigned short* __restrict__ Wo,
                                                 char* __restrict__ ws, unsigned short* __restrict__ out) {
    extern __shared__ __attribute__((aligned(16))) char lds[];
    const rsrc_t rs = __builtin_amdgcn_make_buffer_rsrc(ws, 0, (int)WS_END, 0x00020000);
    if ((blockIdx.x & 7) == 0) { chain_role(ws, lds, rs); return; }
    const int wid = (blockIdx.x >> 3) * 7 + (blockIdx.x & 7) - 1;
    const int wave = __builtin_amdgcn_readfirstlane(threadIdx.x >> 6), lane = threadIdx.x & 63;
    const int un = ((blockIdx.x & 7) - 1) * 32 + (blockIdx.x >> 3);
    const int tile0 = ((un % 14) / 7) * 448 + (un / 14) * 28 + (un % 7) * 4;
    if (tile0 < 448) { ab_prologue<0>(Wq, Wk, Wv, Wo, ws, lds, rs, tile0, wave, lane); w1_phase<0>(ws, lds, rs, tile0, wave, lane); }
    else { ab_prologue<1>(Wq, Wk, Wv, Wo, ws, lds, rs, tile0, wave, lane); w1_phase<1>(ws, lds, rs, tile0, wave, lane); }
    if (wid >= 160) ffn_task(ws, lds, rs, x, b1, b2, wid - 160, wave, lane);
    if (wave == 0) { w3_loop(ws, rs, tile0, lane); return; }
    const int fw = wid * 3 + wave - 1;
    for (int b = 0; b < 32; ++b) {
        {
            const int k = (fw + 672 * 2 - b * 32) % 672;
            if (k < 32) f1_task(ws, rs, 2 * b + (k >> 4), k & 15, lane);
        }
        {
            const int k = (fw + 672 * 2 - 336 - b * 28) % 672;
            if (k < 28) f2_task(ws, rs, out, 2 * b + (k / 14), k % 14, lane);
        }
    }
}

extern "C" void kernel_launch(void* const* d_in, const int* in_sizes, int n_in,
                              void* d_out, int out_size, void* d_ws, size_t ws_size,
                              hipStream_t stream) {
    const unsigned short* x = (const unsigned short*)d_in[0];
    const unsigned short* mem0 = (const unsigned short*)d_in[1];
    const unsigned short* Wq = (const unsigned short*)d_in[2];
    const unsigned short* Wk = (const unsigned short*)d_in[3];
    const unsigned short* Wv = (const unsigned short*)d_in[4];
    const unsigned short* Wo = (const unsigned short*)d_in[5];
    const unsigned short* w1 = (const unsigned short*)d_in[8];
    const unsigned short* b1 = (const unsigned short*)d_in[9];
    const unsigned short* w2 = (const unsigned short*)d_in[10];
    const unsigned short* b2 = (const unsigned short*)d_in[11];
    char* ws = (char*)d_ws;
    unsigned short* out = (unsigned short*)d_out;
    (void)hipFuncSetAttribute((const void*)k_mega, hipFuncAttributeMaxDynamicSharedMemorySize, CH_LDS_SIZE);
    k_prep<<<PREP_GRID, PREP_NT, 0, stream>>>(x, mem0, w1, w2, ws);
    k_mega<<<256, 256, CH_LDS_SIZE, stream>>>(x, b1, b2, Wq, Wk, Wv, Wo, ws, out);
}
```

```cpp
#include <hip/hip_runtime.h>
#include <hip/hip_bf16.h>

typedef __attribute__((ext_vector_type(8))) short bf16x8;
typedef __attribute__((ext_vector_type(4))) short bf16x4;
typedef __attribute__((ext_vector_type(4))) float f32x4;
typedef __attribute__((ext_vector_type(4))) unsigned u32x4;
typedef __attribute__((ext_vector_type(2))) unsigned u32x2;
typedef __attribute__((ext_vector_type(2))) __bf16 bf2_t;
#define DI __device__ __forceinline__

constexpr int DM = 448, KK = 14, NT16 = 28, NH = 16, NSEG = 64, NRT = 128, FFD = 1792, KF = 56;
constexpr size_t KB = 1024;
constexpr size_t OFF_XNF = 0;
constexpr size_t OFF_WF = OFF_XNF + (size_t)NRT * KK * KB;
constexpr size_t OFF_ANF = OFF_WF + (size_t)1008 * KK * KB;
constexpr size_t OFF_W2F = OFF_ANF + (size_t)448 * KK * KB;
constexpr size_t OFF_GTF = OFF_W2F + (size_t)28 * KF * KB;
constexpr size_t OFF_UF = OFF_GTF + (size_t)NH * NRT * KK * KB;
constexpr size_t OFF_FO = OFF_UF + (size_t)NSEG * NT16 * NH * KB;
constexpr size_t OFF_MEMF = OFF_FO + (size_t)2048 * DM * 4;
constexpr size_t OFF_SMX = OFF_MEMF + (size_t)NSEG * 7168;
constexpr int E2B = 640, E2S = 32 * E2B;
constexpr size_t OFF_T2F = OFF_SMX + (size_t)NSEG * E2S;
constexpr size_t OFF_YF = OFF_T2F + (size_t)NH * NSEG * 7168;
constexpr size_t OFF_PXF = OFF_YF + (size_t)NSEG * NT16 * 8 * 512;
constexpr size_t OFF_PMF = OFF_PXF + (size_t)NSEG * 2 * NH * KB;
constexpr size_t OFF_CNT = OFF_PMF + (size_t)NSEG * 2 * 8 * KB;
constexpr int CNT_G = 0, CNT_P = 64, CNT_F = 128, CNT_TMO = 192, CNT_PROG = 193, CNT_T2 = 200, CNT_Y = CNT_T2 + 32 * 16, CNT_AN = CNT_Y + 32 * 7, CNT_BW = CNT_AN + 16, CNT_N = CNT_BW + 8;
constexpr size_t OFF_TMO = OFF_CNT + (size_t)CNT_TMO * 128;
constexpr size_t WS_END = OFF_CNT + (size_t)CNT_N * 128;
constexpr size_t OFF_DBG = WS_END;

constexpr unsigned SENT = 0xFFFFFFFFu;
constexpr unsigned kSpinCap = 1u << 16;

DI unsigned pack2(float a, float b) { bf2_t v = {(__bf16)a, (__bf16)b}; return __builtin_bit_cast(unsigned, v); }
DI float bf_lo(unsigned u) { return __uint_as_float(u << 16); }
DI float bf_hi(unsigned u) { return __uint_as_float(u & 0xFFFF0000u); }
DI f32x4 mfma32(bf16x8 a, bf16x8 b, f32x4 c) { return __builtin_amdgcn_mfma_f32_16x16x32_bf16(a, b, c, 0, 0, 0); }
DI bf16x8 ldfrag(const char* p) { return *(const bf16x8*)p; }
DI bf16x8 pack8(f32x4 a, f32x4 b) {
    u32x4 r = {pack2(a[0], a[1]), pack2(a[2], a[3]), pack2(b[0], b[1]), pack2(b[2], b[3])};
    return __builtin_bit_cast(bf16x8, r);
}
DI bf16x4 pack4(f32x4 a) { u32x2 r = {pack2(a[0], a[1]), pack2(a[2], a[3])}; return __builtin_bit_cast(bf16x4, r); }
DI bool spin_fail(unsigned& spins, unsigned* tmo, unsigned code = 1u) {
    if (++spins > kSpinCap) { unsigned exp_ = 0u; __hip_atomic_compare_exchange_strong(tmo, &exp_, code, __ATOMIC_RELAXED, __ATOMIC_RELAXED, __HIP_MEMORY_SCOPE_AGENT); return true; }
    if ((spins & 63u) == 0u) return __builtin_amdgcn_readfirstlane(__hip_atomic_load(tmo, __ATOMIC_RELAXED, __HIP_MEMORY_SCOPE_AGENT)) != 0u;
    return false;
}
typedef __amdgpu_buffer_rsrc_t rsrc_t;
DI bf16x8 ld8c(rsrc_t rs, size_t off) { return __builtin_bit_cast(bf16x8, __builtin_amdgcn_raw_buffer_load_b128(rs, (unsigned)off, 0, 16)); }
DI u32x4 ld4c(rsrc_t rs, size_t off) { return __builtin_amdgcn_raw_buffer_load_b128(rs, (unsigned)off, 0, 16); }
DI u32x2 ld2c(rsrc_t rs, size_t off) { return __builtin_amdgcn_raw_buffer_load_b64(rs, (unsigned)off, 0, 16); }
DI unsigned ld1c(rsrc_t rs, size_t off) { return __builtin_amdgcn_raw_buffer_load_b32(rs, (unsigned)off, 0, 16); }
DI void st8c(rsrc_t rs, size_t off, bf16x8 v) { __builtin_amdgcn_raw_buffer_store_b128(__builtin_bit_cast(u32x4, v), rs, (unsigned)off, 0, 16); }
DI void st4c(rsrc_t rs, size_t off, u32x4 v) { __builtin_amdgcn_raw_buffer_store_b128(v, rs, (unsigned)off, 0, 16); }
DI void st2c(rsrc_t rs, size_t off, u32x2 v) { __builtin_amdgcn_raw_buffer_store_b64(v, rs, (unsigned)off, 0, 16); }
DI void signal_cnt(char* ws, int idx, int lane) {
    asm volatile("s_waitcnt vmcnt(0)" ::: "memory");
    if (lane == 0) __hip_atomic_fetch_add((unsigned*)(ws + OFF_CNT + (size_t)idx * 128), 1u, __ATOMIC_RELAXED, __HIP_MEMORY_SCOPE_AGENT);
}
DI void wait_cnt(char* ws, int idx, unsigned target) {
    unsigned spins = 0;
    unsigned* p = (unsigned*)(ws + OFF_CNT + (size_t)idx * 128);
    for (;;) {
        unsigned v = __builtin_amdgcn_readfirstlane(__hip_atomic_load(p, __ATOMIC_RELAXED, __HIP_MEMORY_SCOPE_AGENT));
        if (v >= target) break;
        __builtin_amdgcn_s_sleep(8);
        if (spin_fail(spins, (unsigned*)(ws + OFF_TMO), 0x300u + idx)) break;
    }
    asm volatile("" ::: "memory");
}

DI void wait_cnt_slow(char* ws, int idx, unsigned target) {
    unsigned spins = 0;
    unsigned* p = (unsigned*)(ws + OFF_CNT + (size_t)idx * 128);
    for (;;) {
        unsigned v = __builtin_amdgcn_readfirstlane(__hip_atomic_load(p, __ATOMIC_RELAXED, __HIP_MEMORY_SCOPE_AGENT));
        if (v >= target) break;
        if (v + 1 < target) __builtin_amdgcn_s_sleep(100); else __builtin_amdgcn_s_sleep(12);
        if (spin_fail(spins, (unsigned*)(ws + OFF_TMO), 0x400u + idx)) break;
    }
    asm volatile("" ::: "memory");
}

DI float gelu_tanh(float z) {
    float u = 0.7978845608028654f * (z + 0.044715f * z * z * z);
    float e = __expf(2.f * u);
    float t = 1.f - 2.f / (1.f + e);
    return 0.5f * z * (1.f + t);
}

DI float xmax16(float v) { auto r = __builtin_amdgcn_permlane16_swap(__float_as_uint(v), __float_as_uint(v), false, false); return fmaxf(__uint_as_float(r[0]), __uint_as_float(r[1])); }
DI float xmax32(float v) { auto r = __builtin_amdgcn_permlane32_swap(__float_as_uint(v), __float_as_uint(v), false, false); return fmaxf(__uint_as_float(r[0]), __uint_as_float(r[1])); }
DI float xadd16(float v) { auto r = __builtin_amdgcn_permlane16_swap(__float_as_uint(v), __float_as_uint(v), false, false); return __uint_as_float(r[0]) + __uint_as_float(r[1]); }
DI float xadd32(float v) { auto r = __builtin_amdgcn_permlane32_swap(__float_as_uint(v), __float_as_uint(v), false, false); return __uint_as_float(r[0]) + __uint_as_float(r[1]); }
DI float qswap(float v) { return __uint_as_float(__builtin_amdgcn_update_dpp(0, __float_as_uint(v), 0xB1, 0xF, 0xF, true)); }
DI unsigned umax4(u32x4 v) { return max(max(v.x, v.y), max(v.z, v.w)); }
DI float fexp2(float x) { return __builtin_amdgcn_exp2f(x); }

DI void mfma_Aa0(f32x4& acc, const bf16x8& w, const bf16x8& m) { asm volatile("s_nop 1\n\tv_mfma_f32_16x16x32_bf16 %0, %1, %2, 0" : "=&v"(acc) : "a"(w), "v"(m)); }
DI void mfma_Aa(f32x4& acc, const bf16x8& w, const bf16x8& m) { asm volatile("s_nop 1\n\tv_mfma_f32_16x16x32_bf16 %0, %1, %2, %0" : "+v"(acc) : "a"(w), "v"(m)); }
DI void mfma_Ba0(f32x4& acc, const bf16x8& m, const bf16x8& w) { asm volatile("s_nop 1\n\tv_mfma_f32_16x16x32_bf16 %0, %1, %2, 0" : "=&v"(acc) : "v"(m), "a"(w)); }
DI void mfma_Ba(f32x4& acc, const bf16x8& m, const bf16x8& w) { asm volatile("s_nop 1\n\tv_mfma_f32_16x16x32_bf16 %0, %1, %2, %0" : "+v"(acc) : "v"(m), "a"(w)); }
DI void mfma_v0(f32x4& acc, const bf16x8& a, const bf16x8& b) { asm volatile("s_nop 1\n\tv_mfma_f32_16x16x32_bf16 %0, %1, %2, 0" : "=&v"(acc) : "v"(a), "v"(b)); }
DI void mfma_v(f32x4& acc, const bf16x8& a, const bf16x8& b) { asm volatile("s_nop 1\n\tv_mfma_f32_16x16x32_bf16 %0, %1, %2, %0" : "+v"(acc) : "v"(a), "v"(b)); }
DI void mfma16_v0(f32x4& acc, const bf16x4& a, const bf16x4& b) { asm volatile("s_nop 1\n\tv_mfma_f32_16x16x16_bf16 %0, %1, %2, 0" : "=&v"(acc) : "v"(a), "v"(b)); }
DI void mfma16_v(f32x4& acc, const bf16x4& a, const bf16x4& b) { asm volatile("s_nop 1\n\tv_mfma_f32_16x16x16_bf16 %0, %1, %2, %0" : "+v"(acc) : "v"(a), "v"(b)); }
DI void mfma_fence4(f32x4& a, f32x4& b, f32x4& c, f32x4& d) { asm volatile("s_nop 7\n\ts_nop 1" : "+v"(a), "+v"(b), "+v"(c), "+v"(d)); }
DI void pin(f32x4& a) { asm volatile("" : "+v"(a)); }
DI void mfma_fence1(f32x4& a) { asm volatile("s_nop 7\n\ts_nop 1" : "+v"(a)); }

constexpr int PREP_GRID = 256, PREP_NT = 448;

DI void ln_row(const unsigned short* __restrict__ x, char* __restrict__ ws, int row, int lane, const u32x2 (&u)[2]) {
    float v[2][4];
#pragma unroll
    for (int it = 0; it < 2; ++it) { v[it][0] = bf_lo(u[it].x); v[it][1] = bf_hi(u[it].x); v[it][2] = bf_lo(u[it].y); v[it][3] = bf_hi(u[it].y); }
    float sum = 0.f;
#pragma unroll
    for (int it = 0; it < 2; ++it) sum += v[it][0] + v[it][1] + v[it][2] + v[it][3];
#pragma unroll
    for (int o = 32; o >= 1; o >>= 1) sum += __shfl_xor(sum, o);
    const float mean = sum * (1.f / DM);
    float sq = 0.f;
#pragma unroll
    for (int it = 0; it < 2; ++it) {
        if (lane + it * 64 < 112) {
#pragma unroll
            for (int j = 0; j < 4; ++j) { float d = v[it][j] - mean; sq += d * d; }
        }
    }
#pragma unroll
    for (int o = 32; o >= 1; o >>= 1) sq += __shfl_xor(sq, o);
    const float rstd = 1.f / sqrtf(sq * (1.f / DM) + 1e-5f);
    const int rt = row >> 4, ii = row & 15;
#pragma unroll
    for (int it = 0; it < 2; ++it) {
        int grp = lane + it * 64;
        if (grp < 112) {
            int d = grp * 4, kk = d >> 5, dd = d & 31, half = dd >> 4, gg = (dd & 15) >> 2;
            u32x2 o = {pack2((v[it][0] - mean) * rstd, (v[it][1] - mean) * rstd), pack2((v[it][2] - mean) * rstd, (v[it][3] - mean) * rstd)};
            *(u32x2*)(ws + OFF_XNF + ((size_t)(rt * KK + kk) * 64 + (ii + 16 * gg)) * 16 + half * 8) = o;
        }
    }
}

DI bf16x8 w12_load(const unsigned short* __restrict__ w1, const unsigned short* __restrict__ w2, int f, int i, int g) {
    bf16x8 v;
    if (f < 112 * KK) {
        int tf = f / KK, kk = f % KK;
#pragma unroll
        for (int j = 0; j < 8; ++j) v[j] = (short)w1[(size_t)(32 * kk + 16 * (j >> 2) + 4 * g + (j & 3)) * FFD + 16 * tf + i];
    } else {
        int f2 = f - 112 * KK;
        int t = f2 / KF, kf = f2 % KF;
#pragma unroll
        for (int j = 0; j < 8; ++j) v[j] = (short)w2[(size_t)(32 * kf + 16 * (j >> 2) + 4 * g + (j & 3)) * DM + 16 * t + i];
    }
    return v;
}
DI void w12_store(char* __restrict__ ws, int f, int lane, bf16x8 v) {
    if (f < 112 * KK) *(bf16x8*)(ws + OFF_WF + ((size_t)896 * KK + f) * KB + lane * 16) = v;
    else *(bf16x8*)(ws + OFF_W2F + (size_t)(f - 112 * KK) * KB + lane * 16) = v;
}

__global__ __launch_bounds__(PREP_NT) void k_prep(const unsigned short* __restrict__ x, const unsigned short* __restrict__ mem0,
                                              const unsigned short* __restrict__ w1, const unsigned short* __restrict__ w2, char* __restrict__ ws) {
    const int wave = __builtin_amdgcn_readfirstlane(threadIdx.x >> 6), lane = threadIdx.x & 63;
    const int i = lane & 15, g = lane >> 4;
    const int b = blockIdx.x;
    const int gw = b * 7 + wave;
    u32x2 xr[2][2];
#pragma unroll
    for (int n = 0; n < 2; ++n) {
        const int row = gw + n * 1792;
#pragma unroll
        for (int it = 0; it < 2; ++it) {
            const int grp = lane + it * 64;
            xr[n][it] = (u32x2){0u, 0u};
            if (row < 2048 && grp < 112) xr[n][it] = *(const u32x2*)(x + (size_t)row * DM + grp * 4);
        }
    }
    bf16x8 wv[2];
#pragma unroll
    for (int n = 0; n < 2; ++n) { const int f = gw + n * 1792; wv[n] = w12_load(w1, w2, f < 3136 ? f : 0, i, g); }
    {
        const size_t n16 = ((size_t)(NSEG - 1) * 7168 + (size_t)NSEG * E2S) / 16;
        u32x4 sv = {SENT, SENT, SENT, SENT};
        for (size_t idx = (size_t)b * PREP_NT + threadIdx.x; idx < n16; idx += (size_t)PREP_GRID * PREP_NT)
            *(u32x4*)(ws + OFF_MEMF + 7168 + idx * 16) = sv;
    }
    if (b == 255) {
        for (int e = threadIdx.x; e < 8 * 112; e += PREP_NT) {
            int r = e / 112, grp = e % 112;
            int d = grp * 4, t = d >> 4, gg = (d & 15) >> 2;
            u32x2 u = *(const u32x2*)(mem0 + r * DM + d);
            *(u32x2*)(ws + OFF_MEMF + t * 256 + r * 32 + gg * 8) = u;
        }
    }
    if (b == 254) {
        for (int e = threadIdx.x; e < CNT_N; e += PREP_NT) *(unsigned*)(ws + OFF_CNT + (size_t)e * 128) = 0u;
    }
    ln_row(x, ws, gw, lane, xr[0]);
    if (gw + 1792 < 2048) ln_row(x, ws, gw + 1792, lane, xr[1]);
    w12_store(ws, gw, lane, wv[0]);
    if (gw + 1792 < 3136) w12_store(ws, gw + 1792, lane, wv[1]);
}

constexpr int CH_LDS_B = 0;
constexpr int CH_LDS_MEM = 112 * 1024;
constexpr int CH_LDS_RA = CH_LDS_MEM + 7168;
constexpr int CH_LDS_RB = CH_LDS_RA + 8192;
constexpr int CH_LDS_PW = CH_LDS_RB + 4096;
constexpr int CH_LDS_SIZE = CH_LDS_PW + 4096;

#define E2POS 5
#define DNB 8
#define ADMAX 8
#define D1 8
#define D2 0

template <bool B> struct BoolC { static constexpr bool value = B; };
DI void chain_role(char* __restrict__ ws, char* lds, const rsrc_t rs) {
    const int c = blockIdx.x >> 3;
    const int tid = threadIdx.x, wave = __builtin_amdgcn_readfirstlane(tid >> 6), lane = tid & 63;
    const int i = lane & 15, g = lane >> 4;
    const int h = c >> 1, half = c & 1;
    const bool hasB = c < NT16;
    const int tB = hasB ? c : 0;
    unsigned* tmo = (unsigned*)(ws + OFF_TMO);
    const float cs = 0.0625f * 1.4426950408889634f;

    bf16x8 wa[3][KK], wsh[7], wb[2][KK];
    {
        wait_cnt(ws, CNT_AN + h, 28);
        const size_t ap = OFF_ANF + (size_t)(h * 28 + 14 * half) * KK * KB + lane * 16;
#pragma unroll
        for (int t = 0; t < 3; ++t)
#pragma unroll
            for (int kk = 0; kk < KK; ++kk) wa[t][kk] = ld8c(rs, ap + (size_t)((3 * wave + t) * KK + kk) * KB);
#pragma unroll
        for (int k = 0; k < 7; ++k) wsh[k] = ld8c(rs, ap + (size_t)((12 + (wave >> 1)) * KK + 7 * (wave & 1) + k) * KB);
    }
    const int gk0 = wave < 2 ? 4 * wave : 8 + 3 * (wave - 2);
    const int gkn = wave < 2 ? 4 : 3;
    bf16x8 gf[4], uf[4];
    wait_cnt(ws, CNT_G + 0, 224);
    __syncthreads();
    {
        const size_t gp = OFF_GTF + (((size_t)h * NRT + half) * KK + gk0) * KB + lane * 16;
#pragma unroll
        for (int k = 0; k < 4; ++k) gf[k] = ld8c(rs, gp + (size_t)(k < gkn ? k : 0) * KB);
    }
    const char* mp = lds + CH_LDS_MEM + g * 128 + (i & 7) * 16;
    const int hh2 = lane >> 4, r2 = (lane >> 1) & 7, q2 = lane & 1;

    int d1v = hasB ? D1 : DNB, d2v = D2;
    auto step = [&](const int s, auto FIRST) __attribute__((always_inline)) {
        {
            u32x4 x0, x1;
            unsigned spins = 0;
            const size_t o0 = OFF_MEMF + (size_t)(s * 7168 + tid * 16), o1 = o0 + (tid < 192 ? 4096 : 0);
            const size_t oc = OFF_CNT + (size_t)(CNT_G + s + 1) * 128;
            if (s > 0) for (int d = 0; d < d1v; ++d) __builtin_amdgcn_s_sleep(1);
            for (;;) {
                asm volatile("" ::: "memory");
                x0 = ld4c(rs, o0);
                x1 = ld4c(rs, o1);
                unsigned cv = ld1c(rs, oc);
                if (__all((max(umax4(x0), umax4(x1)) != SENT) && (cv >= 224u))) break;
                if (spin_fail(spins, tmo, 0x100u + s)) break;
            }
            if (hasB) { if (spins != 0u && s > 0 && d1v < D1 + ADMAX) d1v += 2; }
            else { if (spins == 0u) { if (d1v > 8) d1v -= 1; } else if (spins > 1u) d1v += 4; }
            const int t0 = tid >> 4, rr = (tid >> 1) & 7, gp = tid & 1;
            char* d0 = lds + CH_LDS_MEM + (t0 >> 1) * 512 + gp * 256 + rr * 16 + (t0 & 1) * 8;
            *(u32x2*)(d0) = (u32x2){x0.x, x0.y};
            *(u32x2*)(d0 + 128) = (u32x2){x0.z, x0.w};
            if (tid < 192) {
                *(u32x2*)(d0 + 8 * 512) = (u32x2){x1.x, x1.y};
                *(u32x2*)(d0 + 8 * 512 + 128) = (u32x2){x1.z, x1.w};
            }
        }
        {
            const size_t up = OFF_UF + (((size_t)s * NT16 + tB) * NH + 4 * wave) * KB + lane * 16;
#pragma unroll
            for (int k = 0; k < 4; ++k) uf[k] = ld8c(rs, up + (size_t)k * KB);
        }
        __syncthreads();
        if (c == 31 && tid == 255) __hip_atomic_store((unsigned*)(ws + OFF_CNT + (size_t)CNT_PROG * 128), (unsigned)s, __ATOMIC_RELAXED, __HIP_MEMORY_SCOPE_AGENT);
        bf16x8 mf[KK];
#pragma unroll
        for (int kk = 0; kk < KK; ++kk) mf[kk] = *(const bf16x8*)(mp + kk * 512);
        {
            bf16x8 mg[4];
            bf16x4 mk[4];
#pragma unroll
            for (int k = 0; k < 4; ++k) mg[k] = *(const bf16x8*)(mp + (gk0 + (k < gkn ? k : 0)) * 512);
#pragma unroll
            for (int t = 0; t < 4; ++t) {
                const int tl = (t < 3) ? 3 * wave + t : 12 + (wave >> 1);
                const int tg = 14 * half + tl;
                mk[t] = *(const bf16x4*)(mp + (tg >> 1) * 512 + (tg & 1) * 8);
            }
            f32x4 ta[4];
            f32x4 sx, sm;
            if (wave & 1) {
                mfma_Aa0(ta[3], wsh[0], mf[7]);
#pragma unroll
                for (int k = 1; k < 7; ++k) mfma_Aa(ta[3], wsh[k], mf[7 + k]);
            } else {
                mfma_Aa0(ta[3], wsh[0], mf[0]);
#pragma unroll
                for (int k = 1; k < 7; ++k) mfma_Aa(ta[3], wsh[k], mf[k]);
            }
            mfma_Aa0(ta[0], wa[0][0], mf[0]);
#pragma unroll
            for (int kk = 1; kk < KK; ++kk) mfma_Aa(ta[0], wa[0][kk], mf[kk]);
            mfma_Aa0(ta[1], wa[1][0], mf[0]);
#pragma unroll
            for (int kk = 1; kk < 4; ++kk) mfma_Aa(ta[1], wa[1][kk], mf[kk]);
            pin(ta[3]); pin(ta[0]);
            mfma16_v0(sm, mk[3], pack4(ta[3]));
            mfma16_v(sm, mk[0], pack4(ta[0]));
#pragma unroll
            for (int kk = 4; kk < KK; ++kk) mfma_Aa(ta[1], wa[1][kk], mf[kk]);
            mfma_Aa0(ta[2], wa[2][0], mf[0]);
#pragma unroll
            for (int kk = 1; kk < 4; ++kk) mfma_Aa(ta[2], wa[2][kk], mf[kk]);
            pin(ta[1]);
            mfma16_v(sm, mk[1], pack4(ta[1]));
#pragma unroll
            for (int kk = 4; kk < KK; ++kk) mfma_Aa(ta[2], wa[2][kk], mf[kk]);
            mfma_v0(sx, gf[0], mg[0]);
            mfma_v(sx, gf[1], mg[1]);
            mfma_v(sx, gf[2], mg[2]);
            if (wave < 2) mfma_v(sx, gf[3], mg[3]);
            pin(ta[2]);
            mfma16_v(sm, mk[2], pack4(ta[2]));
            mfma_fence4(sm, sx, ta[0], ta[1]);
            *(f32x4*)(lds + CH_LDS_RA + (wave * 2 + 0) * 1024 + lane * 16) = sx;
            *(f32x4*)(lds + CH_LDS_RA + (wave * 2 + 1) * 1024 + lane * 16) = sm;
        }
        __syncthreads();
        const size_t e2w = OFF_SMX + (size_t)s * E2S + (size_t)c * E2B;
        if (wave < 2) {
            f32x4 v0 = *(const f32x4*)(lds + CH_LDS_RA + (0 + wave) * 1024 + lane * 16);
            f32x4 v1 = *(const f32x4*)(lds + CH_LDS_RA + (2 + wave) * 1024 + lane * 16);
            f32x4 v2 = *(const f32x4*)(lds + CH_LDS_RA + (4 + wave) * 1024 + lane * 16);
            f32x4 v3 = *(const f32x4*)(lds + CH_LDS_RA + (6 + wave) * 1024 + lane * 16);
            f32x4 v = (v0 + v1) + (v2 + v3);
            if (wave == 0) {
                float m = fmaxf(fmaxf(v[0], v[1]), fmaxf(v[2], v[3]));
                m = xmax16(m); m = xmax32(m);
                const float mc = m * cs;
                float e0 = fexp2(v[0] * cs - mc), e1 = fexp2(v[1] * cs - mc), e2 = fexp2(v[2] * cs - mc), e3 = fexp2(v[3] * cs - mc);
                u32x2 pk = {pack2(e0, e1), pack2(e2, e3)};
                if (i < 8) st2c(rs, e2w + i * 32 + g * 8, pk);
                float sum = (bf_lo(pk.x) + bf_hi(pk.x)) + (bf_lo(pk.y) + bf_hi(pk.y));
                sum = xadd16(sum); sum = xadd32(sum);
                u32x2 st = {__float_as_uint(m), __float_as_uint(sum)};
                if (lane < 8) st2c(rs, e2w + 512 + i * 8, st);
            } else {
                if (i < 8 && g < 2) st4c(rs, e2w + 256 + i * 32 + g * 16, __builtin_bit_cast(u32x4, v));
            }
        }
        {
            const size_t gp = OFF_GTF + (((size_t)h * NRT + 2 * (s + 1) + half) * KK + gk0) * KB + lane * 16;
#pragma unroll
            for (int k = 0; k < 4; ++k) gf[k] = ld8c(rs, gp + (size_t)(k < gkn ? k : 0) * KB);
        }
        if constexpr (decltype(FIRST)::value) {
            wait_cnt(ws, CNT_BW + (tB >> 2), 64);
            const size_t bp = OFF_WF + (size_t)(448 + tB) * KK * KB + lane * 16;
#pragma unroll
            for (int hh = 0; hh < 2; ++hh)
#pragma unroll
                for (int kk = 0; kk < KK; ++kk)
                    *(bf16x8*)(lds + CH_LDS_B + (size_t)((wave * 2 + hh) * KK + kk) * KB + lane * 16) = ld8c(rs, bp + (size_t)((4 * wave + 2 + hh) * 28 * KK + kk) * KB);
#pragma unroll
            for (int hh = 0; hh < 2; ++hh)
#pragma unroll
                for (int kk = 0; kk < KK; ++kk) wb[hh][kk] = ld8c(rs, bp + (size_t)((4 * wave + hh) * 28 * KK + kk) * KB);
        }
        if (hasB) {
            f32x4 ya[4];
            const char* lb = lds + CH_LDS_B + (size_t)(wave * 2) * KK * KB + lane * 16;
            mfma_Ba0(ya[0], mf[0], wb[0][0]);
            mfma_v0(ya[1], mf[0], wb[1][0]);
#pragma unroll
            for (int k = 1; k < KK; ++k) {
                mfma_Ba(ya[0], mf[k], wb[0][k]);
                mfma_v(ya[1], mf[k], wb[1][k]);
            }
            u32x4 qx[4], qm0, qm1, qs;
            const size_t e2r = OFF_SMX + (size_t)s * E2S;
            const size_t ox = e2r + (size_t)(8 * wave) * E2B + (i & 7) * 32 + g * 8;
            const size_t om = e2r + (size_t)(8 * wave + 2 * hh2) * E2B + 256 + r2 * 32 + q2 * 16;
            const size_t os = e2r + (size_t)(8 * wave + 2 * hh2) * E2B + 512 + r2 * 8;
#define E2_LOADS() do { _Pragma("unroll") for (int hh = 0; hh < 4; ++hh) { \
                        u32x2 lo_ = ld2c(rs, ox + (size_t)(2 * hh) * E2B), hi_ = ld2c(rs, ox + (size_t)(2 * hh + 1) * E2B); \
                        qx[hh] = (u32x4){lo_.x, lo_.y, hi_.x, hi_.y}; } \
                    qm0 = ld4c(rs, om); qm1 = ld4c(rs, om + E2B); \
                    { u32x2 lo_ = ld2c(rs, os), hi_ = ld2c(rs, os + E2B); qs = (u32x4){lo_.x, lo_.y, hi_.x, hi_.y}; } } while (0)
            {
                bf16x8 lq[2][4];
#pragma unroll
                for (int k = 0; k < 2; ++k) { lq[0][2 * k] = *(const bf16x8*)(lb + (size_t)k * KB); lq[0][2 * k + 1] = *(const bf16x8*)(lb + (size_t)(KK + k) * KB); }
#pragma unroll
                for (int gq = 0; gq < 7; ++gq) {
                    if (gq < 6) {
#pragma unroll
                        for (int k = 0; k < 2; ++k) {
                            lq[(gq + 1) & 1][2 * k] = *(const bf16x8*)(lb + (size_t)(2 * gq + 2 + k) * KB);
                            lq[(gq + 1) & 1][2 * k + 1] = *(const bf16x8*)(lb + (size_t)(KK + 2 * gq + 2 + k) * KB);
                        }
                    }
                    if (gq == E2POS) { for (int d = 0; d < d2v; ++d) __builtin_amdgcn_s_sleep(1); asm volatile("" ::: "memory"); E2_LOADS(); }
                    __builtin_amdgcn_sched_barrier(0);
                    if (gq == 0) { mfma_v0(ya[2], mf[0], lq[0][0]); mfma_v0(ya[3], mf[0], lq[0][1]); }
                    else { mfma_v(ya[2], mf[2 * gq], lq[gq & 1][0]); mfma_v(ya[3], mf[2 * gq], lq[gq & 1][1]); }
                    mfma_v(ya[2], mf[2 * gq + 1], lq[gq & 1][2]); mfma_v(ya[3], mf[2 * gq + 1], lq[gq & 1][3]);
                }
            }
            mfma_fence4(ya[0], ya[1], ya[2], ya[3]);
            bf16x8 yp0 = pack8(ya[0], ya[1]), yp1 = pack8(ya[2], ya[3]);
            __builtin_amdgcn_sched_barrier(0);
            {
                unsigned spins = 0;
                if (E2POS >= 7) { for (int d = 0; d < d2v; ++d) __builtin_amdgcn_s_sleep(1); asm volatile("" ::: "memory"); E2_LOADS(); }
                for (;;) {
                    unsigned mx = max(max(max(umax4(qx[0]), umax4(qx[1])), max(umax4(qx[2]), umax4(qx[3]))), max(max(umax4(qm0), umax4(qm1)), umax4(qs)));
                    if (__all(mx != SENT)) break;
                    if (spin_fail(spins, tmo, 0x200u + s)) break;
                    asm volatile("" ::: "memory");
                    E2_LOADS();
                }
                if (spins != 0u && s > 0 && d2v < D2 + ADMAX) d2v += 2;
            }
            f32x4 ms = __builtin_bit_cast(f32x4, qm0) + __builtin_bit_cast(f32x4, qm1);
            const float m0 = __uint_as_float(qs.x), s0 = __uint_as_float(qs.y), m1 = __uint_as_float(qs.z), s1 = __uint_as_float(qs.w);
            float mx = fmaxf(fmaxf(ms[0], ms[1]), fmaxf(ms[2], ms[3]));
            mx = fmaxf(mx, qswap(mx));
            const float Mc = fmaxf(mx, fmaxf(m0, m1)) * cs;
            float e0 = fexp2(ms[0] * cs - Mc), e1 = fexp2(ms[1] * cs - Mc), e2 = fexp2(ms[2] * cs - Mc), e3 = fexp2(ms[3] * cs - Mc);
            float f0 = fexp2(m0 * cs - Mc), f1 = fexp2(m1 * cs - Mc);
            float sm = (e0 + e1) + (e2 + e3);
            sm += qswap(sm);
            const float inv = __builtin_amdgcn_rcpf(sm + f0 * s0 + f1 * s1);
            char* pw = lds + CH_LDS_PW + wave * 1024;
            *(u32x2*)(pw + ((hh2 * 8 + r2) * 2 + q2) * 8) = (u32x2){pack2(e0 * inv, e1 * inv), pack2(e2 * inv, e3 * inv)};
            if (q2 == 0) *(u32x2*)(pw + 512 + (r2 * 4 + hh2) * 8) = (u32x2){__float_as_uint(f0 * inv), __float_as_uint(f1 * inv)};
            __builtin_amdgcn_fence(__ATOMIC_RELEASE, "wavefront");
            __builtin_amdgcn_wave_barrier();
            __builtin_amdgcn_fence(__ATOMIC_ACQUIRE, "wavefront");
            __builtin_amdgcn_sched_barrier(0);
            f32x4 dd[8];
#pragma unroll
            for (int hh = 0; hh < 4; ++hh) {
                const u32x4 u = __builtin_bit_cast(u32x4, uf[hh]);
                u32x2 ulo = {u.x, u.y}, uhi = {u.z, u.w}, elo = {qx[hh].x, qx[hh].y}, ehi = {qx[hh].z, qx[hh].w};
                mfma16_v0(dd[2 * hh], __builtin_bit_cast(bf16x4, ulo), __builtin_bit_cast(bf16x4, elo));
                mfma16_v0(dd[2 * hh + 1], __builtin_bit_cast(bf16x4, uhi), __builtin_bit_cast(bf16x4, ehi));
            }
            u32x2 pa[4];
#pragma unroll
            for (int hh = 0; hh < 4; ++hh) pa[hh] = *(const u32x2*)(pw + ((hh * 8 + (i & 7)) * 2 + (g & 1)) * 8);
            f32x4 fa = *(const f32x4*)(pw + 512 + (i & 7) * 32), fb = *(const f32x4*)(pw + 512 + (i & 7) * 32 + 16);
            if (g >= 2) {
#pragma unroll
                for (int hh = 0; hh < 4; ++hh) pa[hh] = (u32x2){0u, 0u};
            }
            mfma_fence4(dd[0], dd[1], dd[2], dd[3]);
            mfma_fence4(dd[4], dd[5], dd[6], dd[7]);
            f32x4 acc = dd[0] * fa[0] + dd[1] * fa[1] + dd[2] * fa[2] + dd[3] * fa[3] + dd[4] * fb[0] + dd[5] * fb[1] + dd[6] * fb[2] + dd[7] * fb[3];
            u32x4 p01 = {pa[0].x, pa[0].y, pa[1].x, pa[1].y}, p23 = {pa[2].x, pa[2].y, pa[3].x, pa[3].y};
            mfma_v(acc, yp0, __builtin_bit_cast(bf16x8, p01));
            mfma_v(acc, yp1, __builtin_bit_cast(bf16x8, p23));
            mfma_fence1(acc);
            *(f32x4*)(lds + CH_LDS_RB + wave * 1024 + lane * 16) = acc;
        }
        __syncthreads();
        if (hasB && wave == 0) {
            f32x4 v0 = *(const f32x4*)(lds + CH_LDS_RB + lane * 16);
            f32x4 v1 = *(const f32x4*)(lds + CH_LDS_RB + 1024 + lane * 16);
            f32x4 v2 = *(const f32x4*)(lds + CH_LDS_RB + 2048 + lane * 16);
            f32x4 v3 = *(const f32x4*)(lds + CH_LDS_RB + 3072 + lane * 16);
            f32x4 v = (v0 + v1) + (v2 + v3);
            if (i < 8) {
                u32x2 pk = {pack2(v[0], v[1]), pack2(v[2], v[3])};
                st2c(rs, OFF_MEMF + (size_t)((s + 1) * 7168 + tB * 256 + i * 32 + g * 8), pk);
            }
        }
    };
    step(0, BoolC<true>{});
    for (int s = 1; s < NSEG - 1; ++s) step(s, BoolC<false>{});
    if (c == 31) {
        unsigned spins = 0;
        const size_t o0 = OFF_MEMF + (size_t)((NSEG - 1) * 7168 + tid * 16), o1 = o0 + (tid < 192 ? 4096 : 0);
        for (;;) {
            asm volatile("" ::: "memory");
            u32x4 x0 = ld4c(rs, o0), x1 = ld4c(rs, o1);
            if (__all(max(umax4(x0), umax4(x1)) != SENT)) break;
            if (spin_fail(spins, tmo, 0x1FFu)) break;
        }
        __syncthreads();
        if (tid == 255) __hip_atomic_store((unsigned*)(ws + OFF_CNT + (size_t)CNT_PROG * 128), (unsigned)(NSEG - 1), __ATOMIC_RELAXED, __HIP_MEMORY_SCOPE_AGENT);
    }
}

constexpr int WK_LDS_SH = 0, WK_LDS_OUT = 32768, WK_LDS_CH = WK_LDS_OUT + 56 * 1024;
constexpr int WK_LDS_SIZE = WK_LDS_CH + 128 * 528;
template <int TYPE>
DI void ab_prologue(const unsigned short* __restrict__ Wq, const unsigned short* __restrict__ Wk, const unsigned short* __restrict__ Wv,
                    const unsigned short* __restrict__ Wo, char* __restrict__ ws, char* lds, const rsrc_t rs, int tile0, int wave, int lane) {
    const int i = lane & 15, g = lane >> 4, tid = threadIdx.x;
    const int h = (tile0 - 448 * TYPE) / 28, ch = ((tile0 - 448 * TYPE) % 28) >> 2;
    const unsigned short* ms = TYPE == 0 ? Wk : Wv;
    u32x4 raw[56];
#pragma unroll
    for (int n = 0; n < 56; ++n) raw[n] = *(const u32x4*)(ms + (size_t)(8 * n + (tid >> 5)) * 4096 + h * 256 + (tid & 31) * 8);
    u32x4 sh[8];
    if (TYPE == 0) {
        const unsigned short* p = Wq + (size_t)(64 * ch + 16 * wave + i) * 4096 + h * 256 + 8 * g;
#pragma unroll
        for (int ke = 0; ke < 8; ++ke) sh[ke] = *(const u32x4*)(p + ke * 32);
    } else {
#pragma unroll
        for (int n = 0; n < 8; ++n) {
            const int e = tid + n * 256, row = e >> 3, c8 = e & 7;
            sh[n] = *(const u32x4*)(Wo + ((size_t)h * 256 + row) * DM + 64 * ch + c8 * 8);
        }
    }
#pragma unroll
    for (int n = 0; n < 8; ++n) asm volatile("" : "+v"(sh[n]));
#pragma unroll
    for (int n = 0; n < 56; ++n) asm volatile("" : "+v"(raw[n]));
    if (TYPE == 0) {
#pragma unroll
        for (int ke = 0; ke < 8; ++ke) *(u32x4*)(lds + WK_LDS_SH + (wave * 8 + ke) * 1024 + lane * 16) = sh[ke];
    } else {
#pragma unroll
        for (int n = 0; n < 8; ++n) *(u32x4*)(lds + WK_LDS_CH + (tid + n * 256) * 16) = sh[n];
        __syncthreads();
        const unsigned short* wo_t = (const unsigned short*)(lds + WK_LDS_CH);
#pragma unroll
        for (int ke = 0; ke < 8; ++ke) {
            bf16x8 v;
#pragma unroll
            for (int j = 0; j < 8; ++j) v[j] = (short)wo_t[(ke * 32 + 8 * g + j) * 64 + 16 * wave + i];
            *(bf16x8*)(lds + WK_LDS_SH + (wave * 8 + ke) * 1024 + lane * 16) = v;
        }
    }
#pragma unroll
    for (int cI = 0; cI < 4; ++cI) {
        __syncthreads();
#pragma unroll
        for (int n = 0; n < 16; ++n)
            if (16 * cI + n < 56) *(u32x4*)(lds + WK_LDS_CH + (8 * n + (tid >> 5)) * 528 + (tid & 31) * 16) = raw[16 * cI + n];
        __syncthreads();
        const int pr = 4 * cI + wave;
        if (pr < KK) {
            bf16x8 cur[2][8];
#pragma unroll
            for (int t = 0; t < 2; ++t)
#pragma unroll
                for (int ke = 0; ke < 8; ++ke) cur[t][ke] = *(const bf16x8*)(lds + WK_LDS_CH + (32 * wave + 16 * t + i) * 528 + ke * 64 + g * 16);
            if (TYPE == 0) {
#pragma unroll
                for (int ap = 0; ap < 2; ++ap) {
                    bf16x8 qf[2][8];
#pragma unroll
                    for (int a = 0; a < 2; ++a)
#pragma unroll
                        for (int ke = 0; ke < 8; ++ke) qf[a][ke] = *(const bf16x8*)(lds + WK_LDS_SH + ((2 * ap + a) * 8 + ke) * 1024 + lane * 16);
#pragma unroll
                    for (int bb = 0; bb < 2; ++bb) {
                        f32x4 d0 = {0.f, 0.f, 0.f, 0.f}, d1 = {0.f, 0.f, 0.f, 0.f};
#pragma unroll
                        for (int ke = 0; ke < 8; ++ke) { d0 = mfma32(qf[0][ke], cur[bb][ke], d0); d1 = mfma32(qf[1][ke], cur[bb][ke], d1); }
                        st8c(rs, OFF_ANF + ((size_t)(h * 28 + 2 * pr + bb) * KK + 2 * ch + ap) * KB + lane * 16, pack8(d0, d1));
                    }
#pragma unroll
                    for (int a = 0; a < 2; ++a) {
                        f32x4 d0 = {0.f, 0.f, 0.f, 0.f}, d1 = {0.f, 0.f, 0.f, 0.f};
#pragma unroll
                        for (int ke = 0; ke < 8; ++ke) { d0 = mfma32(cur[0][ke], qf[a][ke], d0); d1 = mfma32(cur[1][ke], qf[a][ke], d1); }
                        const bf16x8 v = pack8(d0, d1);
                        *(bf16x8*)(lds + WK_LDS_OUT + ((2 * ap + a) * KK + pr) * 1024 + lane * 16) = v;
                        st8c(rs, OFF_WF + ((size_t)(tile0 + 2 * ap + a) * KK + pr) * KB + lane * 16, v);
                    }
                }
            } else {
#pragma unroll
                for (int ct = 0; ct < 4; ++ct) {
                    f32x4 d0 = {0.f, 0.f, 0.f, 0.f}, d1 = {0.f, 0.f, 0.f, 0.f};
#pragma unroll
                    for (int ke = 0; ke < 8; ++ke) {
                        const bf16x8 fx = *(const bf16x8*)(lds + WK_LDS_SH + (ct * 8 + ke) * 1024 + lane * 16);
                        d0 = mfma32(cur[0][ke], fx, d0); d1 = mfma32(cur[1][ke], fx, d1);
                    }
                    const bf16x8 v = pack8(d0, d1);
                    *(bf16x8*)(lds + WK_LDS_OUT + (ct * KK + pr) * 1024 + lane * 16) = v;
                    st8c(rs, OFF_WF + ((size_t)(tile0 + ct) * KK + pr) * KB + lane * 16, v);
                }
            }
        }
    }
    signal_cnt(ws, TYPE == 0 ? CNT_AN + h : CNT_BW + ch, lane);
    __syncthreads();
}

template <int TYPE>
DI void w1_phase(char* __restrict__ ws, char* lds, const rsrc_t rs, int tile0, int wave, int lane) {
    bf16x8 w[4][KK];
#pragma unroll
    for (int t = 0; t < 4; ++t)
#pragma unroll
        for (int kk = 0; kk < KK; ++kk) w[t][kk] = *(const bf16x8*)(lds + WK_LDS_OUT + (t * KK + kk) * 1024 + lane * 16);
    const int h = (tile0 - 448 * TYPE) / 28, t0 = (tile0 - 448 * TYPE) % 28;
    bf16x8 xa[KK], xb[KK];
    {
        const char* xp = ws + OFF_XNF + (size_t)(2 * wave) * KK * KB + lane * 16;
#pragma unroll
        for (int kk = 0; kk < KK; ++kk) xa[kk] = ldfrag(xp + (size_t)kk * KB);
    }
    for (int s = wave; s < NSEG; s += 4) {
        f32x4 acc[2][4];
        {
            const char* xp = ws + OFF_XNF + (size_t)(2 * s + 1) * KK * KB + lane * 16;
#pragma unroll
            for (int kk = 0; kk < KK; ++kk) xb[kk] = ldfrag(xp + (size_t)kk * KB);
        }
        __builtin_amdgcn_sched_barrier(0);
#pragma unroll
        for (int t = 0; t < 4; ++t) { if (TYPE == 0) mfma_Aa0(acc[0][t], w[t][0], xa[0]); else mfma_Ba0(acc[0][t], xa[0], w[t][0]); }
#pragma unroll
        for (int kk = 1; kk < KK; ++kk)
#pragma unroll
            for (int t = 0; t < 4; ++t) { if (TYPE == 0) mfma_Aa(acc[0][t], w[t][kk], xa[kk]); else mfma_Ba(acc[0][t], xa[kk], w[t][kk]); }
        __builtin_amdgcn_sched_barrier(0);
        {
            const int sn = (s + 4 < NSEG) ? s + 4 : s;
            const char* xp = ws + OFF_XNF + (size_t)(2 * sn) * KK * KB + lane * 16;
#pragma unroll
            for (int kk = 0; kk < KK; ++kk) xa[kk] = ldfrag(xp + (size_t)kk * KB);
        }
        __builtin_amdgcn_sched_barrier(0);
#pragma unroll
        for (int t = 0; t < 4; ++t) { if (TYPE == 0) mfma_Aa0(acc[1][t], w[t][0], xb[0]); else mfma_Ba0(acc[1][t], xb[0], w[t][0]); }
#pragma unroll
        for (int kk = 1; kk < KK; ++kk)
#pragma unroll
            for (int t = 0; t < 4; ++t) { if (TYPE == 0) mfma_Aa(acc[1][t], w[t][kk], xb[kk]); else mfma_Ba(acc[1][t], xb[kk], w[t][kk]); }
        mfma_fence4(acc[0][0], acc[0][1], acc[0][2], acc[0][3]);
        mfma_fence4(acc[1][0], acc[1][1], acc[1][2], acc[1][3]);
        if (TYPE == 0) {
#pragma unroll
            for (int q = 0; q < 2; ++q)
#pragma unroll
                for (int p = 0; p < 2; ++p)
                    st8c(rs, OFF_GTF + (((size_t)h * NRT + 2 * s + q) * KK + (t0 >> 1) + p) * KB + lane * 16, pack8(acc[q][2 * p], acc[q][2 * p + 1]));
        } else {
#pragma unroll
            for (int t = 0; t < 4; ++t)
                st8c(rs, OFF_UF + (((size_t)s * NT16 + t0 + t) * NH + h) * KB + lane * 16, pack8(acc[0][t], acc[1][t]));
        }
        signal_cnt(ws, CNT_G + s, lane);
    }
}

DI void ffn_task(char* __restrict__ ws, char* lds, const rsrc_t rs, const unsigned short* __restrict__ x, const unsigned short* __restrict__ b1,
                 const unsigned short* __restrict__ b2, int s, int wave, int lane) {
    const int i = lane & 15, g = lane >> 4;
    {
        bf16x8 xf[2][KK];
        const char* xp = ws + OFF_XNF + (size_t)(2 * s) * KK * KB + lane * 16;
#pragma unroll
        for (int q = 0; q < 2; ++q)
#pragma unroll
            for (int kk = 0; kk < KK; ++kk) xf[q][kk] = ldfrag(xp + (size_t)(q * KK + kk) * KB);
        const char* wp = ws + OFF_WF + (size_t)(896 + 28 * wave) * KK * KB + lane * 16;
        for (int pr = 0; pr < 14; ++pr) {
            bf16x8 wf[2][KK];
#pragma unroll
            for (int t = 0; t < 2; ++t)
#pragma unroll
                for (int kk = 0; kk < KK; ++kk) wf[t][kk] = ldfrag(wp + (size_t)((2 * pr + t) * KK + kk) * KB);
            f32x4 acc[2][2];
#pragma unroll
            for (int q = 0; q < 2; ++q)
#pragma unroll
                for (int t = 0; t < 2; ++t) acc[q][t] = (f32x4){0.f, 0.f, 0.f, 0.f};
#pragma unroll
            for (int kk = 0; kk < KK; ++kk)
#pragma unroll
                for (int q = 0; q < 2; ++q)
#pragma unroll
                    for (int t = 0; t < 2; ++t) acc[q][t] = mfma32(wf[t][kk], xf[q][kk], acc[q][t]);
#pragma unroll
            for (int t = 0; t < 2; ++t) {
                u32x2 bb = *(const u32x2*)(b1 + 16 * (28 * wave + 2 * pr + t) + 4 * g);
                float bv[4] = {bf_lo(bb.x), bf_hi(bb.x), bf_lo(bb.y), bf_hi(bb.y)};
#pragma unroll
                for (int q = 0; q < 2; ++q)
#pragma unroll
                    for (int r = 0; r < 4; ++r) acc[q][t][r] = gelu_tanh(acc[q][t][r] + bv[r]);
            }
#pragma unroll
            for (int q = 0; q < 2; ++q)
                *(bf16x8*)(lds + (size_t)(q * KF + 14 * wave + pr) * KB + lane * 16) = pack8(acc[q][0], acc[q][1]);
        }
    }
    __syncthreads();
    {
        f32x4 acc[2][7];
#pragma unroll
        for (int q = 0; q < 2; ++q)
#pragma unroll
            for (int t = 0; t < 7; ++t) acc[q][t] = (f32x4){0.f, 0.f, 0.f, 0.f};
        const char* wp = ws + OFF_W2F + (size_t)(7 * wave) * KF * KB + lane * 16;
#pragma unroll 4
        for (int kf = 0; kf < KF; ++kf) {
            bf16x8 h0 = *(const bf16x8*)(lds + (size_t)kf * KB + lane * 16), h1 = *(const bf16x8*)(lds + (size_t)(KF + kf) * KB + lane * 16);
#pragma unroll
            for (int t = 0; t < 7; ++t) {
                bf16x8 wf = ldfrag(wp + (size_t)(t * KF + kf) * KB);
                acc[0][t] = mfma32(wf, h0, acc[0][t]);
                acc[1][t] = mfma32(wf, h1, acc[1][t]);
            }
        }
#pragma unroll
        for (int t = 0; t < 7; ++t) {
            const int tc = 7 * wave + t;
            u32x2 bb = *(const u32x2*)(b2 + 16 * tc + 4 * g);
#pragma unroll
            for (int q = 0; q < 2; ++q) {
                const size_t o = (size_t)(32 * s + 16 * q + i) * DM + 16 * tc + 4 * g;
                u32x2 xv = *(const u32x2*)(x + o);
                f32x4 r = {acc[q][t][0] + bf_lo(bb.x) + bf_lo(xv.x), acc[q][t][1] + bf_hi(bb.x) + bf_hi(xv.x), acc[q][t][2] + bf_lo(bb.y) + bf_lo(xv.y), acc[q][t][3] + bf_hi(bb.y) + bf_hi(xv.y)};
                st4c(rs, OFF_FO + o * 4, __builtin_bit_cast(u32x4, r));
            }
        }
        signal_cnt(ws, CNT_F + s, lane);
    }
    __syncthreads();
}

DI void w3_loop(char* __restrict__ ws, const rsrc_t rs, int tile0, int lane) {
    const int i = lane & 15, g = lane >> 4;
    bf16x8 w[4][KK];
    {
        const size_t wp = OFF_WF + (size_t)tile0 * KK * KB + lane * 16;
#pragma unroll
        for (int t = 0; t < 4; ++t)
#pragma unroll
            for (int kk = 0; kk < KK; ++kk) w[t][kk] = ld8c(rs, wp + (size_t)(t * KK + kk) * KB);
    }
    const bool isA = tile0 < 448;
    const int h = (isA ? tile0 : tile0 - 448) / 28, t0 = (isA ? tile0 : tile0 - 448) % 28;
    for (int sp = 0; sp < NSEG / 2; ++sp) {
        bf16x8 xf[KK];
        wait_cnt_slow(ws, CNT_PROG, (unsigned)(sp == NSEG / 2 - 1 ? 2 * sp : 2 * sp + 1));
        {
            const size_t mp = OFF_MEMF + (size_t)(2 * sp + (i >> 3)) * 7168 + (i & 7) * 32 + g * 8;
            unsigned spins = 0;
            for (;;) {
                asm volatile("" ::: "memory");
                unsigned mx = 0;
#pragma unroll
                for (int kk = 0; kk < KK; ++kk) {
                    u32x2 lo_ = ld2c(rs, mp + kk * 512), hi_ = ld2c(rs, mp + kk * 512 + 256);
                    u32x4 xx_ = {lo_.x, lo_.y, hi_.x, hi_.y};
                    mx = max(mx, umax4(xx_));
                    xf[kk] = __builtin_bit_cast(bf16x8, xx_);
                }
                if (__all(mx != SENT)) break;
                __builtin_amdgcn_s_sleep(16);
                if (spin_fail(spins, (unsigned*)(ws + OFF_TMO), 0x500u + sp)) break;
            }
        }
        f32x4 acc[4];
        if (isA) {
#pragma unroll
            for (int t = 0; t < 4; ++t) mfma_Aa0(acc[t], w[t][0], xf[0]);
#pragma unroll
            for (int kk = 1; kk < KK; ++kk)
#pragma unroll
                for (int t = 0; t < 4; ++t) mfma_Aa(acc[t], w[t][kk], xf[kk]);
            mfma_fence4(acc[0], acc[1], acc[2], acc[3]);
#pragma unroll
            for (int p = 0; p < 2; ++p)
                st8c(rs, OFF_T2F + ((size_t)h * NSEG + 2 * sp + (i >> 3)) * 7168 + ((t0 >> 1) + p) * 512 + g * 128 + (i & 7) * 16, pack8(acc[2 * p], acc[2 * p + 1]));
        } else {
#pragma unroll
            for (int t = 0; t < 4; ++t) mfma_Ba0(acc[t], xf[0], w[t][0]);
#pragma unroll
            for (int kk = 1; kk < KK; ++kk)
#pragma unroll
                for (int t = 0; t < 4; ++t) mfma_Ba(acc[t], xf[kk], w[t][kk]);
            mfma_fence4(acc[0], acc[1], acc[2], acc[3]);
#pragma unroll
            for (int t = 0; t < 4; ++t)
                st2c(rs, OFF_YF + (((size_t)(2 * sp + (g >> 1)) * NT16 + t0 + t) * 8 + (h >> 1)) * 512 + (g & 1) * 256 + i * 16 + (h & 1) * 8, __builtin_bit_cast(u32x2, pack4(acc[t])));
        }
        signal_cnt(ws, isA ? CNT_T2 + sp * 16 + h : CNT_Y + sp * 7 + (t0 >> 2), lane);
    }
}

DI void f1_task(char* __restrict__ ws, const rsrc_t rs, int s, int h, int lane) {
    const int i = lane & 15, g = lane >> 4;
    f32x4 sc[2][3];
#pragma unroll
    for (int q = 0; q < 2; ++q)
#pragma unroll
        for (int k = 0; k < 3; ++k) sc[q][k] = (f32x4){0.f, 0.f, 0.f, 0.f};
    const char* xp = ws + OFF_XNF + (size_t)(2 * s) * KK * KB + lane * 16;
    const size_t gp = OFF_GTF + ((size_t)h * NRT + 2 * s) * KK * KB + lane * 16;
    const size_t tp = OFF_T2F + ((size_t)h * NSEG + s) * 7168 + g * 128 + (i & 7) * 16;
    bf16x8 xf[2][KK];
    {
        wait_cnt(ws, CNT_G + s, 224);
        bf16x8 g0[KK], g1[KK];
#pragma unroll
        for (int kk = 0; kk < KK; ++kk) { g0[kk] = ld8c(rs, gp + (size_t)kk * KB); g1[kk] = ld8c(rs, gp + (size_t)(KK + kk) * KB); }
#pragma unroll
        for (int q = 0; q < 2; ++q)
#pragma unroll
            for (int kk = 0; kk < KK; ++kk) xf[q][kk] = ldfrag(xp + (size_t)(q * KK + kk) * KB);
#pragma unroll
        for (int kk = 0; kk < KK; ++kk)
#pragma unroll
            for (int q = 0; q < 2; ++q) {
                sc[q][0] = mfma32(g0[kk], xf[q][kk], sc[q][0]);
                sc[q][1] = mfma32(g1[kk], xf[q][kk], sc[q][1]);
            }
    }
    wait_cnt_slow(ws, CNT_PROG, (unsigned)(s | 1));
    wait_cnt(ws, CNT_T2 + (s >> 1) * 16 + h, 7);
    {
        bf16x8 t2[KK];
#pragma unroll
        for (int kk = 0; kk < KK; ++kk) t2[kk] = ld8c(rs, tp + kk * 512);
#pragma unroll
        for (int kk = 0; kk < KK; ++kk)
#pragma unroll
            for (int q = 0; q < 2; ++q) sc[q][2] = mfma32(t2[kk], xf[q][kk], sc[q][2]);
    }
    const float cs = 0.0625f * 1.4426950408889634f;
    const float NEG = -3.0e38f;
#pragma unroll
    for (int q = 0; q < 2; ++q) {
        const int qi = 16 * q + i;
        float xs[8], ms[4];
#pragma unroll
        for (int j = 0; j < 8; ++j) {
            const int key = 16 * (j >> 2) + 4 * g + (j & 3);
            xs[j] = (key <= qi) ? sc[q][j >> 2][j & 3] : NEG;
        }
#pragma unroll
        for (int j = 0; j < 4; ++j) ms[j] = (g < 2) ? sc[q][2][j] : NEG;
        float m = fmaxf(fmaxf(fmaxf(xs[0], xs[1]), fmaxf(xs[2], xs[3])), fmaxf(fmaxf(xs[4], xs[5]), fmaxf(xs[6], xs[7])));
        m = fmaxf(m, fmaxf(fmaxf(ms[0], ms[1]), fmaxf(ms[2], ms[3])));
        m = xmax16(m); m = xmax32(m);
        const float mc = m * cs;
        float sum = 0.f;
#pragma unroll
        for (int j = 0; j < 8; ++j) { const int key = 16 * (j >> 2) + 4 * g + (j & 3); xs[j] = (key <= qi) ? fexp2(xs[j] * cs - mc) : 0.f; sum += xs[j]; }
#pragma unroll
        for (int j = 0; j < 4; ++j) { ms[j] = (g < 2) ? fexp2(ms[j] * cs - mc) : 0.f; sum += ms[j]; }
        sum = xadd16(sum); sum = xadd32(sum);
        const float inv = 1.f / sum;
        u32x4 px = {pack2(xs[0] * inv, xs[1] * inv), pack2(xs[2] * inv, xs[3] * inv), pack2(xs[4] * inv, xs[5] * inv), pack2(xs[6] * inv, xs[7] * inv)};
        st4c(rs, OFF_PXF + (((size_t)s * 2 + q) * NH + h) * KB + lane * 16, px);
        u32x2 pmv = {pack2(ms[0] * inv, ms[1] * inv), pack2(ms[2] * inv, ms[3] * inv)};
        st2c(rs, OFF_PMF + (((size_t)s * 2 + q) * 8 + (h >> 1)) * KB + lane * 16 + (h & 1) * 8, pmv);
    }
    signal_cnt(ws, CNT_P + s, lane);
}

DI void f2_task(char* __restrict__ ws, const rsrc_t rs, unsigned short* __restrict__ out, int s, int tp2, int lane) {
    const int i = lane & 15, g = lane >> 4;
    wait_cnt(ws, CNT_G + s, 224);
    wait_cnt(ws, CNT_F + s, 4);
    f32x4 acc[2][2];
    bf16x8 ua[2][NH];
    {
        const size_t up = OFF_UF + ((size_t)s * NT16 + 2 * tp2) * NH * KB + lane * 16;
#pragma unroll
        for (int t = 0; t < 2; ++t)
#pragma unroll
            for (int h = 0; h < NH; ++h) ua[t][h] = ld8c(rs, up + (size_t)(t * NH + h) * KB);
    }
#pragma unroll
    for (int q = 0; q < 2; ++q)
#pragma unroll
        for (int t = 0; t < 2; ++t)
            acc[q][t] = __builtin_bit_cast(f32x4, ld4c(rs, OFF_FO + ((size_t)(32 * s + 16 * q + i) * DM + 16 * (2 * tp2 + t) + 4 * g) * 4));
    wait_cnt_slow(ws, CNT_PROG, (unsigned)(s | 1));
    wait_cnt(ws, CNT_Y + (s >> 1) * 7 + (tp2 >> 1), 16);
    wait_cnt(ws, CNT_P + s, 16);
    {
        const size_t pp = OFF_PXF + (size_t)s * 2 * NH * KB + lane * 16;
        bf16x8 p0[NH], p1[NH];
#pragma unroll
        for (int h = 0; h < NH; ++h) { p0[h] = ld8c(rs, pp + (size_t)h * KB); p1[h] = ld8c(rs, pp + (size_t)(NH + h) * KB); }
        const size_t yp = OFF_YF + ((size_t)s * NT16 + 2 * tp2) * 8 * 512 + (g & 1) * 256 + i * 16;
        const size_t pq = OFF_PMF + (size_t)s * 2 * 8 * KB + lane * 16;
        bf16x8 ya[2][8], m0[8], m1[8];
#pragma unroll
        for (int hp = 0; hp < 8; ++hp) {
            m0[hp] = ld8c(rs, pq + (size_t)hp * KB); m1[hp] = ld8c(rs, pq + (size_t)(8 + hp) * KB);
            ya[0][hp] = ld8c(rs, yp + (size_t)hp * 512); ya[1][hp] = ld8c(rs, yp + (size_t)(8 + hp) * 512);
        }
#pragma unroll
        for (int h = 0; h < NH; ++h)
#pragma unroll
            for (int t = 0; t < 2; ++t) {
                acc[0][t] = mfma32(ua[t][h], p0[h], acc[0][t]);
                acc[1][t] = mfma32(ua[t][h], p1[h], acc[1][t]);
            }
#pragma unroll
        for (int hp = 0; hp < 8; ++hp)
#pragma unroll
            for (int t = 0; t < 2; ++t) {
                acc[0][t] = mfma32(ya[t][hp], m0[hp], acc[0][t]);
                acc[1][t] = mfma32(ya[t][hp], m1[hp], acc[1][t]);
            }
    }
#pragma unroll
    for (int q = 0; q < 2; ++q)
#pragma unroll
        for (int t = 0; t < 2; ++t) {
            const size_t o = (size_t)(32 * s + 16 * q + i) * DM + 16 * (2 * tp2 + t) + 4 * g;
            u32x2 r = {pack2(acc[q][t][0], acc[q][t][1]), pack2(acc[q][t][2], acc[q][t][3])};
            *(u32x2*)(out + o) = r;
        }
}

constexpr int MEGA_LDS = WK_LDS_SIZE > CH_LDS_SIZE ? WK_LDS_SIZE : CH_LDS_SIZE;
__global__ __launch_bounds__(256, 1) void k_mega(const unsigned short* __restrict__ x, const unsigned short* __restrict__ b1, const unsigned short* __restrict__ b2,
                                                 const unsigned short* __restrict__ Wq, const unsigned short* __restrict__ Wk,
                                                 const unsigned short* __restrict__ Wv, const unsigned short* __restrict__ Wo,
                                                 char* __restrict__ ws, unsigned short* __restrict__ out) {
    extern __shared__ __attribute__((aligned(16))) char lds[];
    const rsrc_t rs = __builtin_amdgcn_make_buffer_rsrc(ws, 0, (int)WS_END, 0x00020000);
    if ((blockIdx.x & 7) == 0) { chain_role(ws, lds, rs); return; }
    const int wid = (blockIdx.x >> 3) * 7 + (blockIdx.x & 7) - 1;
    const int wave = __builtin_amdgcn_readfirstlane(threadIdx.x >> 6), lane = threadIdx.x & 63;
    const int un = ((blockIdx.x & 7) - 1) * 32 + (blockIdx.x >> 3);
    const int tile0 = ((un % 14) / 7) * 448 + (un / 14) * 28 + (un % 7) * 4;
    if (tile0 < 448) { ab_prologue<0>(Wq, Wk, Wv, Wo, ws, lds, rs, tile0, wave, lane); w1_phase<0>(ws, lds, rs, tile0, wave, lane); }
    else { ab_prologue<1>(Wq, Wk, Wv, Wo, ws, lds, rs, tile0, wave, lane); w1_phase<1>(ws, lds, rs, tile0, wave, lane); }
    if (wid >= 160) ffn_task(ws, lds, rs, x, b1, b2, wid - 160, wave, lane);
    if (wave == 0) { w3_loop(ws, rs, tile0, lane); return; }
    const int fw = wid * 3 + wave - 1;
    for (int b = 0; b < 32; ++b) {
        {
            const int k = (fw + 672 * 2 - b * 32) % 672;
            if (k < 32) f1_task(ws, rs, 2 * b + (k >> 4), k & 15, lane);
        }
        {
            const int k = (fw + 672 * 2 - 336 - b * 28) % 672;
            if (k < 28) f2_task(ws, rs, out, 2 * b + (k / 14), k % 14, lane);
        }
    }
}

extern "C" void kernel_launch(void* const* d_in, const int* in_sizes, int n_in,
                              void* d_out, int out_size, void* d_ws, size_t ws_size,
                              hipStream_t stream) {
    const unsigned short* x = (const unsigned short*)d_in[0];
    const unsigned short* mem0 = (const unsigned short*)d_in[1];
    const unsigned short* Wq = (const unsigned short*)d_in[2];
    const unsigned short* Wk = (const unsigned short*)d_in[3];
    const unsigned short* Wv = (const unsigned short*)d_in[4];
    const unsigned short* Wo = (const unsigned short*)d_in[5];
    const unsigned short* w1 = (const unsigned short*)d_in[8];
    const unsigned short* b1 = (const unsigned short*)d_in[9];
    const unsigned short* w2 = (const unsigned short*)d_in[10];
    const unsigned short* b2 = (const unsigned short*)d_in[11];
    char* ws = (char*)d_ws;
    unsigned short* out = (unsigned short*)d_out;
    (void)hipFuncSetAttribute((const void*)k_mega, hipFuncAttributeMaxDynamicSharedMemorySize, MEGA_LDS);
    k_prep<<<PREP_GRID, PREP_NT, 0, stream>>>(x, mem0, w1, w2, ws);
    k_mega<<<256, 256, MEGA_LDS, stream>>>(x, b1, b2, Wq, Wk, Wv, Wo, ws, out);
}
```
